```python
import torch

def _softmax(x, axis=-1):
    return torch.softmax(x, dim=axis)
def _gelu(x):
    return torch.nn.functional.gelu(x, approximate="tanh")
def _silu(x):
    return torch.nn.functional.silu(x)
def _sigmoid(x):
    return torch.sigmoid(x)
def _layernorm(x, axis=-1, eps=1e-5):
    m = x.mean(dim=axis, keepdim=True)
    v = x.var(dim=axis, keepdim=True, unbiased=False)
    return (x - m) / torch.sqrt(v + eps)
def _scatter_add(tgt, idx, src, axis=0):
    return tgt.clone().index_add_(axis, idx, src)
def _segment_sum(src, seg, n):
    out = torch.zeros((n,) + src.shape[1:], dtype=src.dtype)
    return out.index_add_(0, seg, src)
def _pad(x, pw):
    flat = tuple(v for pair in reversed(pw) for v in pair)
    return torch.nn.functional.pad(x, flat)

kernel_name = 'interleave_layers__mha_block__dense_activation_mish__prompt'

def setup_inputs():

    _w = (lambda a: (lambda t: t.to(torch.bfloat16) if t.is_floating_point() else t)(torch.as_tensor(a)))
    def _A_setup():
        B, S, D, H = 1, 2048, 512, 32
        return {
            "x": _w((lambda *s: torch.randn(*s, dtype=torch.bfloat16) * 0.05)(B, S, D)),
            "wqkv": _w((lambda *s: torch.randn(*s, dtype=torch.bfloat16) * 0.05)(D, 3 * D)),
            "wo": _w((lambda *s: torch.randn(*s, dtype=torch.bfloat16) * 0.05)(D, D)),
        }
    _A_in = _A_setup()

    def _B_setup():
        B, S, D, F = 1, 2048, 512, 1376
        return {
            "x": _w((lambda *s: torch.randn(*s, dtype=torch.bfloat16) * 0.05)(B, S, D)),
            "w1": _w((lambda *s: torch.randn(*s, dtype=torch.bfloat16) * 1.6)(D, F) * (D ** -0.5)),
            "b1": _w((lambda *s: torch.randn(*s, dtype=torch.bfloat16) * 0.2)(F)),
            "w2": _w((lambda *s: torch.randn(*s, dtype=torch.bfloat16) * 0.2)(F, D) * (F ** -0.5)),
        }
    _B_in = _B_setup()

    return {
        "A_x": _A_in["x"],
        "A_wqkv": _A_in["wqkv"],
        "A_wo": _A_in["wo"],
        "B_x": _B_in["x"],
        "B_w1": _B_in["w1"],
        "B_b1": _B_in["b1"],
        "B_w2": _B_in["w2"],
    }

def reference(A_x, A_wqkv, A_wo, B_x, B_w1, B_b1, B_w2):
    def _A_ref(x, wqkv, wo):
        B, S, D = x.shape
        H = 32
        Dh = D // H
        qkv = (lambda x, *d: x.permute(*d))((x @ wqkv).reshape(B, S, 3, H, Dh), 2, 0, 3, 1, 4)
        q, k, v = qkv[0], qkv[1], qkv[2]
        scores = torch.einsum("bhqd,bhkd->bhqk", q, k) * (Dh ** -0.5)
        p = _softmax(scores, axis=-1)
        o = torch.einsum("bhqk,bhkd->bhqd", p, v)
        return (lambda x, *d: x.permute(*d))(o, 0, 2, 1, 3).reshape(B, S, D) @ wo

    def _B_ref(x, w1, b1, w2):
        h = torch.einsum("bsd,df->bsf", x, w1) + b1
        sp = torch.log1p(torch.exp(-torch.abs(h))) + torch.where(h > 0, h, torch.zeros_like(h))
        act = h * torch.tanh(sp)
        return torch.einsum("bsf,fd->bsd", act, w2)

    x = A_x
    x = x + _A_ref(_layernorm(x, axis=-1), A_wqkv, A_wo)
    x = x + _B_ref(_layernorm(x, axis=-1), B_w1, B_b1, B_w2)
    x = x + _A_ref(_layernorm(x, axis=-1), A_wqkv, A_wo)
    x = x + _B_ref(_layernorm(x, axis=-1), B_w1, B_b1, B_w2)
    return x
```

```cpp
#include <hip/hip_runtime.h>
#include <hip/hip_bf16.h>

typedef __attribute__((ext_vector_type(2))) __bf16 bf16x2;
typedef __attribute__((ext_vector_type(8))) __bf16 bf16x8;
typedef __attribute__((ext_vector_type(16))) __bf16 bf16x16;
typedef __attribute__((ext_vector_type(16))) float f32x16;
typedef __attribute__((ext_vector_type(8))) float f32x8;
typedef __attribute__((ext_vector_type(4))) float f32x4;
typedef __attribute__((ext_vector_type(2))) float f32x2;
typedef __attribute__((ext_vector_type(4))) unsigned u32x4;
typedef __attribute__((ext_vector_type(2))) unsigned u32x2;
#define LAS __attribute__((address_space(3)))
#define DI __device__ __forceinline__

constexpr int S_ = 2048, D_ = 512, F_ = 1376, H_ = 32;
constexpr int KS_D = D_ / 16;
constexpr int KS_F = F_ / 16;
constexpr int NT_F = F_ / 32;

constexpr size_t WS_WQKV = 0;
constexpr size_t WS_WO = WS_WQKV + 48 * 32 * 1024;
constexpr size_t WS_W1 = WS_WO + 16 * 32 * 1024;
constexpr size_t WS_W2 = WS_W1 + 43 * 32 * 1024;
constexpr size_t WS_CSQ = WS_W2 + 16 * 86 * 1024;
constexpr size_t WS_CS1 = WS_CSQ + 8192;
constexpr size_t WS_B1 = WS_CS1 + 8192;
constexpr size_t WS_X = WS_B1 + 8192;
constexpr size_t WS_Q = WS_X + (2u << 20);
constexpr size_t WS_K = WS_Q + (2u << 20);
constexpr size_t WS_V = WS_K + (2u << 20);
constexpr size_t WS_O = WS_V + (2u << 20);
constexpr size_t WS_H = WS_O + (2u << 20);

#define MFMA(a, b, c) __builtin_amdgcn_mfma_f32_32x32x16_bf16((a), (b), (c), 0, 0, 0)
#define SBAR() __builtin_amdgcn_sched_barrier(0)
#define PIN(x) asm volatile("" : "+v"(x))

DI unsigned pk2(float lo, float hi) {
  bf16x2 v; v[0] = (__bf16)lo; v[1] = (__bf16)hi;
  return __builtin_bit_cast(unsigned, v);
}
DI float bf_lo(unsigned u) { return __uint_as_float(u << 16); }
DI float bf_hi(unsigned u) { return __uint_as_float(u & 0xffff0000u); }
DI float rbf(float x) { return (float)(__bf16)x; }
DI bf16x8 as_bf(u32x4 v) { return __builtin_bit_cast(bf16x8, v); }
DI float swap_half_sum(float x) {
  auto r = __builtin_amdgcn_permlane32_swap(__float_as_uint(x), __float_as_uint(x), false, false);
  return __uint_as_float(r[0]) + __uint_as_float(r[1]);
}
DI float swap_half_max(float x) {
  auto r = __builtin_amdgcn_permlane32_swap(__float_as_uint(x), __float_as_uint(x), false, false);
  return fmaxf(__uint_as_float(r[0]), __uint_as_float(r[1]));
}
DI void glds16(const char* sbase, unsigned voff, unsigned lds_base) {
  unsigned sv;
  asm volatile("s_mov_b32 %0, m0\n\ts_mov_b32 m0, %3\n\ts_nop 0\n\tglobal_load_lds_dwordx4 %1, %2\n\ts_mov_b32 m0, %0"
               : "=&s"(sv) : "v"(voff), "s"(sbase), "s"(lds_base) : "memory");
}
DI void glds16x2(const char* sbase, unsigned voff, unsigned lds_base) {
  unsigned sv;
  asm volatile("s_mov_b32 %0, m0\n\ts_mov_b32 m0, %3\n\ts_nop 0\n\tglobal_load_lds_dwordx4 %1, %2\n\t"
               "global_load_lds_dwordx4 %1, %2 offset:1024\n\ts_mov_b32 m0, %0"
               : "=&s"(sv) : "v"(voff), "s"(sbase), "s"(lds_base) : "memory");
}
DI void gload4(u32x4& a, u32x4& b, u32x4& c, u32x4& d, const char* sbase, unsigned voff) {
  asm volatile("global_load_dwordx4 %0, %4, %5\n\tglobal_load_dwordx4 %1, %4, %5 offset:1024\n\t"
               "global_load_dwordx4 %2, %4, %5 offset:2048\n\tglobal_load_dwordx4 %3, %4, %5 offset:3072"
               : "=&v"(a), "=&v"(b), "=&v"(c), "=&v"(d) : "v"(voff), "s"(sbase) : "memory");
}

DI void gload1(u32x4& a, const char* sbase, unsigned voff) {
  asm volatile("global_load_dwordx4 %0, %1, %2" : "=&v"(a) : "v"(voff), "s"(sbase) : "memory");
}
DI void glds4(const void* g, unsigned lds_base) {
  unsigned sv;
  asm volatile("s_mov_b32 %0, m0\n\ts_mov_b32 m0, %2\n\ts_nop 0\n\tglobal_load_lds_dword %1, off\n\ts_mov_b32 m0, %0"
               : "=&s"(sv) : "v"(g), "s"(lds_base) : "memory");
}
DI void gload4s(u32x4 (&f)[4], const char* p0, const char* p1, const char* p2, const char* p3, unsigned voff) {
  asm volatile("global_load_dwordx4 %0, %4, %5\n\tglobal_load_dwordx4 %1, %4, %6\n\t"
               "global_load_dwordx4 %2, %4, %7\n\tglobal_load_dwordx4 %3, %4, %8"
               : "=&v"(f[0]), "=&v"(f[1]), "=&v"(f[2]), "=&v"(f[3]) : "v"(voff), "s"(p0), "s"(p1), "s"(p2), "s"(p3) : "memory");
}
template <int N> DI void wait4(u32x4 (&f)[4]) {
  asm volatile("s_waitcnt vmcnt(%4)" : "+v"(f[0]), "+v"(f[1]), "+v"(f[2]), "+v"(f[3]) : "n"(N) : "memory");
}
template <int I> struct IC { static constexpr int v = I; };
template <int I, int N, class F> DI void static_for(F&& f) {
  if constexpr (I < N) { f(IC<I>{}); static_for<I + 1, N>(f); }
}

__global__ __launch_bounds__(1024) void k_prep(const unsigned short* __restrict__ x, const unsigned short* __restrict__ wqkv,
                                               const unsigned short* __restrict__ wo, const unsigned short* __restrict__ w1,
                                               const unsigned short* __restrict__ b1, const unsigned short* __restrict__ w2,
                                               char* __restrict__ ws) {
  __shared__ float red[16][64];
  const int tid = threadIdx.x, lane = tid & 63, w = tid >> 6, r = lane & 31, h = lane >> 5;
  int b = blockIdx.x;
  if (b < 64) {
    const int tt = b;
#pragma unroll
    for (int u = 0; u < 2; ++u) {
      const int ks = 2 * w + u;
      const unsigned short* src = x + (size_t)(tt * 32 + r) * D_ + 16 * ks + 4 * h;
      const u32x2 lo = *(const u32x2*)src, hi = *(const u32x2*)(src + 8);
      u32x4 v = {lo[0], lo[1], hi[0], hi[1]};
      *(u32x4*)(ws + WS_X + (size_t)(tt * 32 + ks) * 1024 + lane * 16) = v;
    }
    return;
  }
  b -= 64;
  const unsigned short* W; int N, nt, ks0, ksn, kst; char* dst; float* cs = nullptr;
  if (b < 48) { W = wqkv; N = 3 * D_; nt = b; ks0 = 0; ksn = 32; kst = 32; dst = ws + WS_WQKV; cs = (float*)(ws + WS_CSQ); }
  else if (b < 48 + 43) { W = w1; N = F_; nt = b - 48; ks0 = 0; ksn = 32; kst = 32; dst = ws + WS_W1; cs = (float*)(ws + WS_CS1); }
  else if (b < 48 + 43 + 16) { W = wo; N = D_; nt = b - 91; ks0 = 0; ksn = 32; kst = 32; dst = ws + WS_WO; }
  else { const int c = b - 107; W = w2; N = D_; nt = c / 3; ks0 = (c % 3) * 32; ksn = 86; kst = 86; dst = ws + WS_W2; }
  float s = 0.f;
#pragma unroll
  for (int u = 0; u < 2; ++u) {
    const int ks = ks0 + 2 * w + u;
    if (ks < ksn) {
      unsigned e[8];
#pragma unroll
      for (int j = 0; j < 8; ++j) e[j] = W[(size_t)(16 * ks + 8 * (j >> 2) + 4 * h + (j & 3)) * N + 32 * nt + r];
      u32x4 v;
#pragma unroll
      for (int j = 0; j < 4; ++j) v[j] = e[2 * j] | (e[2 * j + 1] << 16);
#pragma unroll
      for (int j = 0; j < 8; ++j) s += __uint_as_float(e[j] << 16);
      *(u32x4*)(dst + (size_t)(nt * kst + ks) * 1024 + lane * 16) = v;
    }
  }
  if (cs) {
    red[w][lane] = s;
    __syncthreads();
    if (tid < 32) {
      float t = 0.f;
#pragma unroll
      for (int i = 0; i < 16; ++i) t += red[i][tid] + red[i][tid + 32];
      cs[32 * nt + tid] = t;
      if (W == w1) ((float*)(ws + WS_B1))[32 * nt + tid] = __uint_as_float((unsigned)b1[32 * nt + tid] << 16);
    }
  }
}

DI float mish_f(float hh) {
  const float u = __builtin_amdgcn_exp2f(fminf(hh, 30.f) * 1.4426950408889634f);
  const float n = u * (u + 2.f);
  return hh * n * __builtin_amdgcn_rcpf(n + 2.f);
}

template <int MODE, int SS>
DI void epi_frag(const f32x16& acc, const LAS float* csl, float mr, float rstd, char* dst) {
  unsigned p[4];
#pragma unroll
  for (int gg = 0; gg < 2; ++gg) {
    const int g = 2 * SS + gg;
    float v[4];
    const f32x4 cs = *(const LAS f32x4*)(csl + 8 * g);
    f32x4 bb = cs;
    if (MODE == 1) bb = *(const LAS f32x4*)(csl + 96 + 8 * g);
#pragma unroll
    for (int j = 0; j < 4; ++j) {
      if (MODE == 0) v[j] = fmaf(acc[4 * g + j], rstd, mr * cs[j]);
      else v[j] = mish_f(fmaf(acc[4 * g + j], rstd, fmaf(mr, cs[j], bb[j])));
    }
    p[2 * gg] = pk2(v[0], v[1]); p[2 * gg + 1] = pk2(v[2], v[3]);
  }
  *(u32x4*)dst = (u32x4){p[0], p[1], p[2], p[3]};
}

template <int MODE>
__global__ __launch_bounds__(256, 1) void k_gemm_ln(char* __restrict__ ws) {
  extern __shared__ __attribute__((aligned(16))) char lds[];
  const int tid = threadIdx.x, lane = tid & 63, r = lane & 31, h = lane >> 5;
  const int w = __builtin_amdgcn_readfirstlane(tid >> 6);
  const int bid = blockIdx.x, idx = bid >> 3;
  const int tb = (bid & 7) * 2 + (idx >> 4), nb = idx & 15;
  int t0, t1, t2; bool has3 = true;
  if (MODE == 0) { t0 = nb; t1 = 16 + nb; t2 = 32 + nb; }
  else if (nb < 11) { t0 = 3 * nb; t1 = t0 + 1; t2 = t0 + 2; }
  else { t0 = 33 + 2 * (nb - 11); t1 = t0 + 1; t2 = t1; has3 = false; }
  const int tt = tb * 4 + w;
  const char* Wt = ws + (MODE == 0 ? WS_WQKV : WS_W1);
  const float* csp = (const float*)(ws + (MODE == 0 ? WS_CSQ : WS_CS1));
  const float* bp = (const float*)(ws + WS_B1);
  const unsigned lbase = (unsigned)(size_t)(LAS char*)lds;
  const unsigned voff = lane * 16;

  {
    const int e = 2 * w + h;
    const int tl = (e == 0 || e == 3) ? t0 : ((e == 1 || e == 4) ? t1 : t2);
    glds4(((MODE == 1 && e >= 3) ? bp : csp) + 32 * tl + r, lbase + 98304 + w * 256);
  }
  const LAS float* csl = (const LAS float*)(lds + 98304) + 4 * h;

  u32x4 xf[32];
  const char* xsrc = ws + WS_X + (size_t)tt * 32 * 1024;
#pragma unroll
  for (int c = 0; c < 4; ++c) {
    const int ks = 8 * c + 2 * w;
    glds16x2(Wt + (size_t)(t0 * 32 + ks) * 1024, voff, lbase + ks * 1024);
    glds16x2(Wt + (size_t)(t1 * 32 + ks) * 1024, voff, lbase + 32768 + ks * 1024);
    gload4(xf[8 * c], xf[8 * c + 1], xf[8 * c + 2], xf[8 * c + 3], xsrc + c * 8192, voff);
    gload4(xf[8 * c + 4], xf[8 * c + 5], xf[8 * c + 6], xf[8 * c + 7], xsrc + c * 8192 + 4096, voff);
  }
#pragma unroll
  for (int u = 0; u < 4; ++u) glds16x2(Wt + (size_t)(t2 * 32 + 8 * w + 2 * u) * 1024, voff, lbase + 65536 + (8 * w + 2 * u) * 1024);

  f32x16 a0, a1, a2;
#pragma unroll
  for (int i = 0; i < 16; ++i) { a0[i] = 0.f; a1[i] = 0.f; a2[i] = 0.f; }
  float sm[2] = {0.f, 0.f}, sq[2] = {0.f, 0.f};
  const LAS u32x4* wl = (const LAS u32x4*)(lds) + lane;
  const bf16x2 ones = {(__bf16)1.0f, (__bf16)1.0f};
#define CHUNK(c, N)                                                                                                     \
  do {                                                                                                                  \
    asm volatile("s_waitcnt vmcnt(" #N ")\n\ts_barrier"                                                                 \
                 : "+v"(xf[8 * c]), "+v"(xf[8 * c + 1]), "+v"(xf[8 * c + 2]), "+v"(xf[8 * c + 3]), "+v"(xf[8 * c + 4]), \
                   "+v"(xf[8 * c + 5]), "+v"(xf[8 * c + 6]), "+v"(xf[8 * c + 7])::"memory");                            \
    u32x4 w0[8], w1[8];                                                                                                 \
    _Pragma("unroll") for (int k = 0; k < 8; ++k) { w0[k] = wl[(8 * c + k) * 64]; w1[k] = wl[2048 + (8 * c + k) * 64]; } \
    SBAR();                                                                                                             \
    _Pragma("unroll") for (int k = 0; k < 8; ++k) {                                                                     \
      const int ks = 8 * c + k;                                                                                         \
      a0 = MFMA(as_bf(w0[k]), as_bf(xf[ks]), a0);                                                                       \
      _Pragma("unroll") for (int j = 0; j < 4; ++j) {                                                                   \
        const unsigned xu = xf[ks][j]; const bf16x2 xv = __builtin_bit_cast(bf16x2, xu);                                \
        if (j == 2) { SBAR(); a1 = MFMA(as_bf(w1[k]), as_bf(xf[ks]), a1); }                                             \
        sm[j & 1] = __builtin_amdgcn_fdot2_f32_bf16(xv, ones, sm[j & 1], false);                                        \
        sq[j & 1] = __builtin_amdgcn_fdot2_f32_bf16(xv, xv, sq[j & 1], false);                                          \
      }                                                                                                                 \
      PIN(sm[0]); PIN(sm[1]); PIN(sq[0]); PIN(sq[1]);                                                                   \
      SBAR();                                                                                                           \
    }                                                                                                                   \
  } while (0)
  CHUNK(0, 44); CHUNK(1, 32); CHUNK(2, 20); CHUNK(3, 8);
#undef CHUNK
  const float sum = swap_half_sum(sm[0] + sm[1]), ssq = swap_half_sum(sq[0] + sq[1]);
  const float mu = sum * (1.f / D_);
  const float var = fmaxf(ssq * (1.f / D_) - mu * mu, 0.f);
  const float mean = rbf(mu);
  const float rstd = 1.f / rbf(sqrtf(rbf(rbf(var) + 1e-5f)));
  const float mr = -mean * rstd;
  LAS f32x2* st = (LAS f32x2*)(lds + 99328) + w * 32;
  if (MODE == 0 && h == 0) st[r] = (f32x2){mean, rstd};
  asm volatile("s_waitcnt vmcnt(0)\n\ts_barrier" ::: "memory");

  constexpr float QS = 0.25f * 1.4426950408889634f;
  char* d0; char* d1; int dstep;
  if (MODE == 0) {
    const int pr = (r & 19) | ((r & 4) << 1) | ((r & 8) >> 1);
    d0 = ws + WS_Q + ((size_t)(2 * nb) * 64 + tt) * 1024 + lane * 16;
    d1 = ws + WS_K + ((size_t)(2 * nb) * 64 + tt) * 1024 + h * 512 + pr * 16;
    dstep = 65536;
  } else {
    d0 = ws + WS_H + ((size_t)tt * KS_F + 2 * t0) * 1024 + lane * 16;
    d1 = d0 + 2048;
    dstep = 1024;
  }
  const float rs0 = MODE == 0 ? rstd * QS : rstd, mr0 = MODE == 0 ? mr * QS : mr;
  if (has3) {
    u32x4 wa[8], wb[8];
#define RD8(dst, c) _Pragma("unroll") for (int k = 0; k < 8; ++k) dst[k] = wl[4096 + (8 * (c) + k) * 64]
#define MM8(src, c) _Pragma("unroll") for (int k = 0; k < 8; ++k) a2 = MODE == 0 ? MFMA(as_bf(xf[8 * (c) + k]), as_bf(src[k]), a2) : MFMA(as_bf(src[k]), as_bf(xf[8 * (c) + k]), a2)
#define MIX() _Pragma("unroll") for (int k = 0; k < 8; ++k) { __builtin_amdgcn_sched_group_barrier(0x8, 1, 0); __builtin_amdgcn_sched_group_barrier(0x402, MODE == 0 ? 3 : 12, 0); }
    RD8(wa, 0); SBAR();
    RD8(wb, 1); SBAR(); MM8(wa, 0); epi_frag<MODE, 0>(a0, csl, mr0, rs0, d0); MIX(); SBAR();
    RD8(wa, 2); SBAR(); MM8(wb, 1); epi_frag<MODE, 1>(a0, csl, mr0, rs0, d0 + dstep); MIX(); SBAR();
    RD8(wb, 3); SBAR(); MM8(wa, 2); epi_frag<MODE, 0>(a1, csl + 32, mr, rstd, d1); MIX(); SBAR();
    MM8(wb, 3); epi_frag<MODE, 1>(a1, csl + 32, mr, rstd, d1 + dstep); MIX(); SBAR();
#undef RD8
#undef MM8
#undef MIX
    if (MODE == 0) {
      const float csv = ((const LAS float*)(lds + 98304))[64 + r];
      unsigned p[8];
#pragma unroll
      for (int g = 0; g < 4; ++g) {
        const f32x4 s0 = *(const LAS f32x4*)(st + 8 * g + 4 * h), s1 = *(const LAS f32x4*)(st + 8 * g + 4 * h + 2);
        const float v0 = (a2[4 * g] - s0[0] * csv) * s0[1], v1 = (a2[4 * g + 1] - s0[2] * csv) * s0[3];
        const float v2 = (a2[4 * g + 2] - s1[0] * csv) * s1[1], v3 = (a2[4 * g + 3] - s1[2] * csv) * s1[3];
        p[2 * g] = pk2(v0, v1); p[2 * g + 1] = pk2(v2, v3);
      }
      char* vd = ws + WS_V + ((size_t)(2 * nb + (r >> 4)) * 128 + 2 * tt) * 512 + h * 256 + (r & 15) * 16;
      *(u32x4*)vd = (u32x4){p[0], p[1], p[2], p[3]};
      *(u32x4*)(vd + 512) = (u32x4){p[4], p[5], p[6], p[7]};
    } else {
      epi_frag<MODE, 0>(a2, csl + 64, mr, rstd, d0 + 4096);
      epi_frag<MODE, 1>(a2, csl + 64, mr, rstd, d0 + 5120);
    }
  } else {
    epi_frag<MODE, 0>(a0, csl, mr0, rs0, d0);
    epi_frag<MODE, 1>(a0, csl, mr0, rs0, d0 + dstep);
    epi_frag<MODE, 0>(a1, csl + 32, mr, rstd, d1);
    epi_frag<MODE, 1>(a1, csl + 32, mr, rstd, d1 + dstep);
  }
}

template <int KS, bool FINAL>
__global__ __launch_bounds__(256, 1) void k_gemm_res(char* __restrict__ ws, size_t a_off, size_t w_off, unsigned short* __restrict__ out) {
  __shared__ __attribute__((aligned(16))) f32x4 red[4][3][4][64];
  const int tid = threadIdx.x, lane = tid & 63, r = lane & 31, h = lane >> 5;
  const int w = __builtin_amdgcn_readfirstlane(tid >> 6);
  const int bid = blockIdx.x, idx = bid >> 3;
  const int tb = (bid & 7) * 4 + (idx >> 3), nb = idx & 7;
  constexpr int PER = (KS + 3) / 4;
  constexpr int REM = KS - 4 * (PER - 1);
  const int k0 = w < REM ? w * PER : REM * PER + (w - REM) * (PER - 1);
  const bool full = w < REM;
  char* xs = ws + WS_X + ((size_t)(2 * tb + (w >> 1)) * 32 + 2 * (2 * nb + (w & 1))) * 1024;
  char* xp = xs + lane * 16;
  const unsigned voff = lane * 16;
  u32x4 res0, res1;
  gload1(res0, xs, voff); gload1(res1, xs + 1024, voff);
  const char* A0 = ws + a_off + ((size_t)(2 * tb) * KS + k0) * 1024;
  const char* W0 = ws + w_off + ((size_t)(2 * nb) * KS + k0) * 1024;
  f32x16 acc[4];
#pragma unroll
  for (int t = 0; t < 4; ++t)
#pragma unroll
    for (int i = 0; i < 16; ++i) acc[t][i] = 0.f;
  constexpr int WIN = PER < 12 ? PER : 12;
  u32x4 f[WIN][4];
  auto issue = [&](auto kc) __attribute__((always_inline)) {
    constexpr int k = decltype(kc)::v;
    const int kb = ((k == PER - 1 && REM != 4 && !full) ? k - 1 : k) * 1024;
    gload4s(f[k % WIN], A0 + kb, W0 + kb, A0 + KS * 1024 + kb, W0 + KS * 1024 + kb, voff);
  };
  static_for<0, WIN>(issue);
  static_for<0, PER>([&](auto kc) __attribute__((always_inline)) {
    constexpr int k = decltype(kc)::v;
    constexpr int issued = (k + WIN < PER) ? k + WIN : PER;
    wait4<4 * (issued - 1 - k)>(f[k % WIN]);
    if (k < PER - 1 || REM == 4 || full) {
      acc[0] = MFMA(as_bf(f[k % WIN][1]), as_bf(f[k % WIN][0]), acc[0]);
      acc[2] = MFMA(as_bf(f[k % WIN][1]), as_bf(f[k % WIN][2]), acc[2]);
      acc[1] = MFMA(as_bf(f[k % WIN][3]), as_bf(f[k % WIN][0]), acc[1]);
      acc[3] = MFMA(as_bf(f[k % WIN][3]), as_bf(f[k % WIN][2]), acc[3]);
    }
    SBAR();
    if constexpr (k + WIN < PER) issue(IC<k + WIN>{});
  });
  PIN(res0); PIN(res1);
#pragma unroll
  for (int t = 0; t < 4; ++t) {
    if (t != w) {
      const int slot = (w - t - 1) & 3;
#pragma unroll
      for (int g = 0; g < 4; ++g) red[t][slot][g][lane] = (f32x4){acc[t][4 * g], acc[t][4 * g + 1], acc[t][4 * g + 2], acc[t][4 * g + 3]};
    }
  }
  f32x16 mine = w == 0 ? acc[0] : (w == 1 ? acc[1] : (w == 2 ? acc[2] : acc[3]));
  __syncthreads();
#pragma unroll
  for (int s = 0; s < 3; ++s)
#pragma unroll
    for (int g = 0; g < 4; ++g) {
      const f32x4 v = red[w][s][g][lane];
#pragma unroll
      for (int j = 0; j < 4; ++j) mine[4 * g + j] += v[j];
    }
  unsigned p[8];
#pragma unroll
  for (int i = 0; i < 8; ++i) {
    const unsigned rr = i < 4 ? res0[i] : res1[i - 4];
    p[i] = pk2(bf_lo(rr) + rbf(mine[2 * i]), bf_hi(rr) + rbf(mine[2 * i + 1]));
  }
  if (!FINAL) {
    *(u32x4*)xp = (u32x4){p[0], p[1], p[2], p[3]};
    *(u32x4*)(xp + 1024) = (u32x4){p[4], p[5], p[6], p[7]};
  } else {
    unsigned short* op = out + (size_t)((2 * tb + (w >> 1)) * 32 + r) * D_ + 32 * (2 * nb + (w & 1)) + 4 * h;
#pragma unroll
    for (int g = 0; g < 4; ++g) *(u32x2*)(op + 8 * g) = (u32x2){p[2 * g], p[2 * g + 1]};
  }
}

constexpr int AT_K = 0, AT_V = 65536, AT_ONE = 131072, AT_CMB = AT_ONE + 4096, AT_LDS = AT_CMB + 8 * 64 * 40;
#define SMF(a, b8, c) __builtin_amdgcn_smfmac_f32_32x32x32_bf16((a), __builtin_bit_cast(bf16x16, b8), (c), sidx, 0, 0)
#define LO8(s) __builtin_shufflevector(s, s, 0, 1, 2, 3, 4, 5, 6, 7)
#define HI8(s) __builtin_shufflevector(s, s, 8, 9, 10, 11, 12, 13, 14, 15)

DI float max16(const f32x16& s) {
  float a = fmaxf(fmaxf(s[0], s[1]), s[2]), b = fmaxf(fmaxf(s[3], s[4]), s[5]);
  a = fmaxf(fmaxf(a, s[6]), s[7]); b = fmaxf(fmaxf(b, s[8]), s[9]);
  a = fmaxf(fmaxf(a, s[10]), s[11]); b = fmaxf(fmaxf(b, s[12]), s[13]);
  a = fmaxf(fmaxf(a, s[14]), s[15]);
  return swap_half_max(fmaxf(a, b));
}

__global__ __launch_bounds__(1024) void k_attn(char* __restrict__ ws) {
  extern __shared__ __attribute__((aligned(16))) char lds[];
  const int tid = threadIdx.x, lane = tid & 63, r = lane & 31, h = lane >> 5;
  const int w = __builtin_amdgcn_readfirstlane(tid >> 6);
  const int bid = blockIdx.x;
  const int head = (bid & 7) * 4 + ((bid >> 3) & 3), qb = bid >> 5;
  const int half = w >> 3, qt = qb * 8 + (w & 7);
  const unsigned lbase = (unsigned)(size_t)(LAS char*)lds;
  const unsigned voff = lane * 16;
  const char* Kg = ws + WS_K + (size_t)head * 65536;
  const char* Vg = ws + WS_V + (size_t)head * 65536;
  u32x4 qv;
  gload1(qv, ws + WS_Q + ((size_t)head * 64 + qt) * 1024, voff);
  {
    const int off = (w >> 3) * 32768 + (w & 3) * 1024;
    if (w & 4) glds16(Vg + off, voff, lbase + AT_V + off); else glds16(Kg + off, voff, lbase + AT_K + off);
  }
  {
    const int off = (w >> 3) * 32768 + 4096 + (w & 3) * 1024;
    const char* src = (w & 4) ? Vg : Kg; const unsigned dst = lbase + ((w & 4) ? AT_V : AT_K);
#pragma unroll
    for (int u = 0; u < 7; ++u) glds16(src + off + u * 4096, voff, dst + off + u * 4096);
  }
  if (tid < 256) ((LAS u32x4*)(lds + AT_ONE))[tid] = (u32x4){0x3f803f80u, 0x3f803f80u, 0x3f803f80u, 0x3f803f80u};
  const int sidx = 0xDDDDDDDD;
  const LAS char* kp = (const LAS char*)lds + AT_K + half * 32768 + lane * 16;
  const LAS char* vp0 = (const LAS char*)lds + (r < 16 ? AT_V + half * 32768 : AT_ONE) + h * 256 + (r & 15) * 16;
  const int vstep = r < 16 ? 4096 : 0;
  asm volatile("s_waitcnt vmcnt(7) lgkmcnt(0)\n\ts_barrier" : "+v"(qv)::"memory");
  const bf16x8 q = as_bf(qv);

  f32x16 zero;
#pragma unroll
  for (int i = 0; i < 16; ++i) zero[i] = 0.f;
  f32x16 cin, o = zero, sA, sB;
  float m;
  {
    const f32x16 s = MFMA(*(const LAS bf16x8*)kp, q, zero);
    m = max16(s);
#pragma unroll
    for (int i = 0; i < 16; ++i) { cin[i] = -m; sA[i] = s[i] - m; }
  }
  PIN(cin);
  const LAS char* vp = vp0;
#define EXPS(s, lo, hi) do { _Pragma("unroll") for (int i = lo; i < hi; ++i) s[i] = __builtin_amdgcn_exp2f(s[i]); PIN(s); SBAR(); } while (0)
#define STEP(cur, nxt, KOFF, VOFF)                                                   \
  do {                                                                               \
    const bf16x8 v0 = *(const LAS bf16x8*)(vp + (VOFF));                             \
    const bf16x8 v1 = *(const LAS bf16x8*)(vp + (VOFF) + 512);                       \
    SBAR();                                                                          \
    EXPS(cur, 0, 4);                                                                 \
    nxt = MFMA(kf, q, cin); PIN(nxt); SBAR();                                        \
    kf = *(const LAS bf16x8*)(kp + (KOFF)); SBAR();                                  \
    EXPS(cur, 4, 10);                                                                \
    o = SMF(v0, LO8(cur), o); PIN(o); SBAR();                                        \
    EXPS(cur, 10, 16);                                                               \
    o = SMF(v1, HI8(cur), o); PIN(o); SBAR();                                        \
  } while (0)
  bf16x8 kf = *(const LAS bf16x8*)(kp + 1024);
  STEP(sA, sB, 2048, 0);
  STEP(sB, sA, 3072, 1024);
  asm volatile("s_waitcnt vmcnt(0)\n\ts_barrier" ::: "memory");
  STEP(sA, sB, 4096, 2048);
  STEP(sB, sA, 5120, 3072);
  for (int it = 1; it < 8; ++it) {
    kp += 4096; vp += vstep;
    STEP(sA, sB, 2048, 0);
    STEP(sB, sA, 3072, 1024);
    STEP(sA, sB, 4096, 2048);
    STEP(sB, sA, 5120, 3072);
  }
#undef STEP
  if (__any(!(o[8] < 1.0995116e12f))) {
    kp = (const LAS char*)lds + AT_K + half * 32768 + lane * 16;
    vp = vp0; o = zero; m = -3.0e38f;
    for (int t = 0; t < 32; ++t) {
      f32x16 s = MFMA(*(const LAS bf16x8*)(kp + t * 1024), q, zero);
      const float mn = fmaxf(m, max16(s));
      const float al = __builtin_amdgcn_exp2f(m - mn);
      m = mn;
#pragma unroll
      for (int i = 0; i < 16; ++i) { o[i] *= al; s[i] = __builtin_amdgcn_exp2f(s[i] - mn); }
      const int vo = r < 16 ? t * 1024 : 0;
      o = SMF(*(const LAS bf16x8*)(vp + vo), LO8(s), o);
      o = SMF(*(const LAS bf16x8*)(vp + vo + 512), HI8(s), o);
    }
  }
  LAS float* cmb = (LAS float*)(lds + AT_CMB) + (w & 7) * 640 + lane;
  if (half == 1) {
#pragma unroll
    for (int i = 0; i < 9; ++i) cmb[i * 64] = o[i];
    cmb[9 * 64] = m;
  }
  __syncthreads();
  if (half == 0) {
    const float m2 = cmb[9 * 64];
    const float mm = fmaxf(m, m2);
    const float f1 = __builtin_amdgcn_exp2f(m - mm), f2 = __builtin_amdgcn_exp2f(m2 - mm);
    const float inv = 1.f / (o[8] * f1 + cmb[8 * 64] * f2);
    const float g1 = f1 * inv, g2 = f2 * inv;
    unsigned p[4];
#pragma unroll
    for (int i = 0; i < 4; ++i) p[i] = pk2(o[2 * i] * g1 + cmb[2 * i * 64] * g2, o[2 * i + 1] * g1 + cmb[(2 * i + 1) * 64] * g2);
    *(u32x4*)(ws + WS_O + ((size_t)qt * 32 + head) * 1024 + lane * 16) = (u32x4){p[0], p[1], p[2], p[3]};
  }
}

extern "C" void kernel_launch(void* const* d_in, const int* in_sizes, int n_in, void* d_out, int out_size, void* d_ws,
                              size_t ws_size, hipStream_t stream) {
  const unsigned short* x = (const unsigned short*)d_in[0];
  const unsigned short* wqkv = (const unsigned short*)d_in[1];
  const unsigned short* wo = (const unsigned short*)d_in[2];
  const unsigned short* w1 = (const unsigned short*)d_in[4];
  const unsigned short* b1 = (const unsigned short*)d_in[5];
  const unsigned short* w2 = (const unsigned short*)d_in[6];
  char* ws = (char*)d_ws;
  unsigned short* out = (unsigned short*)d_out;
  constexpr int LN_LDS = 98304 + 2048;
  (void)hipFuncSetAttribute((const void*)k_gemm_ln<0>, hipFuncAttributeMaxDynamicSharedMemorySize, LN_LDS);
  (void)hipFuncSetAttribute((const void*)k_gemm_ln<1>, hipFuncAttributeMaxDynamicSharedMemorySize, LN_LDS);
  (void)hipFuncSetAttribute((const void*)k_attn, hipFuncAttributeMaxDynamicSharedMemorySize, AT_LDS);
  k_prep<<<64 + 48 + 43 + 16 + 48, 1024, 0, stream>>>(x, wqkv, wo, w1, b1, w2, ws);
  for (int l = 0; l < 2; ++l) {
    k_gemm_ln<0><<<256, 256, LN_LDS, stream>>>(ws);
    k_attn<<<256, 1024, AT_LDS, stream>>>(ws);
    k_gemm_res<KS_D, false><<<256, 256, 0, stream>>>(ws, WS_O, WS_WO, out);
    k_gemm_ln<1><<<256, 256, LN_LDS, stream>>>(ws);
    if (l == 0) k_gemm_res<KS_F, false><<<256, 256, 0, stream>>>(ws, WS_H, WS_W2, out);
    else k_gemm_res<KS_F, true><<<256, 256, 0, stream>>>(ws, WS_H, WS_W2, out);
  }
}
```

```cpp
#include <hip/hip_runtime.h>
#include <hip/hip_bf16.h>

typedef __attribute__((ext_vector_type(2))) __bf16 bf16x2;
typedef __attribute__((ext_vector_type(8))) __bf16 bf16x8;
typedef __attribute__((ext_vector_type(16))) __bf16 bf16x16;
typedef __attribute__((ext_vector_type(16))) float f32x16;
typedef __attribute__((ext_vector_type(8))) float f32x8;
typedef __attribute__((ext_vector_type(4))) float f32x4;
typedef __attribute__((ext_vector_type(2))) float f32x2;
typedef __attribute__((ext_vector_type(4))) unsigned u32x4;
typedef __attribute__((ext_vector_type(2))) unsigned u32x2;
typedef unsigned long long u64;
#define LAS __attribute__((address_space(3)))
#define GAS __attribute__((address_space(1)))
typedef GAS u64 gu64;
#define DI __device__ __forceinline__
#define RLX_AGENT __ATOMIC_RELAXED, __HIP_MEMORY_SCOPE_AGENT

constexpr int S_ = 2048, D_ = 512, F_ = 1376;
constexpr int KS_F = F_ / 16;

constexpr unsigned MB = 1u << 20;
constexpr unsigned WS_FLAG = 0;
constexpr unsigned WS_WQKV = 128 * 1024;
constexpr unsigned WS_WO = WS_WQKV + 48 * 32 * 1024;
constexpr unsigned WS_W1 = WS_WO + 16 * 32 * 1024;
constexpr unsigned WS_W2 = WS_W1 + 43 * 32 * 1024;
constexpr unsigned WS_CSP = WS_W2 + 16 * 86 * 1024;
constexpr unsigned WS_ST = WS_CSP + 2 * 16 * 32 * 384;
constexpr unsigned WS_X = WS_ST + 4 * 64 * 4096;
constexpr unsigned WS_L = WS_X + 8 * MB;
constexpr unsigned L_Q = 0, L_K = 2 * MB, L_V = 4 * MB, L_O = 6 * MB, L_H = 8 * MB, L_SZ = 14 * MB;
constexpr unsigned WS_END = WS_L + 2 * L_SZ;
constexpr int FL_X = 1;
constexpr int FL_H = FL_X + 64;
constexpr int FL_Q = FL_H + 32;
constexpr int FL_O = FL_Q + 32;
constexpr int FL_WQ = FL_O + 64;
constexpr int FL_W1 = FL_WQ + 16;
constexpr int FL_W2 = FL_W1 + 16;
static_assert((FL_W2 + 8) * 256 <= 128 * 1024, "flag region");

constexpr int LD_W = 0;
constexpr int LD_V = 65536;
constexpr int LD_EX = 98304;
constexpr int LD_ONE = 131072;
constexpr int LD_CMB = 135168;
constexpr int LD_SC = 147456;
constexpr int LD_ST = 155648;
constexpr int LD_PAR = 157696;
constexpr int LD_CTR = LD_PAR + 1280;
constexpr int LD_DBG = LD_CTR + 256;
constexpr int LDS_BYTES = 163840;

#define MFMA(a, b, c) __builtin_amdgcn_mfma_f32_32x32x16_bf16((a), (b), (c), 0, 0, 0)
#define SBAR() __builtin_amdgcn_sched_barrier(0)
#define PIN(x) asm volatile("" : "+v"(x))
#define BARRIER_LDS() asm volatile("s_waitcnt lgkmcnt(0)\n\ts_barrier" ::: "memory")
#define BARRIER_ALL() asm volatile("s_waitcnt vmcnt(0) lgkmcnt(0)\n\ts_barrier" ::: "memory")

#define STAMP(i)

DI unsigned pk2(float lo, float hi) {
  bf16x2 v; v[0] = (__bf16)lo; v[1] = (__bf16)hi;
  return __builtin_bit_cast(unsigned, v);
}
DI float bf_lo(unsigned u) { return __uint_as_float(u << 16); }
DI float bf_hi(unsigned u) { return __uint_as_float(u & 0xffff0000u); }
DI float rbf(float x) { return (float)(__bf16)x; }
DI bf16x8 as_bf(u32x4 v) { return __builtin_bit_cast(bf16x8, v); }
DI float swap_half_sum(float x) {
  auto r = __builtin_amdgcn_permlane32_swap(__float_as_uint(x), __float_as_uint(x), false, false);
  return __uint_as_float(r[0]) + __uint_as_float(r[1]);
}
DI float swap_half_max(float x) {
  auto r = __builtin_amdgcn_permlane32_swap(__float_as_uint(x), __float_as_uint(x), false, false);
  return fmaxf(__uint_as_float(r[0]), __uint_as_float(r[1]));
}
DI void glds16(const char* sbase, unsigned voff, unsigned lds_base) {
  unsigned sv;
  asm volatile("s_mov_b32 %0, m0\n\ts_mov_b32 m0, %3\n\ts_nop 2\n\tglobal_load_lds_dwordx4 %1, %2 sc1\n\ts_mov_b32 m0, %0"
               : "=&s"(sv) : "v"(voff), "s"(sbase), "s"(lds_base) : "memory");
}
DI void glds16x2(const char* sbase, unsigned voff, unsigned lds_base) {
  unsigned sv;
  asm volatile("s_mov_b32 %0, m0\n\ts_mov_b32 m0, %3\n\ts_nop 2\n\tglobal_load_lds_dwordx4 %1, %2 sc1\n\t"
               "global_load_lds_dwordx4 %1, %2 offset:1024 sc1\n\ts_mov_b32 m0, %0"
               : "=&s"(sv) : "v"(voff), "s"(sbase), "s"(lds_base) : "memory");
}
template <int I> struct IC { static constexpr int v = I; };
template <int I, int N, class F> DI void static_for(F&& f) {
  if constexpr (I < N) { f(IC<I>{}); static_for<I + 1, N>(f); }
}

struct Ctx {
  char* ws; LAS char* lds; __amdgpu_buffer_rsrc_t rs;
  u64 token; unsigned lbase, voff;
  int bid, lane, w, r, h, tb, nb, tb64, nb8, head, qb;
  bool dead;
};
DI void fresh(Ctx& c) {
  asm volatile("" : "+s"(c.bid));
  asm volatile("" : "+s"(c.w));
  asm volatile("" : "+v"(c.lane));
  const int g = c.bid & 7, idx = c.bid >> 3;
  c.r = c.lane & 31; c.h = c.lane >> 5; c.voff = c.lane * 16;
  c.tb = 2 * g + (idx >> 4); c.nb = idx & 15; c.tb64 = 4 * g + (idx >> 3); c.nb8 = idx & 7;
  c.head = 4 * g + (idx & 3); c.qb = idx >> 2;
}
DI u32x4 ld16(const Ctx& c, unsigned voff, unsigned soff) { return __builtin_amdgcn_raw_buffer_load_b128(c.rs, voff, soff, 16); }
DI float ld4(const Ctx& c, unsigned voff, unsigned soff) { return __uint_as_float(__builtin_amdgcn_raw_buffer_load_b32(c.rs, voff, soff, 16)); }
DI void st16(const Ctx& c, u32x4 v, unsigned voff, unsigned soff) { __builtin_amdgcn_raw_buffer_store_b128(v, c.rs, voff + soff, 0, 16); }
DI void st4(const Ctx& c, float v, unsigned voff, unsigned soff) { __builtin_amdgcn_raw_buffer_store_b32(__float_as_uint(v), c.rs, voff, soff, 16); }
DI gu64* flag_ptr(const Ctx& c, int line, int slot) { return (gu64*)(c.ws + WS_FLAG) + line * 32 + slot; }

DI void publish(const Ctx& c, int ctr, unsigned n, int line, int slot) {
  asm volatile("s_waitcnt vmcnt(0)" ::: "memory");
  if (c.lane == 0) {
    const unsigned old = __hip_atomic_fetch_add((LAS unsigned*)(c.lds + LD_CTR) + ctr, 1u, __ATOMIC_RELAXED, __HIP_MEMORY_SCOPE_WORKGROUP);
    if (old == n - 1) __hip_atomic_store(flag_ptr(c, line, slot), c.token, RLX_AGENT);
  }
}
DI void poll(Ctx& c, int line, int slot) {
  if (c.w == 0 && !c.dead) {
    gu64* p = flag_ptr(c, line, slot);
    unsigned spins = 0;
    while (!__all(__hip_atomic_load(p, RLX_AGENT) == c.token)) {
      if (++spins > (1u << 18)) { c.dead = true; break; }
    }
    __builtin_amdgcn_fence(__ATOMIC_ACQUIRE, "wavefront");
  }
}
template <int NP> DI void help_dma(const Ctx& c, unsigned src, int dst) {
#pragma unroll
  for (int u = 0; u < NP / 4; ++u) {
    const int p = (c.w & 3) + 4 * u;
    glds16x2(c.ws + src + p * 2048, c.voff, c.lbase + dst + p * 2048);
  }
}
DI void dma_wqkv(const Ctx& c) {
  help_dma<16>(c, WS_WQKV + c.nb * 32768, LD_W);
  help_dma<16>(c, WS_WQKV + (16 + c.nb) * 32768, LD_W + 32768);
  help_dma<16>(c, WS_WQKV + (32 + c.nb) * 32768, LD_W + 65536);
}
template <int P0, int P1> DI void dma_w2(const Ctx& c) {
#pragma unroll
  for (int u = 0; u < (P1 - P0) / 4; ++u) {
    const int p = P0 + (c.w & 3) + 4 * u;
    const int kq = p / 18, nt = (p / 9) & 1, kk = 2 * (p % 9);
    glds16x2(c.ws + WS_W2 + ((2 * c.nb8 + nt) * KS_F + kq * 21 + (kq < 2 ? kq : 2) + kk) * 1024, c.voff, c.lbase + LD_W + p * 2048);
  }
}
DI unsigned warm8k(const Ctx& c, unsigned off) { return __builtin_amdgcn_raw_buffer_load_b32(c.rs, c.lane * 128, off, 0); }

DI void store_x(const Ctx& c, int inst, const u32x4 (&res)[2]) {
  const int tt = 2 * c.tb64 + (c.w >> 1), nt = 2 * c.nb8 + (c.w & 1);
  st16(c, res[0], c.voff, WS_X + inst * (2 * MB) + (tt * 32 + 2 * nt) * 1024);
  st16(c, res[1], c.voff, WS_X + inst * (2 * MB) + (tt * 32 + 2 * nt + 1) * 1024);
  float s1 = 0.f, s2 = 0.f;
#pragma unroll
  for (int i = 0; i < 8; ++i) {
    const unsigned u = res[i >> 2][i & 3];
    const float a = bf_lo(u), b = bf_hi(u);
    s1 += a + b; s2 = fmaf(a, a, fmaf(b, b, s2));
  }
  auto r1 = __builtin_amdgcn_permlane32_swap(__float_as_uint(s1), __float_as_uint(s2), false, false);
  st4(c, __uint_as_float(r1[0]) + __uint_as_float(r1[1]), c.lane * 4, WS_ST + inst * (64 * 4096) + tt * 4096 + nt * 256);
}

DI void wfrag_load(const unsigned short* W, int N, int nt, int ks, int r, int h, unsigned (&e)[8]) {
#pragma unroll
  for (int j = 0; j < 8; ++j) e[j] = W[(size_t)(16 * ks + 8 * (j >> 2) + 4 * h + (j & 3)) * N + 32 * nt + r];
}
DI void wfrag_store(const Ctx& c, const unsigned (&e)[8], unsigned dst, bool docs, unsigned csdst) {
  u32x4 v;
#pragma unroll
  for (int j = 0; j < 4; ++j) v[j] = e[2 * j] | (e[2 * j + 1] << 16);
  st16(c, v, c.voff, dst);
  if (docs) {
    float s = 0.f;
#pragma unroll
    for (int j = 0; j < 8; ++j) s += __uint_as_float(e[j] << 16);
    s = swap_half_sum(s);
    if (c.h == 0) st4(c, s, c.r * 4, csdst);
  }
}

DI float mish_f(float hh) {
  const float u = __builtin_amdgcn_exp2f(fminf(hh, 30.f) * 1.4426950408889634f);
  const float n = u * (u + 2.f);
  return hh * n * __builtin_amdgcn_rcpf(n + 2.f);
}
DI void pin2(u32x2& v) { PIN(v); }
template <int MODE>
DI u32x2 epi4(float a0, float a1, float a2, float a3, const LAS float* par, float mr, float rstd) {
  const float a[4] = {a0, a1, a2, a3};
  float v[4];
  const f32x4 cs = *(const LAS f32x4*)par;
  f32x4 bb = cs;
  if (MODE == 1) bb = *(const LAS f32x4*)(par + 96);
#pragma unroll
  for (int j = 0; j < 4; ++j) {
    if (MODE == 0) v[j] = fmaf(a[j], rstd, mr * cs[j]);
    else v[j] = mish_f(fmaf(a[j], rstd, fmaf(mr, cs[j], bb[j])));
  }
  return (u32x2){pk2(v[0], v[1]), pk2(v[2], v[3])};
}
template <int MODE, int SS>
DI u32x4 epi_frag(const f32x16& a, const LAS float* par, float mr, float rstd) {
  const u32x2 lo = epi4<MODE>(a[8 * SS], a[8 * SS + 1], a[8 * SS + 2], a[8 * SS + 3], par + 16 * SS, mr, rstd);
  const u32x2 hi = epi4<MODE>(a[8 * SS + 4], a[8 * SS + 5], a[8 * SS + 6], a[8 * SS + 7], par + 16 * SS + 8, mr, rstd);
  return (u32x4){lo[0], lo[1], hi[0], hi[1]};
}
#define LO8(s) __builtin_shufflevector(s, s, 0, 1, 2, 3, 4, 5, 6, 7)
#define HI8(s) __builtin_shufflevector(s, s, 8, 9, 10, 11, 12, 13, 14, 15)
DI f32x4 q4(const f32x8& v, int g) { return g ? (f32x4){v[4], v[5], v[6], v[7]} : (f32x4){v[0], v[1], v[2], v[3]}; }
DI f32x8 cat8(f32x4 a, f32x4 b) { return __builtin_shufflevector(a, b, 0, 1, 2, 3, 4, 5, 6, 7); }

template <int MODE>
DI void phase_ln(Ctx& c, int l, int inst, u32x4 (&w2r)[8]) {
  const int PH = (1 + 5 * l + (MODE ? 3 : 0)) * 10; (void)PH;
  const int tt = 4 * c.tb + c.w;
  const bool has3 = MODE == 0 || c.nb < 11;
  const unsigned xs = WS_X + inst * (2 * MB) + tt * 32768;
  constexpr int PD = 16;
  u32x4 xf[32], sp[4];
#pragma unroll
  for (int k = 0; k < PD; ++k) xf[k] = ld16(c, c.voff, xs + k * 1024);
  SBAR();
  const LAS u32x4* wl = (const LAS u32x4*)(c.lds + LD_W) + c.lane;
  f32x16 aA, aB, aC;
#pragma unroll
  for (int i = 0; i < 16; ++i) { aA[i] = 0.f; aB[i] = 0.f; aC[i] = 0.f; }
  u32x4 fa[3], fb[3];
  fa[0] = wl[0]; fb[0] = wl[2048]; fa[1] = wl[64]; fb[1] = wl[2048 + 64];
  float mean = 0.f, rstd = 0.f, sc4[4];
  LAS float* sc = (LAS float*)(c.lds + LD_SC) + c.w * 256;
  LAS f32x2* st = (LAS f32x2*)(c.lds + LD_ST) + c.w * 32;
  static_for<0, 32>([&](auto kc) __attribute__((always_inline)) {
    constexpr int k = decltype(kc)::v;
    if constexpr (k + 2 < 32) { fa[(k + 2) % 3] = wl[(k + 2) * 64]; fb[(k + 2) % 3] = wl[2048 + (k + 2) * 64]; }
    if constexpr (k + PD < 32) xf[k + PD] = ld16(c, c.voff, xs + (k + PD) * 1024);
    if constexpr (k >= 16 && k < 18) { sp[2 * k - 32] = ld16(c, c.voff, WS_ST + inst * (64 * 4096) + tt * 4096 + (2 * k - 32) * 1024); sp[2 * k - 31] = ld16(c, c.voff, WS_ST + inst * (64 * 4096) + tt * 4096 + (2 * k - 31) * 1024); }
    if constexpr (k == 27) {
      ((LAS f32x4*)sc)[c.lane] = (__builtin_bit_cast(f32x4, sp[0]) + __builtin_bit_cast(f32x4, sp[1])) +
                                 (__builtin_bit_cast(f32x4, sp[2]) + __builtin_bit_cast(f32x4, sp[3]));
    }
    if constexpr (k == 28) {
#pragma unroll
      for (int i = 0; i < 4; ++i) sc4[i] = sc[i * 64 + c.lane];
    }
    if constexpr (k == 30) {
      const float s = (sc4[0] + sc4[1]) + (sc4[2] + sc4[3]);
      auto rr = __builtin_amdgcn_permlane32_swap(__float_as_uint(s), __float_as_uint(s), false, false);
      const float mu = __uint_as_float(rr[0]) * (1.f / D_);
      const float var = fmaxf(__uint_as_float(rr[1]) * (1.f / D_) - mu * mu, 0.f);
      mean = rbf(mu);
      rstd = __builtin_amdgcn_rcpf(rbf(__builtin_amdgcn_sqrtf(rbf(rbf(var) + 1e-5f))));
      PIN(mean); PIN(rstd);
      if (MODE == 0 && c.h == 0) st[c.r] = (f32x2){mean, rstd};
    }
    SBAR();
    aA = MFMA(as_bf(fa[k % 3]), as_bf(xf[k]), aA);
    aB = MFMA(as_bf(fb[k % 3]), as_bf(xf[k]), aB);
    SBAR();
  });
  STAMP(PH + 2);
  if (l == 0) BARRIER_LDS();
  const LAS float* par = (const LAS float*)(c.lds + LD_PAR) + MODE * 96 + 4 * c.h;
  const unsigned lay = WS_L + l * L_SZ;
  constexpr float QS = 0.25f * 1.4426950408889634f;
  const float mr = -mean * rstd, rsA = MODE == 0 ? rstd * QS : rstd, mrA = MODE == 0 ? mr * QS : mr;
  unsigned voB, soA, soB, stepAB;
  if (MODE == 0) {
    voB = c.h * 512 + ((c.r & 19) | ((c.r & 4) << 1) | ((c.r & 8) >> 1)) * 16;
    soA = lay + L_Q + (2 * c.nb * 64 + tt) * 1024; soB = soA + (L_K - L_Q); stepAB = 65536;
  } else {
    voB = c.voff;
    soA = lay + L_H + (tt * KS_F + 2 * (c.nb < 11 ? 3 * c.nb : 33 + 2 * (c.nb - 11))) * 1024; soB = soA + 2048; stepAB = 1024;
  }
  const int k0q = c.w * 21 + (c.w < 2 ? c.w : 2);
  const unsigned w2s = WS_W2 + ((2 * c.nb8 + (c.w & 1)) * KS_F + k0q + 18) * 1024;
  const unsigned w2o = WS_W2 + ((2 * c.nb8 + 1 - (c.w & 1)) * KS_F + k0q + 18) * 1024;
#define W2R(i) do { if (MODE == 1) { const int kk = ((i) == 3 && c.w >= 2) ? 2 : (i);     \
    w2r[2 * (i)] = ld16(c, c.voff, w2s + kk * 1024); w2r[2 * (i) + 1] = ld16(c, c.voff, w2o + kk * 1024); } } while (0)
  if (has3) {
    u32x4 wc[2][4];
#pragma unroll
    for (int k = 0; k < 4; ++k) wc[0][k] = wl[4096 + k * 64];
    u32x2 lo;
    static_for<0, 8>([&](auto qc) __attribute__((always_inline)) {
      constexpr int q = decltype(qc)::v;
      if constexpr (q + 1 < 8) {
#pragma unroll
        for (int k = 0; k < 4; ++k) wc[(q + 1) & 1][k] = wl[4096 + (4 * q + 4 + k) * 64];
      }
      if constexpr (q >= 4) W2R(q - 4);
      SBAR();
#pragma unroll
      for (int k = 0; k < 4; ++k)
        aC = MODE == 0 ? MFMA(as_bf(xf[4 * q + k]), as_bf(wc[q & 1][k]), aC) : MFMA(as_bf(wc[q & 1][k]), as_bf(xf[4 * q + k]), aC);
      const f32x16& a = q < 4 ? aA : aB;
      constexpr int g = q & 3;
      const u32x2 e = epi4<MODE>(a[4 * g], a[4 * g + 1], a[4 * g + 2], a[4 * g + 3], par + 8 * q, q < 4 ? mrA : mr, q < 4 ? rsA : rstd);
      if constexpr (q & 1) st16(c, (u32x4){lo[0], lo[1], e[0], e[1]}, q < 4 ? c.voff : voB, (q < 4 ? soA : soB) + (g >> 1) * stepAB);
      else { lo = e; pin2(lo); }
#pragma unroll
      for (int k = 0; k < 4; ++k) { __builtin_amdgcn_sched_group_barrier(0x8, 1, 0); __builtin_amdgcn_sched_group_barrier(0x402, MODE == 0 ? 3 : 10, 0); }
      SBAR();
    });
    STAMP(PH + 3);
    if (MODE == 1) BARRIER_LDS();
    if (MODE == 0) {
      const float csv = ((const LAS float*)(c.lds + LD_PAR))[64 + c.r];
      unsigned p[8];
#pragma unroll
      for (int g = 0; g < 4; ++g) {
        const f32x4 s0 = *(const LAS f32x4*)(st + 8 * g + 4 * c.h), s1 = *(const LAS f32x4*)(st + 8 * g + 4 * c.h + 2);
        const float v0 = (aC[4 * g] - s0[0] * csv) * s0[1], v1 = (aC[4 * g + 1] - s0[2] * csv) * s0[3];
        const float v2 = (aC[4 * g + 2] - s1[0] * csv) * s1[1], v3 = (aC[4 * g + 3] - s1[2] * csv) * s1[3];
        p[2 * g] = pk2(v0, v1); p[2 * g + 1] = pk2(v2, v3);
      }
      const unsigned vo = (c.r >> 4) * 65536 + c.h * 256 + (c.r & 15) * 16, so = lay + L_V + (2 * c.nb * 128 + 2 * tt) * 512;
      st16(c, (u32x4){p[0], p[1], p[2], p[3]}, vo, so);
      st16(c, (u32x4){p[4], p[5], p[6], p[7]}, vo, so + 512);
    } else {
      st16(c, epi_frag<MODE, 0>(aC, par + 64, mr, rstd), c.voff, soA + 4096);
      st16(c, epi_frag<MODE, 1>(aC, par + 64, mr, rstd), c.voff, soA + 5120);
    }
  } else {
    BARRIER_LDS();
    W2R(0); W2R(1); W2R(2); W2R(3);
    st16(c, epi_frag<MODE, 0>(aA, par, mrA, rsA), c.voff, soA);
    st16(c, epi_frag<MODE, 1>(aA, par, mrA, rsA), c.voff, soA + stepAB);
    st16(c, epi_frag<MODE, 0>(aB, par + 32, mr, rstd), voB, soB);
    st16(c, epi_frag<MODE, 1>(aB, par + 32, mr, rstd), voB, soB + stepAB);
  }
#undef W2R
  STAMP(PH + 6);
  if (MODE == 0) publish(c, 4 + l, 4, FL_Q + l * 16 + c.nb, c.tb);
  else publish(c, 6 + l, 4, FL_H + l * 16 + c.tb, c.nb);
}
DI void help_colsum(const Ctx& c, int mode) {
  const int t = (c.w - 4) * 64 + c.lane;
  if (t < 96) {
    float v[32];
#pragma unroll
    for (int i = 0; i < 32; ++i) v[i] = ld4(c, t * 4, WS_CSP + ((mode * 16 + c.nb) * 32 + i) * 384);
    float s = 0.f;
#pragma unroll
    for (int i = 0; i < 32; i += 4) s += (v[i] + v[i + 1]) + (v[i + 2] + v[i + 3]);
    ((LAS float*)(c.lds + LD_PAR))[mode * 96 + t] = s;
  }
}

DI void reduce_res(Ctx& c, const f32x16 (&a)[4], u32x4 (&res)[2], int PH) {
  LAS f32x4* red = (LAS f32x4*)(c.lds + LD_EX) + c.lane;
#pragma unroll
  for (int j = 1; j < 4; ++j) {
    LAS f32x4* p = red + ((c.w ^ j) * 3 + j - 1) * 256;
#pragma unroll
    for (int g = 0; g < 4; ++g) p[g * 64] = (f32x4){a[j][4 * g], a[j][4 * g + 1], a[j][4 * g + 2], a[j][4 * g + 3]};
  }
  STAMP(PH + 3);
  BARRIER_LDS();
  STAMP(PH + 4);
  const LAS f32x4* q = red + c.w * 768;
#pragma unroll
  for (int g = 0; g < 4; ++g) {
    const f32x4 s = ((f32x4){a[0][4 * g], a[0][4 * g + 1], a[0][4 * g + 2], a[0][4 * g + 3]} + q[g * 64]) + (q[256 + g * 64] + q[512 + g * 64]);
    const unsigned u0 = res[g >> 1][2 * (g & 1)], u1 = res[g >> 1][2 * (g & 1) + 1];
    res[g >> 1][2 * (g & 1)] = pk2(bf_lo(u0) + rbf(s[0]), bf_hi(u0) + rbf(s[1]));
    res[g >> 1][2 * (g & 1) + 1] = pk2(bf_lo(u1) + rbf(s[2]), bf_hi(u1) + rbf(s[3]));
  }
}
#define MM4(w0, w1, x0, x1) do { a[0] = MFMA(as_bf(w0), as_bf(x0), a[0]); a[1] = MFMA(as_bf(w1), as_bf(x0), a[1]); \
                                 a[2] = MFMA(as_bf(w0), as_bf(x1), a[2]); a[3] = MFMA(as_bf(w1), as_bf(x1), a[3]); } while (0)

DI void phase_wo(Ctx& c, int l, const u32x4 (&wo)[16], u32x4 (&res)[2]) {
  const unsigned xs = WS_L + l * L_SZ + L_O + ((2 * c.tb64 + (c.w >> 1)) * 32 + 8 * c.w) * 1024;
  const unsigned xo = WS_L + l * L_SZ + L_O + ((2 * c.tb64 + 1 - (c.w >> 1)) * 32 + 8 * c.w) * 1024;
  u32x4 xf[16];
#pragma unroll
  for (int k = 0; k < 8; ++k) { xf[2 * k] = ld16(c, c.voff, xs + k * 1024); xf[2 * k + 1] = ld16(c, c.voff, xo + k * 1024); }
  SBAR();
  f32x16 a[4];
#pragma unroll
  for (int i = 0; i < 64; ++i) a[i >> 4][i & 15] = 0.f;
#pragma unroll
  for (int k = 0; k < 8; ++k) { MM4(wo[2 * k], wo[2 * k + 1], xf[2 * k], xf[2 * k + 1]); SBAR(); }
  STAMP((3 + 5 * l) * 10 + 2);
  reduce_res(c, a, res, (3 + 5 * l) * 10);
}

DI void phase_w2(Ctx& c, int l, const u32x4 (&w2r)[8], u32x4 (&res)[2]) {
  const int k0 = c.w * 21 + (c.w < 2 ? c.w : 2);
  const unsigned xs = WS_L + l * L_SZ + L_H + ((2 * c.tb64 + (c.w >> 1)) * KS_F + k0) * 1024;
  const unsigned xo = WS_L + l * L_SZ + L_H + ((2 * c.tb64 + 1 - (c.w >> 1)) * KS_F + k0) * 1024;
  constexpr int WIN = 8;
  u32x4 xf[2 * WIN];
#pragma unroll
  for (int k = 0; k < WIN; ++k) { xf[2 * k] = ld16(c, c.voff, xs + k * 1024); xf[2 * k + 1] = ld16(c, c.voff, xo + k * 1024); }
  SBAR();
  const LAS u32x4* w0 = (const LAS u32x4*)(c.lds + LD_W + c.w * 36864 + (c.w & 1) * 18432) + c.lane;
  const LAS u32x4* w1 = (const LAS u32x4*)(c.lds + LD_W + c.w * 36864 + (1 - (c.w & 1)) * 18432) + c.lane;
  f32x16 a[4];
#pragma unroll
  for (int i = 0; i < 64; ++i) a[i >> 4][i & 15] = 0.f;
  u32x4 f0[2], f1[2];
  f0[0] = w0[0]; f1[0] = w1[0];
  static_for<0, 22>([&](auto kc) __attribute__((always_inline)) {
    constexpr int k = decltype(kc)::v;
    if constexpr (k + 1 < 18) { f0[(k + 1) & 1] = w0[(k + 1) * 64]; f1[(k + 1) & 1] = w1[(k + 1) * 64]; }
    SBAR();
    if (k < 21 || c.w < 2) {
      if constexpr (k < 18) MM4(f0[k & 1], f1[k & 1], xf[2 * (k % WIN)], xf[2 * (k % WIN) + 1]);
      else MM4(w2r[2 * (k - 18)], w2r[2 * (k - 18) + 1], xf[2 * (k % WIN)], xf[2 * (k % WIN) + 1]);
    }
    SBAR();
    if constexpr (k + WIN < 22) {
      const int kk = (k + WIN == 21 && c.w >= 2) ? 20 : k + WIN;
      xf[2 * (k % WIN)] = ld16(c, c.voff, xs + kk * 1024); xf[2 * (k % WIN) + 1] = ld16(c, c.voff, xo + kk * 1024);
    }
  });
  STAMP((5 + 5 * l) * 10 + 2);
  BARRIER_LDS();
  reduce_res(c, a, res, (5 + 5 * l) * 10);
}

#define SMF(a, b8, c) __builtin_amdgcn_smfmac_f32_32x32x32_bf16((a), __builtin_bit_cast(bf16x16, b8), (c), sidx, 0, 0)
#define LDF(p) (*(const LAS bf16x8*)(p))
DI float max16(const f32x16& s) {
  float a = fmaxf(fmaxf(s[0], s[1]), s[2]), b = fmaxf(fmaxf(s[3], s[4]), s[5]);
  a = fmaxf(fmaxf(a, s[6]), s[7]); b = fmaxf(fmaxf(b, s[8]), s[9]);
  a = fmaxf(fmaxf(a, s[10]), s[11]); b = fmaxf(fmaxf(b, s[12]), s[13]);
  a = fmaxf(fmaxf(a, s[14]), s[15]);
  return swap_half_max(fmaxf(a, b));
}
DI void attn_slow(const LAS char* kp, const LAS char* vp, bool isv, bf16x8 q, f32x16& o, float& m) {
  const int sidx = 0xDDDDDDDD;
  f32x16 zero;
#pragma unroll
  for (int i = 0; i < 16; ++i) zero[i] = 0.f;
  o = zero; m = -3.0e38f;
  for (int t = 0; t < 32; ++t) {
    f32x16 s = MFMA(LDF(kp + t * 1024), q, zero);
    const float mn = fmaxf(m, max16(s));
    const float al = __builtin_amdgcn_exp2f(m - mn);
    m = mn;
#pragma unroll
    for (int i = 0; i < 16; ++i) { o[i] *= al; s[i] = __builtin_amdgcn_exp2f(s[i] - mn); }
    const int vo = isv ? t * 1024 : 0;
    o = SMF(LDF(vp + vo), LO8(s), o);
    o = SMF(LDF(vp + vo + 512), HI8(s), o);
  }
}

template <int half>
DI void phase_attn(Ctx& c, int l, u32x4 (&wo)[16]) {
  const int qt = c.qb * 8 + (c.w & 3) * 2;
  const unsigned lay = WS_L + l * L_SZ;
  const char* Kg = c.ws + lay + L_K + c.head * 65536 + half * 32768 + (c.w & 3) * 1024;
  const char* Vg = Kg + (L_V - L_K);
  const unsigned kd = c.lbase + LD_W + half * 32768 + (c.w & 3) * 1024, vd = kd + (LD_V - LD_W);
  u32x4 qv0, qv1;
  asm volatile("s_nop 4\n\tglobal_load_dwordx4 %0, %2, %3 sc1\n\tglobal_load_dwordx4 %1, %2, %3 offset:1024 sc1"
               : "=&v"(qv0), "=&v"(qv1) : "v"(c.voff), "s"(c.ws + lay + L_Q + (c.head * 64 + qt) * 1024) : "memory");
#define KVDMA(u) do { glds16(Kg + (u) * 4096, c.voff, kd + (u) * 4096); glds16(Vg + (u) * 4096, c.voff, vd + (u) * 4096); } while (0)
  KVDMA(0); KVDMA(1);
  if (half == 0) {
    const unsigned o1 = (c.lane & 11) == 0 ? 0x3f803f80u : 0u;
    ((LAS u32x4*)(c.lds + LD_ONE))[c.w * 64 + c.lane] = (u32x4){o1, o1, o1, o1};
  }
  const int sidx = 0xDDDDDDDD;
  const bool isv = c.r < 16;
  const LAS char* kp0 = c.lds + LD_W + half * 32768 + c.lane * 16;
  const LAS char* vp0 = c.lds + (isv ? LD_V + half * 32768 : LD_ONE) + c.h * 256 + (c.r & 15) * 16;
  const int vstep = isv ? 4096 : 0;
  asm volatile("s_waitcnt vmcnt(2) lgkmcnt(0)\n\ts_barrier" : "+v"(qv0), "+v"(qv1)::"memory");
  STAMP((2 + 5 * l) * 10 + 1);
  const bf16x8 q0 = as_bf(qv0), q1 = as_bf(qv1);
  f32x16 zero;
#pragma unroll
  for (int i = 0; i < 16; ++i) zero[i] = 0.f;
  f32x16 cin0, cin1, o0 = zero, o1 = zero, sA, sB;
  float m0, m1;
  const LAS char* kp = kp0; const LAS char* vp = vp0;
  bf16x8 va0, va1, vb0, vb1, ka, kb;
  ka = LDF(kp); va0 = LDF(vp); va1 = LDF(vp + 512);
  {
    const f32x16 s = MFMA(ka, q0, zero), s1 = MFMA(ka, q1, zero);
    m0 = max16(s); m1 = max16(s1);
#pragma unroll
    for (int i = 0; i < 16; ++i) { cin0[i] = -m0; cin1[i] = -m1; sA[i] = s[i] - m0; }
  }
  PIN(cin0); PIN(cin1);
  const unsigned wos = WS_WO + ((2 * c.nb8 + (c.w & 1)) * 32 + 8 * c.w) * 1024;
  const unsigned woo = WS_WO + ((2 * c.nb8 + 1 - (c.w & 1)) * 32 + 8 * c.w) * 1024;
  unsigned wm[4] = {0u, 0u, 0u, 0u};
  const unsigned wm1 = WS_W1 + (c.nb < 11 ? 3 * c.nb * 32768 + ((c.tb & 1) * 4 + (c.w & 3)) * 12288 : (33 + 2 * (c.nb - 11)) * 32768 + ((c.tb & 1) * 4 + (c.w & 3)) * 7168);
  const unsigned wm2 = WS_W2 + 2 * c.nb8 * KS_F * 1024 + ((c.tb64 & 3) * 4 + (c.w & 3)) * 11008;
#define EXPS(s, lo, hi) do { _Pragma("unroll") for (int i = lo; i < hi; ++i) s[i] = __builtin_amdgcn_exp2f(s[i]); PIN(s); SBAR(); } while (0)
#define STEP(KOFF, VOFF, kc, kn, c0, c1, n0, n1, X0, X1)                             \
  do {                                                                               \
    kn = LDF(kp + (KOFF)); n0 = LDF(vp + (VOFF)); n1 = LDF(vp + (VOFF) + 512);       \
    SBAR();                                                                          \
    EXPS(sA, 0, 4);                                                                  \
    sB = MFMA(kc, q1, cin1); PIN(sB); SBAR();                                        \
    X0; SBAR();                                                                      \
    EXPS(sA, 4, 10);                                                                 \
    o0 = SMF(c0, LO8(sA), o0); PIN(o0); SBAR();                                      \
    EXPS(sA, 10, 16);                                                                \
    o0 = SMF(c1, HI8(sA), o0); PIN(o0); SBAR();                                      \
    EXPS(sB, 0, 4);                                                                  \
    sA = MFMA(kn, q0, cin0); PIN(sA); SBAR();                                        \
    X1; SBAR();                                                                      \
    EXPS(sB, 4, 10);                                                                 \
    o1 = SMF(c0, LO8(sB), o1); PIN(o1); SBAR();                                      \
    EXPS(sB, 10, 16);                                                                \
    o1 = SMF(c1, HI8(sB), o1); PIN(o1); SBAR();                                      \
  } while (0)
#define QUAD(A0, A1, B0, B1, C0, C1, MID, D0, D1)                                    \
  do {                                                                               \
    STEP(1024, 1024, ka, kb, va0, va1, vb0, vb1, A0, A1);                            \
    STEP(2048, 2048, kb, ka, vb0, vb1, va0, va1, B0, B1);                            \
    STEP(3072, 3072, ka, kb, va0, va1, vb0, vb1, C0, C1);                            \
    MID; vp += vstep;                                                                \
    STEP(4096, 0, kb, ka, vb0, vb1, va0, va1, D0, D1);                               \
    kp += 4096;                                                                      \
  } while (0)
#define NOP (void)0
#define WOL(k) do { if (!half) { wo[2 * (k)] = ld16(c, c.voff, wos + (k) * 1024); wo[2 * (k) + 1] = ld16(c, c.voff, woo + (k) * 1024); } \
                    else if ((k) < 4) wm[(k) & 3] = warm8k(c, (k) < 2 ? wm1 + (k) * 4096 : wm2 + ((k) - 2) * 2816); } while (0)
  QUAD(KVDMA(2), KVDMA(3), KVDMA(4), KVDMA(5), KVDMA(6), KVDMA(7), asm volatile("s_waitcnt vmcnt(12)\n\ts_barrier" ::: "memory"), NOP, NOP);
  QUAD(NOP, NOP, NOP, NOP, NOP, NOP, asm volatile("s_waitcnt vmcnt(0)\n\ts_barrier" ::: "memory"), NOP, NOP);
  STAMP((2 + 5 * l) * 10 + 5);
  QUAD(WOL(0), WOL(1), WOL(2), WOL(3), WOL(4), WOL(5), NOP, WOL(6), WOL(7));
  for (int it = 3; it < 8; ++it) QUAD(NOP, NOP, NOP, NOP, NOP, NOP, NOP, NOP, NOP);
#undef QUAD
#undef STEP
#undef KVDMA
#undef WOL
  STAMP((2 + 5 * l) * 10 + 2);
  asm volatile("" ::"v"(wm[0]), "v"(wm[1]), "v"(wm[2]), "v"(wm[3]));
  if (__any(!(o0[8] < 1.0995116e12f))) attn_slow(kp0, vp0, isv, q0, o0, m0);
  if (__any(!(o1[8] < 1.0995116e12f))) attn_slow(kp0, vp0, isv, q1, o1, m1);
  LAS float* cmb = (LAS float*)(c.lds + LD_CMB) + (c.w & 3) * 1280 + c.lane;
  if (half == 1) {
#pragma unroll
    for (int i = 0; i < 9; ++i) { cmb[i * 64] = o0[i]; cmb[640 + i * 64] = o1[i]; }
    cmb[9 * 64] = m0; cmb[640 + 9 * 64] = m1;
  }
  STAMP((2 + 5 * l) * 10 + 3);
  BARRIER_LDS();
  STAMP((2 + 5 * l) * 10 + 4);
  if (half == 0) {
#pragma unroll
    for (int u = 0; u < 2; ++u) {
      const f32x16& o = u ? o1 : o0; const float m = u ? m1 : m0; const LAS float* cm = cmb + u * 640;
      const float m2 = cm[9 * 64];
      const float mm = fmaxf(m, m2);
      const float f1 = __builtin_amdgcn_exp2f(m - mm), f2 = __builtin_amdgcn_exp2f(m2 - mm);
      const float inv = __builtin_amdgcn_rcpf(o[8] * f1 + cm[8 * 64] * f2);
      const float g1 = f1 * inv, g2 = f2 * inv;
      unsigned p[4];
#pragma unroll
      for (int i = 0; i < 4; ++i) p[i] = pk2(o[2 * i] * g1 + cm[2 * i * 64] * g2, o[2 * i + 1] * g1 + cm[(2 * i + 1) * 64] * g2);
      st16(c, (u32x4){p[0], p[1], p[2], p[3]}, c.voff, lay + L_O + ((qt + u) * 32 + c.head) * 1024);
    }
    STAMP((2 + 5 * l) * 10 + 6);
    asm volatile("s_waitcnt vmcnt(0)" ::: "memory");
    if (c.lane == 0) __hip_atomic_store(flag_ptr(c, FL_O + l * 32 + c.qb * 4 + c.w, c.head), c.token, RLX_AGENT);
  } else {
    const int t0 = c.nb < 11 ? 3 * c.nb : 33 + 2 * (c.nb - 11);
    const int np = c.nb < 11 ? 12 : 8;
    for (int u = 0; u < np; ++u) {
      const int p = (c.w & 3) + 4 * u;
      glds16x2(c.ws + WS_W1 + t0 * 32768 + p * 2048, c.voff, c.lbase + LD_W + p * 2048);
    }
  }
}

struct In { const unsigned short *x, *wqkv, *wo, *w1, *b1, *w2; unsigned short* out; };
DI u64 uniform64(u64 v) {
  return ((u64)__builtin_amdgcn_readfirstlane((unsigned)(v >> 32)) << 32) | (unsigned)__builtin_amdgcn_readfirstlane((unsigned)v);
}

DI void run_main(Ctx& c, const In& in) {
  const u64 epoch = __hip_atomic_load((gu64*)c.ws, RLX_AGENT);
  u32x4 res[2];
  {
    if (c.w == 0 && c.lane < 16) ((LAS unsigned*)(c.lds + LD_CTR))[c.lane] = 0u;
    const unsigned short* src = in.x + (size_t)((2 * c.tb64 + (c.w >> 1)) * 32 + c.r) * D_ + 32 * (2 * c.nb8 + (c.w & 1)) + 4 * c.h;
    u32x2 xr[4];
#pragma unroll
    for (int i = 0; i < 4; ++i) xr[i] = *(const u32x2*)(src + 8 * i);
    BARRIER_LDS();
    c.token = uniform64(epoch) + 1;
    res[0] = (u32x4){xr[0][0], xr[0][1], xr[1][0], xr[1][1]}; res[1] = (u32x4){xr[2][0], xr[2][1], xr[3][0], xr[3][1]};
    store_x(c, 0, res);
    publish(c, 0, 4, FL_X + c.tb, c.nb);
    STAMP(1);
    poll(c, (c.lane & 16) ? FL_WQ + c.nb : FL_X + c.tb, c.lane & 15);
    BARRIER_LDS();
    STAMP(2);
    BARRIER_LDS();
  }
  for (int l = 0; l < 2; ++l) {
    u32x4 wo[16], w2r[8];
    STAMP((1 + 5 * l) * 10 + 1);
    fresh(c);
    phase_ln<0>(c, l, 2 * l, w2r);
    fresh(c);
    STAMP((1 + 5 * l) * 10 + 9);
    {
      int line = FL_Q + l * 16 + (c.head >> 1), slot = c.lane & 15;
      if (l == 0 && c.lane >= 16) { if (c.lane < 32) line = FL_W1 + c.nb; else { line = FL_W2 + c.nb8; slot = c.lane & 31; } }
      poll(c, line, slot);
    }
    BARRIER_LDS();
    STAMP((2 + 5 * l) * 10);
    phase_attn<0>(c, l, wo);
    fresh(c);
    STAMP((2 + 5 * l) * 10 + 9);
    poll(c, FL_O + l * 32 + c.tb64, c.lane & 31);
    BARRIER_LDS();
    STAMP((3 + 5 * l) * 10 + 1);
    phase_wo(c, l, wo, res);
    store_x(c, 2 * l + 1, res);
    STAMP((3 + 5 * l) * 10 + 6);
    publish(c, 8 + l, 4, FL_X + (2 * l + 1) * 16 + c.tb, c.nb);
    fresh(c);
    STAMP((3 + 5 * l) * 10 + 9);
    poll(c, FL_X + (2 * l + 1) * 16 + c.tb, c.lane & 15);
    BARRIER_LDS();
    STAMP((4 + 5 * l) * 10 + 1);
    phase_ln<1>(c, l, 2 * l + 1, w2r);
    fresh(c);
    STAMP((4 + 5 * l) * 10 + 9);
    poll(c, FL_H + l * 16 + c.tb, c.lane & 15);
    STAMP((4 + 5 * l) * 10 + 8);
    BARRIER_LDS();
    STAMP((5 + 5 * l) * 10 + 1);
    phase_w2(c, l, w2r, res);
    fresh(c);
    if (l == 0) {
      store_x(c, 2, res);
      STAMP((5 + 5 * l) * 10 + 6);
      publish(c, 10, 4, FL_X + 2 * 16 + c.tb, c.nb);
      STAMP((5 + 5 * l) * 10 + 9);
      poll(c, FL_X + 2 * 16 + c.tb, c.lane & 15);
      STAMP((5 + 5 * l) * 10 + 8);
      BARRIER_LDS();
    }
  }
  unsigned short* op = in.out + (size_t)((2 * c.tb64 + (c.w >> 1)) * 32 + c.r) * D_ + 32 * (2 * c.nb8 + (c.w & 1)) + 4 * c.h;
#pragma unroll
  for (int i = 0; i < 4; ++i) *(u32x2*)(op + 8 * i) = (u32x2){res[i >> 1][2 * (i & 1)], res[i >> 1][2 * (i & 1) + 1]};
  STAMP(109);
  if (c.bid == 0 && c.w == 0 && c.lane == 0) __hip_atomic_store((gu64*)c.ws, c.token, RLX_AGENT);
}

DI void run_help(Ctx& c, const In& in) {
  const u64 epoch = __hip_atomic_load((gu64*)c.ws, RLX_AGENT);
  {
    const int t = (c.w & 3) * 64 + c.lane;
    const int bcol = 32 * (c.nb < 11 ? 3 * c.nb : 33 + 2 * (c.nb - 11)) + t;
    const unsigned bv = in.b1[bcol < F_ ? bcol : 0];
    unsigned e0[8], e1[8];
    const int qk = 2 * c.tb + (c.w & 1), qa = (c.w >> 1) & 1;
    wfrag_load(in.wqkv, 3 * D_, qa * 16 + c.nb, qk, c.r, c.h, e0);
    wfrag_load(in.wqkv, 3 * D_, 32 + c.nb, qk, c.r, c.h, e1);
    BARRIER_LDS();
    c.token = uniform64(epoch) + 1;
    if (t < 96) ((LAS float*)(c.lds + LD_PAR))[192 + t] = __uint_as_float(bv << 16);
    wfrag_store(c, e0, WS_WQKV + ((qa * 16 + c.nb) * 32 + qk) * 1024, true, WS_CSP + (c.nb * 32 + qk) * 384 + qa * 128);
    if (qa == 0) wfrag_store(c, e1, WS_WQKV + ((32 + c.nb) * 32 + qk) * 1024, true, WS_CSP + (c.nb * 32 + qk) * 384 + 256);
    publish(c, 1, 4, FL_WQ + c.nb, c.tb);
    STAMP(1);
    BARRIER_LDS();
    STAMP(2);
    dma_wqkv(c);
    BARRIER_ALL();
    STAMP(11);
    fresh(c);
    help_colsum(c, 0);
    BARRIER_LDS();
    const int t0 = c.nb < 11 ? 3 * c.nb : 33 + 2 * (c.nb - 11), nt1 = c.nb < 11 ? 3 : 2;
    unsigned e[4][8];
    unsigned dst[4], csd[4]; bool ok[4];
#pragma unroll
    for (int u = 0; u < 4; ++u) {
      const int j = (c.w & 3) + 4 * u;
      const unsigned short* W; int N, nt, ks;
      if (j < 6) { W = in.w1; N = F_; ok[u] = (j >> 1) < nt1; nt = t0 + (ok[u] ? j >> 1 : 0); ks = 2 * c.tb + (j & 1); dst[u] = WS_W1 + (nt * 32 + ks) * 1024; }
      else if (j < 8) { W = in.wo; N = D_; ok[u] = true; nt = 2 * c.nb8 + (j & 1); ks = c.tb64; dst[u] = WS_WO + (nt * 32 + ks) * 1024; }
      else { W = in.w2; N = D_; ks = c.tb64 + 32 * ((j - 8) >> 1); ok[u] = ks < KS_F; if (!ok[u]) ks = 0; nt = 2 * c.nb8 + (j & 1); dst[u] = WS_W2 + (nt * KS_F + ks) * 1024; }
      csd[u] = WS_CSP + ((16 + c.nb) * 32 + ks) * 384 + (j >> 1) * 128;
      wfrag_load(W, N, nt, ks, c.r, c.h, e[u]);
    }
#pragma unroll
    for (int u = 0; u < 4; ++u)
      if (ok[u]) wfrag_store(c, e[u], dst[u], (c.w & 3) + 4 * u < 6, csd[u]);
    asm volatile("s_waitcnt vmcnt(0)" ::: "memory");
    if (c.lane == 0) {
      const unsigned old = __hip_atomic_fetch_add((LAS unsigned*)(c.lds + LD_CTR) + 2, 1u, __ATOMIC_RELAXED, __HIP_MEMORY_SCOPE_WORKGROUP);
      if (old == 3) {
        __hip_atomic_store(flag_ptr(c, FL_W1 + c.nb, c.tb), c.token, RLX_AGENT);
        __hip_atomic_store(flag_ptr(c, FL_W2 + c.nb8, c.tb64), c.token, RLX_AGENT);
      }
    }
    STAMP(19);
  }
  for (int l = 0; l < 2; ++l) {
    u32x4 wo[16];
    BARRIER_LDS();
    STAMP((2 + 5 * l) * 10);
    fresh(c);
    phase_attn<1>(c, l, wo);
    fresh(c);
    STAMP((2 + 5 * l) * 10 + 9);
    BARRIER_LDS();
    BARRIER_LDS();
    BARRIER_ALL();
    STAMP((4 + 5 * l) * 10 + 1);
    if (l == 0) help_colsum(c, 1);
    dma_w2<48, 72>(c);
    if (l == 0) {
      BARRIER_LDS();
      const unsigned wq = WS_WQKV + c.nb * 32768 + (c.tb & 1) * 16384 + (c.w & 1) * 8192;
      const unsigned w0 = warm8k(c, wq + ((c.w >> 1) & 1) * (16 * 32768)), w1 = warm8k(c, wq + 32 * 32768);
      asm volatile("" ::"v"(w0), "v"(w1));
    }
    STAMP((4 + 5 * l) * 10 + 2);
    BARRIER_LDS();
    STAMP((4 + 5 * l) * 10 + 3);
    dma_w2<0, 48>(c);
    STAMP((4 + 5 * l) * 10 + 9);
    BARRIER_ALL();
    STAMP((5 + 5 * l) * 10 + 1);
    BARRIER_LDS();
    BARRIER_LDS();
    if (l == 0) {
      fresh(c);
      dma_wqkv(c);
      STAMP((5 + 5 * l) * 10 + 9);
      BARRIER_ALL();
    }
  }
}

__global__ __launch_bounds__(512, 2) void mega(const unsigned short* __restrict__ x, const unsigned short* __restrict__ wqkv,
                                               const unsigned short* __restrict__ wo_g, const unsigned short* __restrict__ w1,
                                               const unsigned short* __restrict__ b1, const unsigned short* __restrict__ w2,
                                               char* __restrict__ ws, unsigned short* __restrict__ out) {
  extern __shared__ __attribute__((aligned(16))) char lds_[];
  Ctx c;
  c.ws = ws; c.lds = (LAS char*)lds_; c.rs = __builtin_amdgcn_make_buffer_rsrc(ws, 0, WS_END, 0x00020000);
  c.lbase = (unsigned)(size_t)c.lds;
  c.bid = blockIdx.x; c.lane = threadIdx.x & 63; c.w = __builtin_amdgcn_readfirstlane(threadIdx.x >> 6); c.dead = false;
  fresh(c);
  STAMP(0);
  const In in = {x, wqkv, wo_g, w1, b1, w2, out};
  if (c.w < 4) run_main(c, in); else run_help(c, in);
}

extern "C" void kernel_launch(void* const* d_in, const int* in_sizes, int n_in, void* d_out, int out_size, void* d_ws,
                              size_t ws_size, hipStream_t stream) {
  (void)hipFuncSetAttribute((const void*)mega, hipFuncAttributeMaxDynamicSharedMemorySize, LDS_BYTES);
  mega<<<256, 512, LDS_BYTES, stream>>>((const unsigned short*)d_in[0], (const unsigned short*)d_in[1], (const unsigned short*)d_in[2],
                                        (const unsigned short*)d_in[4], (const unsigned short*)d_in[5], (const unsigned short*)d_in[6],
                                        (char*)d_ws, (unsigned short*)d_out);
}
```

```cpp
#include <hip/hip_runtime.h>
#include <hip/hip_bf16.h>

typedef __attribute__((ext_vector_type(2))) __bf16 bf16x2;
typedef __attribute__((ext_vector_type(8))) __bf16 bf16x8;
typedef __attribute__((ext_vector_type(16))) __bf16 bf16x16;
typedef __attribute__((ext_vector_type(16))) float f32x16;
typedef __attribute__((ext_vector_type(8))) float f32x8;
typedef __attribute__((ext_vector_type(4))) float f32x4;
typedef __attribute__((ext_vector_type(2))) float f32x2;
typedef __attribute__((ext_vector_type(4))) unsigned u32x4;
typedef __attribute__((ext_vector_type(2))) unsigned u32x2;
typedef unsigned long long u64;
#define LAS __attribute__((address_space(3)))
#define GAS __attribute__((address_space(1)))
typedef GAS u64 gu64;
#define DI __device__ __forceinline__
#define RLX_AGENT __ATOMIC_RELAXED, __HIP_MEMORY_SCOPE_AGENT

constexpr int S_ = 2048, D_ = 512, F_ = 1376;
constexpr int KS_F = F_ / 16;

constexpr unsigned MB = 1u << 20;
constexpr unsigned WS_FLAG = 0;
constexpr unsigned WS_WQKV = 128 * 1024;
constexpr unsigned WS_WO = WS_WQKV + 48 * 32 * 1024;
constexpr unsigned WS_W1 = WS_WO + 16 * 32 * 1024;
constexpr unsigned WS_W2 = WS_W1 + 43 * 32 * 1024;
constexpr unsigned WS_CSP = WS_W2 + 16 * 86 * 1024;
constexpr unsigned WS_ST = WS_CSP + 2 * 16 * 32 * 384;
constexpr unsigned WS_X = WS_ST + 4 * 64 * 4096;
constexpr unsigned WS_L = WS_X + 8 * MB;
constexpr unsigned L_Q = 0, L_K = 2 * MB, L_V = 4 * MB, L_O = 6 * MB, L_H = 8 * MB, L_SZ = 14 * MB;
constexpr unsigned WS_END = WS_L + 2 * L_SZ;
constexpr int FL_X = 1;
constexpr int FL_H = FL_X + 64;
constexpr int FL_Q = FL_H + 32;
constexpr int FL_O = FL_Q + 32;
constexpr int FL_WQ = FL_O + 64;
constexpr int FL_W1 = FL_WQ + 16;
constexpr int FL_W2 = FL_W1 + 16;
static_assert((FL_W2 + 8) * 256 <= 128 * 1024, "flag region");

constexpr int LD_W = 0;
constexpr int LD_V = 65536;
constexpr int LD_EX = 98304;
constexpr int LD_ONE = 131072;
constexpr int LD_CMB = 135168;
constexpr int LD_W2E = 122880;
constexpr int LD_SC = 151552;
constexpr int LD_ST = 155648;
constexpr int LD_PAR = 157696;
constexpr int LD_CTR = LD_PAR + 1280;
constexpr int LD_DBG = LD_CTR + 256;
constexpr int LDS_BYTES = 163840;

#define MFMA(a, b, c) __builtin_amdgcn_mfma_f32_32x32x16_bf16((a), (b), (c), 0, 0, 0)
#define SBAR() __builtin_amdgcn_sched_barrier(0)
#define PIN(x) asm volatile("" : "+v"(x))
#define BARRIER_LDS() asm volatile("s_waitcnt lgkmcnt(0)\n\ts_barrier" ::: "memory")
#define BARRIER_ALL() asm volatile("s_waitcnt vmcnt(0) lgkmcnt(0)\n\ts_barrier" ::: "memory")

#define STAMP(i)

DI unsigned pk2(float lo, float hi) {
  bf16x2 v; v[0] = (__bf16)lo; v[1] = (__bf16)hi;
  return __builtin_bit_cast(unsigned, v);
}
DI float bf_lo(unsigned u) { return __uint_as_float(u << 16); }
DI float bf_hi(unsigned u) { return __uint_as_float(u & 0xffff0000u); }
DI float rbf(float x) { return (float)(__bf16)x; }
DI bf16x8 as_bf(u32x4 v) { return __builtin_bit_cast(bf16x8, v); }
DI float swap_half_sum(float x) {
  auto r = __builtin_amdgcn_permlane32_swap(__float_as_uint(x), __float_as_uint(x), false, false);
  return __uint_as_float(r[0]) + __uint_as_float(r[1]);
}
DI float swap_half_max(float x) {
  auto r = __builtin_amdgcn_permlane32_swap(__float_as_uint(x), __float_as_uint(x), false, false);
  return fmaxf(__uint_as_float(r[0]), __uint_as_float(r[1]));
}
DI void glds16(const char* sbase, unsigned voff, unsigned lds_base) {
  unsigned sv;
  asm volatile("s_mov_b32 %0, m0\n\ts_mov_b32 m0, %3\n\ts_nop 2\n\tglobal_load_lds_dwordx4 %1, %2 sc1\n\ts_mov_b32 m0, %0"
               : "=&s"(sv) : "v"(voff), "s"(sbase), "s"(lds_base) : "memory");
}
DI void glds16x2(const char* sbase, unsigned voff, unsigned lds_base) {
  unsigned sv;
  asm volatile("s_mov_b32 %0, m0\n\ts_mov_b32 m0, %3\n\ts_nop 2\n\tglobal_load_lds_dwordx4 %1, %2 sc1\n\t"
               "global_load_lds_dwordx4 %1, %2 offset:1024 sc1\n\ts_mov_b32 m0, %0"
               : "=&s"(sv) : "v"(voff), "s"(sbase), "s"(lds_base) : "memory");
}
DI void glds16x4(const char* sbase, unsigned voff, unsigned lds_base) {
  unsigned sv;
  asm volatile("s_mov_b32 %0, m0\n\ts_mov_b32 m0, %3\n\ts_nop 2\n\tglobal_load_lds_dwordx4 %1, %2 sc1\n\t"
               "global_load_lds_dwordx4 %1, %2 offset:1024 sc1\n\tglobal_load_lds_dwordx4 %1, %2 offset:2048 sc1\n\t"
               "global_load_lds_dwordx4 %1, %2 offset:3072 sc1\n\ts_mov_b32 m0, %0"
               : "=&s"(sv) : "v"(voff), "s"(sbase), "s"(lds_base) : "memory");
}
template <int I> struct IC { static constexpr int v = I; };
template <int I, int N, class F> DI void static_for(F&& f) {
  if constexpr (I < N) { f(IC<I>{}); static_for<I + 1, N>(f); }
}

struct Ctx {
  char* ws; LAS char* lds; __amdgpu_buffer_rsrc_t rs;
  u64 token; unsigned lbase, voff;
  int bid, lane, w, r, h, tb, nb, tb64, nb8, head, qb;
  bool dead;
};
DI void fresh(Ctx& c) {
  asm volatile("" : "+s"(c.bid));
  asm volatile("" : "+s"(c.w));
  asm volatile("" : "+v"(c.lane));
  const int g = c.bid & 7, idx = c.bid >> 3;
  c.r = c.lane & 31; c.h = c.lane >> 5; c.voff = c.lane * 16;
  c.tb = 2 * g + (idx >> 4); c.nb = idx & 15; c.tb64 = 4 * g + (idx >> 3); c.nb8 = idx & 7;
  c.head = 4 * g + (idx & 3); c.qb = idx >> 2;
}
DI u32x4 ld16(const Ctx& c, unsigned voff, unsigned soff) { return __builtin_amdgcn_raw_buffer_load_b128(c.rs, voff, soff, 16); }
DI float ld4(const Ctx& c, unsigned voff, unsigned soff) { return __uint_as_float(__builtin_amdgcn_raw_buffer_load_b32(c.rs, voff, soff, 16)); }
DI void st16(const Ctx& c, u32x4 v, unsigned voff, unsigned soff) { __builtin_amdgcn_raw_buffer_store_b128(v, c.rs, voff + soff, 0, 16); }
DI void st4(const Ctx& c, float v, unsigned voff, unsigned soff) { __builtin_amdgcn_raw_buffer_store_b32(__float_as_uint(v), c.rs, voff, soff, 16); }
DI gu64* flag_ptr(const Ctx& c, int line, int slot) { return (gu64*)(c.ws + WS_FLAG) + line * 32 + slot; }

DI void publish(const Ctx& c, int ctr, unsigned n, int line, int slot) {
  asm volatile("s_waitcnt vmcnt(0)" ::: "memory");
  if (c.lane == 0) {
    const unsigned old = __hip_atomic_fetch_add((LAS unsigned*)(c.lds + LD_CTR) + ctr, 1u, __ATOMIC_RELAXED, __HIP_MEMORY_SCOPE_WORKGROUP);
    if (old == n - 1) __hip_atomic_store(flag_ptr(c, line, slot), c.token, RLX_AGENT);
  }
}
DI void poll(Ctx& c, int line, int slot) {
  if (c.w == 0 && !c.dead) {
    gu64* p = flag_ptr(c, line, slot);
    unsigned spins = 0;
    while (!__all(__hip_atomic_load(p, RLX_AGENT) == c.token)) {
      if (++spins > (1u << 18)) { c.dead = true; break; }
    }
    __builtin_amdgcn_fence(__ATOMIC_ACQUIRE, "wavefront");
  }
}
template <int NQ> DI void help_dma(const Ctx& c, unsigned src, int dst) {
#pragma unroll
  for (int u = 0; u < NQ / 4; ++u) {
    const int p = (c.w & 3) + 4 * u;
    glds16x4(c.ws + src + p * 4096, c.voff, c.lbase + dst + p * 4096);
  }
}
DI void dma_wqkv(const Ctx& c) {
  help_dma<8>(c, WS_WQKV + c.nb * 32768, LD_W);
  help_dma<8>(c, WS_WQKV + (16 + c.nb) * 32768, LD_W + 32768);
  help_dma<8>(c, WS_WQKV + (32 + c.nb) * 32768, LD_W + 65536);
}
DI int w2_run(int run) { return run < 6 ? LD_W + run * 14336 : LD_W2E + (run - 6) * 14336; }
template <int P0, int P1> DI void dma_w2(const Ctx& c) {
#pragma unroll
  for (int u = 0; u < (P1 - P0 + 3) / 4; ++u) {
    const int p = P0 + (c.w & 3) + 4 * u;
    if (p < P1) {
      const int run = p / 7, kq = run >> 1, kk = 2 * (p % 7);
      glds16x2(c.ws + WS_W2 + ((2 * c.nb8 + (run & 1)) * KS_F + kq * 21 + (kq < 2 ? kq : 2) + kk) * 1024, c.voff,
               c.lbase + w2_run(run) + kk * 1024);
    }
  }
}
DI unsigned warm8k(const Ctx& c, unsigned off) { return __builtin_amdgcn_raw_buffer_load_b32(c.rs, c.lane * 128, off, 0); }

DI void store_x(const Ctx& c, int inst, const u32x4 (&res)[2]) {
  const int tt = 2 * c.tb64 + (c.w >> 1), nt = 2 * c.nb8 + (c.w & 1);
  st16(c, res[0], c.voff, WS_X + inst * (2 * MB) + (tt * 32 + 2 * nt) * 1024);
  st16(c, res[1], c.voff, WS_X + inst * (2 * MB) + (tt * 32 + 2 * nt + 1) * 1024);
  float s1 = 0.f, s2 = 0.f;
#pragma unroll
  for (int i = 0; i < 8; ++i) {
    const unsigned u = res[i >> 2][i & 3];
    const float a = bf_lo(u), b = bf_hi(u);
    s1 += a + b; s2 = fmaf(a, a, fmaf(b, b, s2));
  }
  auto r1 = __builtin_amdgcn_permlane32_swap(__float_as_uint(s1), __float_as_uint(s2), false, false);
  st4(c, __uint_as_float(r1[0]) + __uint_as_float(r1[1]), c.lane * 4, WS_ST + inst * (64 * 4096) + tt * 4096 + nt * 256);
}

DI void wfrag_load(const unsigned short* W, int N, int nt, int ks, int r, int h, unsigned (&e)[8]) {
#pragma unroll
  for (int j = 0; j < 8; ++j) e[j] = W[(size_t)(16 * ks + 8 * (j >> 2) + 4 * h + (j & 3)) * N + 32 * nt + r];
}
DI void wfrag_store(const Ctx& c, const unsigned (&e)[8], unsigned dst, bool docs, unsigned csdst) {
  u32x4 v;
#pragma unroll
  for (int j = 0; j < 4; ++j) v[j] = e[2 * j] | (e[2 * j + 1] << 16);
  st16(c, v, c.voff, dst);
  if (docs) {
    float s = 0.f;
#pragma unroll
    for (int j = 0; j < 8; ++j) s += __uint_as_float(e[j] << 16);
    s = swap_half_sum(s);
    if (c.h == 0) st4(c, s, c.r * 4, csdst);
  }
}

DI float mish_f(float hh) {
  const float u = __builtin_amdgcn_exp2f(fminf(hh, 30.f) * 1.4426950408889634f);
  const float n = u * (u + 2.f);
  return hh * n * __builtin_amdgcn_rcpf(n + 2.f);
}
template <int MODE>
DI u32x2 epi4(float a0, float a1, float a2, float a3, const LAS float* par, float mr, float rstd) {
  const float a[4] = {a0, a1, a2, a3};
  float v[4];
  const f32x4 cs = *(const LAS f32x4*)par;
  f32x4 bb = cs;
  if (MODE == 1) bb = *(const LAS f32x4*)(par + 96);
#pragma unroll
  for (int j = 0; j < 4; ++j) {
    if (MODE == 0) v[j] = fmaf(a[j], rstd, mr * cs[j]);
    else v[j] = mish_f(fmaf(a[j], rstd, fmaf(mr, cs[j], bb[j])));
  }
  return (u32x2){pk2(v[0], v[1]), pk2(v[2], v[3])};
}
template <int MODE, int SS>
DI u32x4 epi_frag(const f32x16& a, const LAS float* par, float mr, float rstd) {
  const u32x2 lo = epi4<MODE>(a[8 * SS], a[8 * SS + 1], a[8 * SS + 2], a[8 * SS + 3], par + 16 * SS, mr, rstd);
  const u32x2 hi = epi4<MODE>(a[8 * SS + 4], a[8 * SS + 5], a[8 * SS + 6], a[8 * SS + 7], par + 16 * SS + 8, mr, rstd);
  return (u32x4){lo[0], lo[1], hi[0], hi[1]};
}
#define LO8(s) __builtin_shufflevector(s, s, 0, 1, 2, 3, 4, 5, 6, 7)
#define HI8(s) __builtin_shufflevector(s, s, 8, 9, 10, 11, 12, 13, 14, 15)
DI f32x4 q4(const f32x8& v, int g) { return g ? (f32x4){v[4], v[5], v[6], v[7]} : (f32x4){v[0], v[1], v[2], v[3]}; }
DI f32x8 cat8(f32x4 a, f32x4 b) { return __builtin_shufflevector(a, b, 0, 1, 2, 3, 4, 5, 6, 7); }

template <int MODE, int T>
DI void ln_epilogue(Ctx& c, int l, const f32x16& a, const f32x8& ch, float mean, float rstd) {
  const int tt = 4 * c.tb + (c.w & 3);
  const LAS float* par = (const LAS float*)(c.lds + LD_PAR) + MODE * 96 + 4 * c.h;
  const unsigned lay = WS_L + l * L_SZ;
  if (MODE == 0) {
    const float rs = T ? rstd : rstd * (0.25f * 1.4426950408889634f), mr = -mean * rs;
    const unsigned vo = T ? c.h * 512 + ((c.r & 19) | ((c.r & 4) << 1) | ((c.r & 8) >> 1)) * 16 : c.voff;
    const unsigned so = lay + (T ? L_K : L_Q) + (2 * c.nb * 64 + tt) * 1024;
    st16(c, epi_frag<0, 0>(a, par + 32 * T, mr, rs), vo, so);
    st16(c, epi_frag<0, 1>(a, par + 32 * T, mr, rs), vo, so + 65536);
    const LAS f32x2* st = (const LAS f32x2*)(c.lds + LD_ST) + (c.w & 3) * 32 + 16 * T + 4 * c.h;
    const float csv = ((const LAS float*)(c.lds + LD_PAR))[64 + c.r];
    unsigned p[4];
#pragma unroll
    for (int g = 0; g < 2; ++g) {
      const f32x4 s0 = *(const LAS f32x4*)(st + 8 * g), s1 = *(const LAS f32x4*)(st + 8 * g + 2);
      const float v0 = (ch[4 * g] - s0[0] * csv) * s0[1], v1 = (ch[4 * g + 1] - s0[2] * csv) * s0[3];
      const float v2 = (ch[4 * g + 2] - s1[0] * csv) * s1[1], v3 = (ch[4 * g + 3] - s1[2] * csv) * s1[3];
      p[2 * g] = pk2(v0, v1); p[2 * g + 1] = pk2(v2, v3);
    }
    st16(c, (u32x4){p[0], p[1], p[2], p[3]}, (c.r >> 4) * 65536 + c.h * 256 + (c.r & 15) * 16, lay + L_V + (2 * c.nb * 128 + 2 * tt + T) * 512);
    publish(c, 4 + l, 8, FL_Q + l * 16 + c.nb, c.tb);
  } else {
    const float mr = -mean * rstd;
    const unsigned so = lay + L_H + (tt * KS_F + 2 * (c.nb < 11 ? 3 * c.nb : 33 + 2 * (c.nb - 11))) * 1024;
    st16(c, epi_frag<1, 0>(a, par + 32 * T, mr, rstd), c.voff, so + T * 2048);
    st16(c, epi_frag<1, 1>(a, par + 32 * T, mr, rstd), c.voff, so + T * 2048 + 1024);
    if (c.nb < 11) {
      const u32x2 lo = epi4<1>(ch[0], ch[1], ch[2], ch[3], par + 64 + 16 * T, mr, rstd);
      const u32x2 hi = epi4<1>(ch[4], ch[5], ch[6], ch[7], par + 72 + 16 * T, mr, rstd);
      st16(c, (u32x4){lo[0], lo[1], hi[0], hi[1]}, c.voff, so + 4096 + T * 1024);
    }
    publish(c, 6 + l, 8, FL_H + l * 16 + c.tb, c.nb);
  }
}

template <int MODE>
DI void phase_ln(Ctx& c, int l, int inst, u32x4 (&w2r)[16]) {
  const int PH = (1 + 5 * l + (MODE ? 3 : 0)) * 10; (void)PH;
  const int tt = 4 * c.tb + c.w;
  const bool has3 = MODE == 0 || c.nb < 11;
  const unsigned xs = WS_X + inst * (2 * MB) + tt * 32768;
  constexpr int PD = 12;
  u32x4 xf[PD], sp[4];
#pragma unroll
  for (int k = 0; k < PD; ++k) xf[k] = ld16(c, c.voff, xs + k * 1024);
  SBAR();
  const LAS u32x4* wl = (const LAS u32x4*)(c.lds + LD_W) + c.lane;
  const LAS u32x4* wl2 = wl + 4096;
  f32x16 aA, aB, aC;
#pragma unroll
  for (int i = 0; i < 16; ++i) { aA[i] = 0.f; aB[i] = 0.f; aC[i] = 0.f; }
  u32x4 fa[2], fb[2], fc[2];
  fa[0] = wl[0]; fb[0] = wl[2048]; fc[0] = wl2[0];
  float mean = 0.f, rstd = 0.f, sc4[4];
  LAS float* sc = (LAS float*)(c.lds + LD_SC) + c.w * 256;
  const int k0q = c.w * 21 + (c.w < 2 ? c.w : 2);
  const unsigned w2s = WS_W2 + ((2 * c.nb8 + (c.w & 1)) * KS_F + k0q + 14) * 1024;
  const unsigned w2o = WS_W2 + ((2 * c.nb8 + 1 - (c.w & 1)) * KS_F + k0q + 14) * 1024;
  static_for<0, 32>([&](auto kc) __attribute__((always_inline)) {
    constexpr int k = decltype(kc)::v;
    aA = MFMA(as_bf(fa[k & 1]), as_bf(xf[k % PD]), aA);
    SBAR();
    if constexpr (k + 1 < 32) fa[(k + 1) & 1] = wl[(k + 1) * 64];
    if constexpr (k >= 1 && k - 1 + PD < 32) xf[(k - 1) % PD] = ld16(c, c.voff, xs + (k - 1 + PD) * 1024);
    if constexpr (k >= 21 && k < 25) sp[k - 21] = ld16(c, c.voff, WS_ST + inst * (64 * 4096) + tt * 4096 + (k - 21) * 1024);
    SBAR();
    aB = MFMA(as_bf(fb[k & 1]), as_bf(xf[k % PD]), aB);
    SBAR();
    if constexpr (k + 1 < 32) fb[(k + 1) & 1] = wl[2048 + (k + 1) * 64];
    if constexpr (MODE == 1 && k >= 16) {
      constexpr int i = k - 16;
      const int kk = ((i >> 1) == 7 && c.w >= 2) ? 6 : (i >> 1);
      w2r[i] = ld16(c, c.voff, ((i & 1) ? w2o : w2s) + kk * 1024);
    }
    if constexpr (k == 29) {
      ((LAS f32x4*)sc)[c.lane] = (__builtin_bit_cast(f32x4, sp[0]) + __builtin_bit_cast(f32x4, sp[1])) +
                                 (__builtin_bit_cast(f32x4, sp[2]) + __builtin_bit_cast(f32x4, sp[3]));
#pragma unroll
      for (int i = 0; i < 4; ++i) sc4[i] = sc[i * 64 + c.lane];
    }
    if constexpr (k == 31) {
      const float s = (sc4[0] + sc4[1]) + (sc4[2] + sc4[3]);
      auto rr = __builtin_amdgcn_permlane32_swap(__float_as_uint(s), __float_as_uint(s), false, false);
      const float mu = __uint_as_float(rr[0]) * (1.f / D_);
      const float var = fmaxf(__uint_as_float(rr[1]) * (1.f / D_) - mu * mu, 0.f);
      mean = rbf(mu);
      rstd = __builtin_amdgcn_rcpf(rbf(__builtin_amdgcn_sqrtf(rbf(rbf(var) + 1e-5f))));
      if (c.h == 0) ((LAS f32x2*)(c.lds + LD_ST))[c.w * 32 + c.r] = (f32x2){mean, rstd};
    }
    SBAR();
    if (has3) aC = MODE == 0 ? MFMA(as_bf(xf[k % PD]), as_bf(fc[k & 1]), aC) : MFMA(as_bf(fc[k & 1]), as_bf(xf[k % PD]), aC);
    SBAR();
    if constexpr (k + 1 < 32) fc[(k + 1) & 1] = wl2[(k + 1) * 64];
    SBAR();
  });
  STAMP(PH + 2);
  {
    LAS f32x4* ex = (LAS f32x4*)(c.lds + LD_EX) + c.w * 384 + c.lane;
#pragma unroll
    for (int g = 0; g < 4; ++g) ex[g * 64] = (f32x4){aB[4 * g], aB[4 * g + 1], aB[4 * g + 2], aB[4 * g + 3]};
    ex[256] = (f32x4){aC[8], aC[9], aC[10], aC[11]}; ex[320] = (f32x4){aC[12], aC[13], aC[14], aC[15]};
  }
  BARRIER_LDS();
  STAMP(PH + 4);
  ln_epilogue<MODE, 0>(c, l, aA, LO8(aC), mean, rstd);
}
template <int MODE>
DI void help_ln(Ctx& c, int l) {
  BARRIER_LDS();
  const LAS f32x4* ex = (const LAS f32x4*)(c.lds + LD_EX) + (c.w & 3) * 384 + c.lane;
  const f32x2 ms = ((const LAS f32x2*)(c.lds + LD_ST))[(c.w & 3) * 32 + c.r];
  const f32x8 lo = cat8(ex[0], ex[64]), hi = cat8(ex[128], ex[192]);
  const f32x16 a = __builtin_shufflevector(lo, hi, 0, 1, 2, 3, 4, 5, 6, 7, 8, 9, 10, 11, 12, 13, 14, 15);
  ln_epilogue<MODE, 1>(c, l, a, cat8(ex[256], ex[320]), ms[0], ms[1]);
}
DI void help_colsum(const Ctx& c, int mode) {
  const int t = (c.w - 4) * 64 + c.lane;
  if (t < 96) {
    float v[32];
#pragma unroll
    for (int i = 0; i < 32; ++i) v[i] = ld4(c, t * 4, WS_CSP + ((mode * 16 + c.nb) * 32 + i) * 384);
    float s = 0.f;
#pragma unroll
    for (int i = 0; i < 32; i += 4) s += (v[i] + v[i + 1]) + (v[i + 2] + v[i + 3]);
    ((LAS float*)(c.lds + LD_PAR))[mode * 96 + t] = s;
  }
}

DI void reduce_res(Ctx& c, const f32x16 (&a)[4], u32x4 (&res)[2], int PH) {
  LAS f32x4* red = (LAS f32x4*)(c.lds + LD_EX) + c.lane;
#pragma unroll
  for (int j = 1; j < 4; ++j) {
    LAS f32x4* p = red + ((c.w ^ j) * 3 + j - 1) * 256;
#pragma unroll
    for (int g = 0; g < 4; ++g) p[g * 64] = (f32x4){a[j][4 * g], a[j][4 * g + 1], a[j][4 * g + 2], a[j][4 * g + 3]};
  }
  STAMP(PH + 3);
  BARRIER_LDS();
  STAMP(PH + 4);
  const LAS f32x4* q = red + c.w * 768;
#pragma unroll
  for (int g = 0; g < 4; ++g) {
    const f32x4 s = ((f32x4){a[0][4 * g], a[0][4 * g + 1], a[0][4 * g + 2], a[0][4 * g + 3]} + q[g * 64]) + (q[256 + g * 64] + q[512 + g * 64]);
    const unsigned u0 = res[g >> 1][2 * (g & 1)], u1 = res[g >> 1][2 * (g & 1) + 1];
    res[g >> 1][2 * (g & 1)] = pk2(bf_lo(u0) + rbf(s[0]), bf_hi(u0) + rbf(s[1]));
    res[g >> 1][2 * (g & 1) + 1] = pk2(bf_lo(u1) + rbf(s[2]), bf_hi(u1) + rbf(s[3]));
  }
}
#define MM4(w0, w1, x0, x1) do { a[0] = MFMA(as_bf(w0), as_bf(x0), a[0]); a[1] = MFMA(as_bf(w1), as_bf(x0), a[1]); \
                                 a[2] = MFMA(as_bf(w0), as_bf(x1), a[2]); a[3] = MFMA(as_bf(w1), as_bf(x1), a[3]); } while (0)

DI void phase_wo(Ctx& c, int l, const u32x4 (&wo)[16], u32x4 (&res)[2]) {
  const unsigned xs = WS_L + l * L_SZ + L_O + ((2 * c.tb64 + (c.w >> 1)) * 32 + 8 * c.w) * 1024;
  const unsigned xo = WS_L + l * L_SZ + L_O + ((2 * c.tb64 + 1 - (c.w >> 1)) * 32 + 8 * c.w) * 1024;
  u32x4 xf[16];
#pragma unroll
  for (int k = 0; k < 8; ++k) { xf[2 * k] = ld16(c, c.voff, xs + k * 1024); xf[2 * k + 1] = ld16(c, c.voff, xo + k * 1024); }
  SBAR();
  f32x16 a[4];
#pragma unroll
  for (int i = 0; i < 64; ++i) a[i >> 4][i & 15] = 0.f;
#pragma unroll
  for (int k = 0; k < 8; ++k) { MM4(wo[2 * k], wo[2 * k + 1], xf[2 * k], xf[2 * k + 1]); SBAR(); }
  STAMP((3 + 5 * l) * 10 + 2);
  reduce_res(c, a, res, (3 + 5 * l) * 10);
}

DI void phase_w2(Ctx& c, int l, const u32x4 (&w2r)[16], u32x4 (&res)[2]) {
  const int k0 = c.w * 21 + (c.w < 2 ? c.w : 2);
  const unsigned xs = WS_L + l * L_SZ + L_H + ((2 * c.tb64 + (c.w >> 1)) * KS_F + k0) * 1024;
  const unsigned xo = WS_L + l * L_SZ + L_H + ((2 * c.tb64 + 1 - (c.w >> 1)) * KS_F + k0) * 1024;
  constexpr int WIN = 10;
  u32x4 xf[2 * WIN];
#pragma unroll
  for (int k = 0; k < WIN; ++k) { xf[2 * k] = ld16(c, c.voff, xs + k * 1024); xf[2 * k + 1] = ld16(c, c.voff, xo + k * 1024); }
  SBAR();
  const LAS u32x4* w0 = (const LAS u32x4*)(c.lds + w2_run(2 * c.w + (c.w & 1))) + c.lane;
  const LAS u32x4* w1 = (const LAS u32x4*)(c.lds + w2_run(2 * c.w + 1 - (c.w & 1))) + c.lane;
  f32x16 a[4];
#pragma unroll
  for (int i = 0; i < 64; ++i) a[i >> 4][i & 15] = 0.f;
  u32x4 f0[2], f1[2];
  f0[0] = w0[0]; f1[0] = w1[0];
  static_for<0, 22>([&](auto kc) __attribute__((always_inline)) {
    constexpr int k = decltype(kc)::v;
    const u32x4 g0 = k < 14 ? f0[k & 1] : w2r[k < 14 ? 0 : 2 * (k - 14)], g1 = k < 14 ? f1[k & 1] : w2r[k < 14 ? 0 : 2 * (k - 14) + 1];
    const u32x4 x0 = xf[2 * (k % WIN)], x1 = xf[2 * (k % WIN) + 1];
    constexpr int kn = k - 1 + WIN;
    const int kk = (kn == 21 && c.w >= 2) ? 20 : kn;
    if (k < 21 || c.w < 2) {
      a[0] = MFMA(as_bf(g0), as_bf(x0), a[0]);
      SBAR();
      if constexpr (k >= 1 && kn < 22) xf[2 * ((k - 1) % WIN)] = ld16(c, c.voff, xs + kk * 1024);
      SBAR();
      a[1] = MFMA(as_bf(g1), as_bf(x0), a[1]);
      SBAR();
      if constexpr (k + 1 < 14) f0[(k + 1) & 1] = w0[(k + 1) * 64];
      SBAR();
      a[2] = MFMA(as_bf(g0), as_bf(x1), a[2]);
      SBAR();
      if constexpr (k >= 1 && kn < 22) xf[2 * ((k - 1) % WIN) + 1] = ld16(c, c.voff, xo + kk * 1024);
      SBAR();
      a[3] = MFMA(as_bf(g1), as_bf(x1), a[3]);
      SBAR();
      if constexpr (k + 1 < 14) f1[(k + 1) & 1] = w1[(k + 1) * 64];
      SBAR();
    }
  });
  STAMP((5 + 5 * l) * 10 + 2);
  BARRIER_LDS();
  reduce_res(c, a, res, (5 + 5 * l) * 10);
}

#define SMF(a, b8, c) __builtin_amdgcn_smfmac_f32_32x32x32_bf16((a), __builtin_bit_cast(bf16x16, b8), (c), sidx, 0, 0)
#define LDF(p) (*(const LAS bf16x8*)(p))
DI float max16(const f32x16& s) {
  float a = fmaxf(fmaxf(s[0], s[1]), s[2]), b = fmaxf(fmaxf(s[3], s[4]), s[5]);
  a = fmaxf(fmaxf(a, s[6]), s[7]); b = fmaxf(fmaxf(b, s[8]), s[9]);
  a = fmaxf(fmaxf(a, s[10]), s[11]); b = fmaxf(fmaxf(b, s[12]), s[13]);
  a = fmaxf(fmaxf(a, s[14]), s[15]);
  return swap_half_max(fmaxf(a, b));
}
DI void attn_slow(const LAS char* kp, const LAS char* vp, bool isv, bf16x8 q, f32x16& o, float& m) {
  const int sidx = 0xDDDDDDDD;
  f32x16 zero;
#pragma unroll
  for (int i = 0; i < 16; ++i) zero[i] = 0.f;
  o = zero; m = -3.0e38f;
  for (int t = 0; t < 32; ++t) {
    f32x16 s = MFMA(LDF(kp + t * 1024), q, zero);
    const float mn = fmaxf(m, max16(s));
    const float al = __builtin_amdgcn_exp2f(m - mn);
    m = mn;
#pragma unroll
    for (int i = 0; i < 16; ++i) { o[i] *= al; s[i] = __builtin_amdgcn_exp2f(s[i] - mn); }
    const int vo = isv ? t * 1024 : 0;
    o = SMF(LDF(vp + vo), LO8(s), o);
    o = SMF(LDF(vp + vo + 512), HI8(s), o);
  }
}

#define EX1(r) "v_exp_f32 v" #r ", v" #r "\n\t"
#define ASTEP(KOFF, VOFF, kc_, kn_, v0_, v1_, nv0_, pv1_)                                                                  \
  asm volatile("s_waitcnt lgkmcnt(0)\n\t" EX1(64) EX1(65)                                                                  \
               "v_smfmac_f32_32x32x32_bf16 %[o1], %[pv1], v[88:95], %[idx]\n\t"                                            \
               "ds_read_b128 %[nv0], %[vp] offset:%[vo]\n\tds_read_b128 %[pv1], %[vp] offset:%[vo1]\n\t"                   \
               EX1(66) EX1(67) EX1(68)                                                                                     \
               "v_mfma_f32_32x32x16_bf16 v[80:95], %[kc], %[q1], %[c1]\n\t"                                                \
               "ds_read_b128 %[kc], %[kp] offset:%[ko]\n\t"                                                                \
               EX1(69) EX1(70) EX1(71) EX1(72) EX1(73)                                                                     \
               "v_smfmac_f32_32x32x32_bf16 %[o0], %[v0], v[64:71], %[idx]\n\t"                                             \
               EX1(74) EX1(75) EX1(76) EX1(77) EX1(78) EX1(79) EX1(80) EX1(81)                                             \
               "v_smfmac_f32_32x32x32_bf16 %[o0], %[v1], v[72:79], %[idx]\n\t"                                             \
               EX1(82) EX1(83) EX1(84)                                                                                     \
               "v_mfma_f32_32x32x16_bf16 v[64:79], %[kn], %[q0], %[c0]\n\t"                                                \
               EX1(85) EX1(86) EX1(87) EX1(88) EX1(89)                                                                     \
               "v_smfmac_f32_32x32x32_bf16 %[o1], %[v0], v[80:87], %[idx]\n\t"                                             \
               EX1(90) EX1(91) EX1(92) EX1(93) EX1(94) "v_exp_f32 v95, v95"                                                \
               : "+{v[64:79]}"(sA), "+{v[80:95]}"(sB), [o0] "+v"(o0), [o1] "+v"(o1), [kc] "+v"(kc_), [nv0] "=&v"(nv0_),    \
                 [pv1] "+v"(pv1_)                                                                                          \
               : [kn] "v"(kn_), [v0] "v"(v0_), [v1] "v"(v1_), [q0] "v"(q0), [q1] "v"(q1), [c0] "v"(cin0), [c1] "v"(cin1),  \
                 [idx] "v"(sidx), [kp] "v"(kpa), [vp] "v"(vpa), [ko] "n"(KOFF), [vo] "n"(VOFF), [vo1] "n"((VOFF) + 512)     \
               : "memory")

template <int half>
DI void phase_attn(Ctx& c, int l, u32x4 (&wo)[16]) {
  const int qt = c.qb * 8 + (c.w & 3) * 2;
  const unsigned lay = WS_L + l * L_SZ;
  const char* kvg = c.ws + lay + ((c.w & 1) ? L_V : L_K) + c.head * 65536 + ((c.w >> 1) & 1) * 32768 + half * 4096;
  const unsigned kvd = c.lbase + ((c.w & 1) ? LD_V : LD_W) + ((c.w >> 1) & 1) * 32768 + half * 4096;
  u32x4 qv0, qv1;
  asm volatile("s_nop 4\n\tglobal_load_dwordx4 %0, %2, %3 sc1\n\tglobal_load_dwordx4 %1, %2, %3 offset:1024 sc1"
               : "=&v"(qv0), "=&v"(qv1) : "v"(c.voff), "s"(c.ws + lay + L_Q + (c.head * 64 + qt) * 1024) : "memory");
#define KV4(u) glds16x4(kvg + (u) * 8192, c.voff, kvd + (u) * 8192)
  glds16x4(kvg, c.voff, kvd);
  if (half == 0) {
    const unsigned o1 = (c.lane & 11) == 0 ? 0x3f803f80u : 0u;
    ((LAS u32x4*)(c.lds + LD_ONE))[c.w * 64 + c.lane] = (u32x4){o1, o1, o1, o1};
  }
  int sidx = 0xDDDDDDDD;
  const bool isv = c.r < 16;
  const LAS char* kp0 = c.lds + LD_W + half * 32768 + c.lane * 16;
  const LAS char* vp0 = c.lds + (isv ? LD_V + half * 32768 : LD_ONE) + c.h * 256 + (c.r & 15) * 16;
  const int vstep = isv ? 4096 : 0;
  if (half == 0) asm volatile("s_waitcnt vmcnt(0) lgkmcnt(0)\n\ts_barrier" : "+v"(qv0), "+v"(qv1)::"memory");
  else asm volatile("s_waitcnt vmcnt(4)\n\ts_barrier" : "+v"(qv0), "+v"(qv1)::"memory");
  STAMP((2 + 5 * l) * 10 + 1);
  const bf16x8 q0 = as_bf(qv0), q1 = as_bf(qv1);
  f32x16 zero;
#pragma unroll
  for (int i = 0; i < 16; ++i) zero[i] = 0.f;
  f32x16 cin0, cin1, o0 = zero, o1 = zero, sA, sB = zero;
  float m0, m1;
  bf16x8 ka = LDF(kp0), kb = LDF(kp0 + 1024), va0 = LDF(vp0), va1 = LDF(vp0 + 512), vb0, vb1 = as_bf((u32x4){0u, 0u, 0u, 0u});
  {
    const f32x16 s = MFMA(ka, q0, zero), s1 = MFMA(ka, q1, zero);
    m0 = max16(s); m1 = max16(s1);
#pragma unroll
    for (int i = 0; i < 16; ++i) { cin0[i] = -m0; cin1[i] = -m1; sA[i] = s[i] - m0; }
  }
  unsigned kpa = (unsigned)(size_t)kp0, vpa = (unsigned)(size_t)vp0;
  asm volatile("s_nop 1" : "+v"(cin0), "+v"(cin1), "+v"(sA), "+v"(sB), "+v"(o0), "+v"(o1), "+v"(sidx), "+v"(vb1));
  const unsigned wos = WS_WO + ((2 * c.nb8 + (c.w & 1)) * 32 + 8 * c.w) * 1024;
  const unsigned woo = WS_WO + ((2 * c.nb8 + 1 - (c.w & 1)) * 32 + 8 * c.w) * 1024;
  unsigned wm[4] = {0u, 0u, 0u, 0u};
  const unsigned wm1 = WS_W1 + (c.nb < 11 ? 3 * c.nb * 32768 + ((c.tb & 1) * 4 + (c.w & 3)) * 12288 : (33 + 2 * (c.nb - 11)) * 32768 + ((c.tb & 1) * 4 + (c.w & 3)) * 7168);
  const unsigned wm2 = WS_W2 + 2 * c.nb8 * KS_F * 1024 + ((c.tb64 & 3) * 4 + (c.w & 3)) * 11008;
#define QUAD(A, B, MID, C, D)                                                        \
  do {                                                                               \
    ASTEP(2048, 1024, ka, kb, va0, va1, vb0, vb1); A;                                \
    ASTEP(3072, 2048, kb, ka, vb0, vb1, va0, va1); B; MID;                           \
    ASTEP(4096, 3072, ka, kb, va0, va1, vb0, vb1); C;                                \
    vpa += vstep;                                                                    \
    ASTEP(5120, 0, kb, ka, vb0, vb1, va0, va1); D;                                   \
    kpa += 4096; QSTAMP();                                                           \
  } while (0)
#define NOP (void)0
#define QSTAMP()
#define WOL(k) do { if (half == 0) wo[k] = ld16(c, c.voff, (((k) & 1) ? woo : wos) + ((k) >> 1) * 1024);                    \
                    else if (((k) & 3) == 0) wm[(k) >> 2] = warm8k(c, (k) < 8 ? wm1 + ((k) >> 2) * 4096 : wm2 + (((k) >> 2) - 2) * 2816); \
                    SBAR(); } while (0)
#define WBAR(n) asm volatile("s_waitcnt vmcnt(" #n ")\n\ts_barrier" ::: "memory")
#define PREPCHK() do { if (half == 0 && l == 0) poll(c, c.lane < 32 ? FL_W1 + c.nb : FL_W2 + c.nb8, c.lane < 32 ? (c.lane & 15) : (c.lane & 31)); } while (0)
  KV4(1);
  QUAD(KV4(2), NOP, WBAR(8), KV4(3), NOP);
  QUAD(NOP, NOP, WBAR(0), NOP, NOP);
  QUAD(NOP, NOP, NOP, NOP, NOP);
  QUAD(NOP, PREPCHK(), if (l == 0) WBAR(0), WOL(0); WOL(1), WOL(2); WOL(3));
  STAMP((2 + 5 * l) * 10 + 5);
  QUAD(WOL(4), WOL(5), NOP, WOL(6), WOL(7));
  QUAD(WOL(8), WOL(9), NOP, WOL(10), WOL(11));
  QUAD(WOL(12), WOL(13), NOP, WOL(14), WOL(15));
  QUAD(NOP, NOP, NOP, NOP, NOP);
#undef WBAR
#undef PREPCHK
  asm volatile("s_nop 1\n\tv_smfmac_f32_32x32x32_bf16 %[o1], %[pv1], v[88:95], %[idx]\n\ts_waitcnt lgkmcnt(0)\n\ts_nop 7\n\ts_nop 7"
               : [o1] "+v"(o1), "+v"(o0), "+v"(ka), "+v"(kb), "+v"(va0), "+v"(va1) : [pv1] "v"(vb1), "{v[80:95]}"(sB), [idx] "v"(sidx));
#undef QUAD
#undef KV4
#undef WOL
  STAMP((2 + 5 * l) * 10 + 2);
  asm volatile("" ::"v"(wm[0]), "v"(wm[1]), "v"(wm[2]), "v"(wm[3]));
  if (__any(!(o0[8] < 1.0995116e12f))) attn_slow(kp0, vp0, isv, q0, o0, m0);
  if (__any(!(o1[8] < 1.0995116e12f))) attn_slow(kp0, vp0, isv, q1, o1, m1);
  LAS f32x4* cmb = (LAS f32x4*)(c.lds + LD_CMB) + (c.w & 3) * 320 + c.lane;
  if (half == 1) {
    cmb[0] = (f32x4){o0[0], o0[1], o0[2], o0[3]}; cmb[64] = (f32x4){o0[4], o0[5], o0[6], o0[7]};
    cmb[128] = (f32x4){o1[0], o1[1], o1[2], o1[3]}; cmb[192] = (f32x4){o1[4], o1[5], o1[6], o1[7]};
    cmb[256] = (f32x4){o0[8], m0, o1[8], m1};
  }
  STAMP((2 + 5 * l) * 10 + 3);
  BARRIER_LDS();
  STAMP((2 + 5 * l) * 10 + 4);
  if (half == 0) {
    const f32x4 lm = cmb[256];
#pragma unroll
    for (int u = 0; u < 2; ++u) {
      const f32x16& o = u ? o1 : o0; const float m = u ? m1 : m0;
      const f32x8 p2 = cat8(cmb[128 * u], cmb[128 * u + 64]);
      const float m2 = lm[2 * u + 1];
      const float mm = fmaxf(m, m2);
      const float f1 = __builtin_amdgcn_exp2f(m - mm), f2 = __builtin_amdgcn_exp2f(m2 - mm);
      const float inv = __builtin_amdgcn_rcpf(o[8] * f1 + lm[2 * u] * f2);
      const float g1 = f1 * inv, g2 = f2 * inv;
      unsigned p[4];
#pragma unroll
      for (int i = 0; i < 4; ++i) p[i] = pk2(o[2 * i] * g1 + p2[2 * i] * g2, o[2 * i + 1] * g1 + p2[2 * i + 1] * g2);
      st16(c, (u32x4){p[0], p[1], p[2], p[3]}, c.voff, lay + L_O + ((qt + u) * 32 + c.head) * 1024);
    }
    STAMP((2 + 5 * l) * 10 + 6);
    asm volatile("s_waitcnt vmcnt(0)" ::: "memory");
    if (c.lane == 0) __hip_atomic_store(flag_ptr(c, FL_O + l * 32 + c.qb * 4 + c.w, c.head), c.token, RLX_AGENT);
  } else {
    const unsigned src = WS_W1 + (c.nb < 11 ? 3 * c.nb : 33 + 2 * (c.nb - 11)) * 32768;
    help_dma<16>(c, src, LD_W);
    if (c.nb < 11) help_dma<8>(c, src + 65536, LD_W + 65536);
  }
}

struct In { const unsigned short *x, *wqkv, *wo, *w1, *b1, *w2; unsigned short* out; };
DI u64 uniform64(u64 v) {
  return ((u64)__builtin_amdgcn_readfirstlane((unsigned)(v >> 32)) << 32) | (unsigned)__builtin_amdgcn_readfirstlane((unsigned)v);
}

DI void run_main(Ctx& c, const In& in) {
  const u64 epoch = __hip_atomic_load((gu64*)c.ws, RLX_AGENT);
  u32x4 res[2];
  {
    if (c.w == 0 && c.lane < 16) ((LAS unsigned*)(c.lds + LD_CTR))[c.lane] = 0u;
    const unsigned short* src = in.x + (size_t)((2 * c.tb64 + (c.w >> 1)) * 32 + c.r) * D_ + 32 * (2 * c.nb8 + (c.w & 1)) + 4 * c.h;
    u32x2 xr[4];
#pragma unroll
    for (int i = 0; i < 4; ++i) xr[i] = *(const u32x2*)(src + 8 * i);
    BARRIER_LDS();
    c.token = uniform64(epoch) + 1;
    res[0] = (u32x4){xr[0][0], xr[0][1], xr[1][0], xr[1][1]}; res[1] = (u32x4){xr[2][0], xr[2][1], xr[3][0], xr[3][1]};
    store_x(c, 0, res);
    publish(c, 0, 4, FL_X + c.tb, c.nb);
    STAMP(1);
    poll(c, (c.lane & 16) ? FL_WQ + c.nb : FL_X + c.tb, c.lane & 15);
    BARRIER_LDS();
    STAMP(2);
    BARRIER_LDS();
  }
  for (int l = 0; l < 2; ++l) {
    u32x4 wo[16], w2r[16];
    STAMP((1 + 5 * l) * 10 + 1);
    fresh(c);
    phase_ln<0>(c, l, 2 * l, w2r);
    fresh(c);
    STAMP((1 + 5 * l) * 10 + 9);
    poll(c, FL_Q + l * 16 + (c.head >> 1), c.lane & 15);
    BARRIER_LDS();
    STAMP((2 + 5 * l) * 10);
    phase_attn<0>(c, l, wo);
    fresh(c);
    STAMP((2 + 5 * l) * 10 + 9);
    poll(c, FL_O + l * 32 + c.tb64, c.lane & 31);
    BARRIER_LDS();
    STAMP((3 + 5 * l) * 10 + 1);
    phase_wo(c, l, wo, res);
    store_x(c, 2 * l + 1, res);
    STAMP((3 + 5 * l) * 10 + 6);
    publish(c, 8 + l, 4, FL_X + (2 * l + 1) * 16 + c.tb, c.nb);
    fresh(c);
    STAMP((3 + 5 * l) * 10 + 9);
    poll(c, FL_X + (2 * l + 1) * 16 + c.tb, c.lane & 15);
    BARRIER_LDS();
    STAMP((4 + 5 * l) * 10 + 1);
    phase_ln<1>(c, l, 2 * l + 1, w2r);
    fresh(c);
    STAMP((4 + 5 * l) * 10 + 9);
    poll(c, FL_H + l * 16 + c.tb, c.lane & 15);
    STAMP((4 + 5 * l) * 10 + 8);
    BARRIER_LDS();
    STAMP((5 + 5 * l) * 10 + 1);
    phase_w2(c, l, w2r, res);
    fresh(c);
    if (l == 0) {
      store_x(c, 2, res);
      STAMP((5 + 5 * l) * 10 + 6);
      publish(c, 10, 4, FL_X + 2 * 16 + c.tb, c.nb);
      STAMP((5 + 5 * l) * 10 + 9);
      poll(c, FL_X + 2 * 16 + c.tb, c.lane & 15);
      STAMP((5 + 5 * l) * 10 + 8);
      BARRIER_LDS();
    }
  }
  unsigned short* op = in.out + (size_t)((2 * c.tb64 + (c.w >> 1)) * 32 + c.r) * D_ + 32 * (2 * c.nb8 + (c.w & 1)) + 4 * c.h;
#pragma unroll
  for (int i = 0; i < 4; ++i) *(u32x2*)(op + 8 * i) = (u32x2){res[i >> 1][2 * (i & 1)], res[i >> 1][2 * (i & 1) + 1]};
  STAMP(109);
  if (c.bid == 0 && c.w == 0 && c.lane == 0) __hip_atomic_store((gu64*)c.ws, c.token, RLX_AGENT);
}

DI void run_help(Ctx& c, const In& in) {
  const u64 epoch = __hip_atomic_load((gu64*)c.ws, RLX_AGENT);
  {
    const int t = (c.w & 3) * 64 + c.lane;
    const int bcol = 32 * (c.nb < 11 ? 3 * c.nb : 33 + 2 * (c.nb - 11)) + t;
    const unsigned bv = in.b1[bcol < F_ ? bcol : 0];
    unsigned e0[8], e1[8];
    const int qk = 2 * c.tb + (c.w & 1), qa = (c.w >> 1) & 1;
    wfrag_load(in.wqkv, 3 * D_, qa * 16 + c.nb, qk, c.r, c.h, e0);
    wfrag_load(in.wqkv, 3 * D_, 32 + c.nb, qk, c.r, c.h, e1);
    BARRIER_LDS();
    c.token = uniform64(epoch) + 1;
    if (t < 96) ((LAS float*)(c.lds + LD_PAR))[192 + t] = __uint_as_float(bv << 16);
    wfrag_store(c, e0, WS_WQKV + ((qa * 16 + c.nb) * 32 + qk) * 1024, true, WS_CSP + (c.nb * 32 + qk) * 384 + qa * 128);
    if (qa == 0) wfrag_store(c, e1, WS_WQKV + ((32 + c.nb) * 32 + qk) * 1024, true, WS_CSP + (c.nb * 32 + qk) * 384 + 256);
    publish(c, 1, 4, FL_WQ + c.nb, c.tb);
    STAMP(1);
    BARRIER_LDS();
    STAMP(2);
    dma_wqkv(c);
    BARRIER_ALL();
    STAMP(11);
    fresh(c);
    help_colsum(c, 0);
    const int t0 = c.nb < 11 ? 3 * c.nb : 33 + 2 * (c.nb - 11), nt1 = c.nb < 11 ? 3 : 2;
    unsigned e[4][8];
    unsigned dst[4], csd[4]; bool ok[4];
#pragma unroll
    for (int u = 0; u < 4; ++u) {
      const int j = (c.w & 3) + 4 * u;
      const unsigned short* W; int N, nt, ks;
      if (j < 6) { W = in.w1; N = F_; ok[u] = (j >> 1) < nt1; nt = t0 + (ok[u] ? j >> 1 : 0); ks = 2 * c.tb + (j & 1); dst[u] = WS_W1 + (nt * 32 + ks) * 1024; }
      else if (j < 8) { W = in.wo; N = D_; ok[u] = true; nt = 2 * c.nb8 + (j & 1); ks = c.tb64; dst[u] = WS_WO + (nt * 32 + ks) * 1024; }
      else { W = in.w2; N = D_; ks = c.tb64 + 32 * ((j - 8) >> 1); ok[u] = ks < KS_F; if (!ok[u]) ks = 0; nt = 2 * c.nb8 + (j & 1); dst[u] = WS_W2 + (nt * KS_F + ks) * 1024; }
      csd[u] = WS_CSP + ((16 + c.nb) * 32 + ks) * 384 + (j >> 1) * 128;
      wfrag_load(W, N, nt, ks, c.r, c.h, e[u]);
    }
    SBAR();
    STAMP(12);
    help_ln<0>(c, 0);
#pragma unroll
    for (int u = 0; u < 4; ++u)
      if (ok[u]) wfrag_store(c, e[u], dst[u], (c.w & 3) + 4 * u < 6, csd[u]);
    asm volatile("s_waitcnt vmcnt(0)" ::: "memory");
    if (c.lane == 0) {
      const unsigned old = __hip_atomic_fetch_add((LAS unsigned*)(c.lds + LD_CTR) + 2, 1u, __ATOMIC_RELAXED, __HIP_MEMORY_SCOPE_WORKGROUP);
      if (old == 3) {
        __hip_atomic_store(flag_ptr(c, FL_W1 + c.nb, c.tb), c.token, RLX_AGENT);
        __hip_atomic_store(flag_ptr(c, FL_W2 + c.nb8, c.tb64), c.token, RLX_AGENT);
      }
    }
  }
  for (int l = 0; l < 2; ++l) {
    u32x4 wo[16];
    fresh(c);
    if (l == 1) help_ln<0>(c, l);
    STAMP((1 + 5 * l) * 10 + 9);
    BARRIER_LDS();
    STAMP((2 + 5 * l) * 10);
    fresh(c);
    phase_attn<1>(c, l, wo);
    fresh(c);
    STAMP((2 + 5 * l) * 10 + 9);
    BARRIER_LDS();
    BARRIER_LDS();
    if (l == 0) help_colsum(c, 1);
    BARRIER_ALL();
    STAMP((4 + 5 * l) * 10 + 1);
    dma_w2<42, 56>(c);
    if (l == 0) {
      const unsigned wq = WS_WQKV + c.nb * 32768 + (c.tb & 1) * 16384 + (c.w & 1) * 8192;
      const unsigned w0 = warm8k(c, wq + ((c.w >> 1) & 1) * (16 * 32768)), w1 = warm8k(c, wq + 32 * 32768);
      asm volatile("" ::"v"(w0), "v"(w1));
    }
    STAMP((4 + 5 * l) * 10 + 2);
    help_ln<1>(c, l);
    STAMP((4 + 5 * l) * 10 + 6);
    dma_w2<0, 42>(c);
    STAMP((4 + 5 * l) * 10 + 9);
    BARRIER_ALL();
    STAMP((5 + 5 * l) * 10 + 1);
    BARRIER_LDS();
    BARRIER_LDS();
    if (l == 0) {
      fresh(c);
      dma_wqkv(c);
      STAMP((5 + 5 * l) * 10 + 9);
      BARRIER_ALL();
    }
  }
}

__global__ __launch_bounds__(512, 2) void mega(const unsigned short* __restrict__ x, const unsigned short* __restrict__ wqkv,
                                               const unsigned short* __restrict__ wo_g, const unsigned short* __restrict__ w1,
                                               const unsigned short* __restrict__ b1, const unsigned short* __restrict__ w2,
                                               char* __restrict__ ws, unsigned short* __restrict__ out) {
  extern __shared__ __attribute__((aligned(16))) char lds_[];
  Ctx c;
  c.ws = ws; c.lds = (LAS char*)lds_; c.rs = __builtin_amdgcn_make_buffer_rsrc(ws, 0, WS_END, 0x00020000);
  c.lbase = (unsigned)(size_t)c.lds;
  c.bid = blockIdx.x; c.lane = threadIdx.x & 63; c.w = __builtin_amdgcn_readfirstlane(threadIdx.x >> 6); c.dead = false;
  fresh(c);
  STAMP(0);
  const In in = {x, wqkv, wo_g, w1, b1, w2, out};
  if (c.w < 4) run_main(c, in); else run_help(c, in);
}

extern "C" void kernel_launch(void* const* d_in, const int* in_sizes, int n_in, void* d_out, int out_size, void* d_ws,
                              size_t ws_size, hipStream_t stream) {
  (void)hipFuncSetAttribute((const void*)mega, hipFuncAttributeMaxDynamicSharedMemorySize, LDS_BYTES);
  mega<<<256, 512, LDS_BYTES, stream>>>((const unsigned short*)d_in[0], (const unsigned short*)d_in[1], (const unsigned short*)d_in[2],
                                        (const unsigned short*)d_in[4], (const unsigned short*)d_in[5], (const unsigned short*)d_in[6],
                                        (char*)d_ws, (unsigned short*)d_out);
}
```

```cpp
#include <hip/hip_runtime.h>
#include <hip/hip_bf16.h>

typedef __attribute__((ext_vector_type(2))) __bf16 bf16x2;
typedef __attribute__((ext_vector_type(8))) __bf16 bf16x8;
typedef __attribute__((ext_vector_type(16))) __bf16 bf16x16;
typedef __attribute__((ext_vector_type(16))) float f32x16;
typedef __attribute__((ext_vector_type(8))) float f32x8;
typedef __attribute__((ext_vector_type(4))) float f32x4;
typedef __attribute__((ext_vector_type(2))) float f32x2;
typedef __attribute__((ext_vector_type(4))) unsigned u32x4;
typedef __attribute__((ext_vector_type(2))) unsigned u32x2;
typedef unsigned long long u64;
#define LAS __attribute__((address_space(3)))
#define GAS __attribute__((address_space(1)))
typedef GAS u64 gu64;
#define DI __device__ __forceinline__
#define RLX_AGENT __ATOMIC_RELAXED, __HIP_MEMORY_SCOPE_AGENT

constexpr int S_ = 2048, D_ = 512, F_ = 1376;
constexpr int KS_F = F_ / 16;

constexpr unsigned MB = 1u << 20;
constexpr unsigned WS_FLAG = 0;
constexpr unsigned WS_WQKV = 128 * 1024;
constexpr unsigned WS_WO = WS_WQKV + 48 * 32 * 1024;
constexpr unsigned WS_W1 = WS_WO + 16 * 32 * 1024;
constexpr unsigned WS_W2 = WS_W1 + 43 * 32 * 1024;
constexpr unsigned WS_CSP = WS_W2 + 16 * 86 * 1024;
constexpr unsigned WS_ST = WS_CSP + 2 * 16 * 32 * 384;
constexpr unsigned WS_X = WS_ST + 4 * 64 * 4096;
constexpr unsigned WS_L = WS_X + 8 * MB;
constexpr unsigned L_Q = 0, L_K = 2 * MB, L_V = 4 * MB, L_O = 6 * MB, L_H = 8 * MB, L_SZ = 14 * MB;
constexpr unsigned WS_END = WS_L + 2 * L_SZ;
constexpr int FL_X = 1;
constexpr int FL_H = FL_X + 64;
constexpr int FL_Q = FL_H + 32;
constexpr int FL_O = FL_Q + 32;
constexpr int FL_WQ = FL_O + 64;
constexpr int FL_W1 = FL_WQ + 16;
constexpr int FL_W2 = FL_W1 + 16;
static_assert((FL_W2 + 8) * 256 <= 128 * 1024, "flag region");

constexpr int LD_W = 0;
constexpr int LD_V = 65536;
constexpr int LD_EX = 98304;
constexpr int LD_ONE = 131072;
constexpr int LD_CMB = 135168;
constexpr int LD_W2E = 122880;
constexpr int LD_SC = 151552;
constexpr int LD_ST = 155648;
constexpr int LD_PAR = 157696;
constexpr int LD_CTR = LD_PAR + 1280;
constexpr int LD_DBG = LD_CTR + 256;
constexpr int LDS_BYTES = 163840;

#define MFMA(a, b, c) __builtin_amdgcn_mfma_f32_32x32x16_bf16((a), (b), (c), 0, 0, 0)
#define SBAR() __builtin_amdgcn_sched_barrier(0)
#define PIN(x) asm volatile("" : "+v"(x))
#define BARRIER_LDS() asm volatile("s_waitcnt lgkmcnt(0)\n\ts_barrier" ::: "memory")
#define BARRIER_ALL() asm volatile("s_waitcnt vmcnt(0) lgkmcnt(0)\n\ts_barrier" ::: "memory")

#define STAMP(i)

DI unsigned pk2(float lo, float hi) {
  bf16x2 v; v[0] = (__bf16)lo; v[1] = (__bf16)hi;
  return __builtin_bit_cast(unsigned, v);
}
DI float bf_lo(unsigned u) { return __uint_as_float(u << 16); }
DI float bf_hi(unsigned u) { return __uint_as_float(u & 0xffff0000u); }
DI float rbf(float x) { return (float)(__bf16)x; }
DI bf16x8 as_bf(u32x4 v) { return __builtin_bit_cast(bf16x8, v); }
DI float swap_half_sum(float x) {
  auto r = __builtin_amdgcn_permlane32_swap(__float_as_uint(x), __float_as_uint(x), false, false);
  return __uint_as_float(r[0]) + __uint_as_float(r[1]);
}
DI float swap_half_max(float x) {
  auto r = __builtin_amdgcn_permlane32_swap(__float_as_uint(x), __float_as_uint(x), false, false);
  return fmaxf(__uint_as_float(r[0]), __uint_as_float(r[1]));
}
DI void glds16(const char* sbase, unsigned voff, unsigned lds_base) {
  unsigned sv;
  asm volatile("s_mov_b32 %0, m0\n\ts_mov_b32 m0, %3\n\ts_nop 2\n\tglobal_load_lds_dwordx4 %1, %2 sc1\n\ts_mov_b32 m0, %0"
               : "=&s"(sv) : "v"(voff), "s"(sbase), "s"(lds_base) : "memory");
}
DI void glds16x2(const char* sbase, unsigned voff, unsigned lds_base) {
  unsigned sv;
  asm volatile("s_mov_b32 %0, m0\n\ts_mov_b32 m0, %3\n\ts_nop 2\n\tglobal_load_lds_dwordx4 %1, %2 sc1\n\t"
               "global_load_lds_dwordx4 %1, %2 offset:1024 sc1\n\ts_mov_b32 m0, %0"
               : "=&s"(sv) : "v"(voff), "s"(sbase), "s"(lds_base) : "memory");
}
DI void glds16x4(const char* sbase, unsigned voff, unsigned lds_base) {
  unsigned sv;
  asm volatile("s_mov_b32 %0, m0\n\ts_mov_b32 m0, %3\n\ts_nop 2\n\tglobal_load_lds_dwordx4 %1, %2 sc1\n\t"
               "global_load_lds_dwordx4 %1, %2 offset:1024 sc1\n\tglobal_load_lds_dwordx4 %1, %2 offset:2048 sc1\n\t"
               "global_load_lds_dwordx4 %1, %2 offset:3072 sc1\n\ts_mov_b32 m0, %0"
               : "=&s"(sv) : "v"(voff), "s"(sbase), "s"(lds_base) : "memory");
}
template <int I> struct IC { static constexpr int v = I; };
template <int I, int N, class F> DI void static_for(F&& f) {
  if constexpr (I < N) { f(IC<I>{}); static_for<I + 1, N>(f); }
}

struct Ctx {
  char* ws; LAS char* lds; __amdgpu_buffer_rsrc_t rs;
  u64 token; unsigned lbase, voff;
  int bid, lane, w, r, h, tb, nb, tb64, nb8, head, qb;
  bool dead;
};
DI void fresh(Ctx& c) {
  asm volatile("" : "+s"(c.bid));
  asm volatile("" : "+s"(c.w));
  asm volatile("" : "+v"(c.lane));
  const int g = c.bid & 7, idx = c.bid >> 3;
  c.r = c.lane & 31; c.h = c.lane >> 5; c.voff = c.lane * 16;
  c.tb = 2 * g + (idx >> 4); c.nb = idx & 15; c.tb64 = 4 * g + (idx >> 3); c.nb8 = idx & 7;
  c.head = 4 * g + (idx & 3); c.qb = idx >> 2;
}
DI u32x4 ld16(const Ctx& c, unsigned voff, unsigned soff) { return __builtin_amdgcn_raw_buffer_load_b128(c.rs, voff, soff, 16); }
DI float ld4(const Ctx& c, unsigned voff, unsigned soff) { return __uint_as_float(__builtin_amdgcn_raw_buffer_load_b32(c.rs, voff, soff, 16)); }
DI void st16(const Ctx& c, u32x4 v, unsigned voff, unsigned soff) { __builtin_amdgcn_raw_buffer_store_b128(v, c.rs, voff + soff, 0, 16); }
DI void st4(const Ctx& c, float v, unsigned voff, unsigned soff) { __builtin_amdgcn_raw_buffer_store_b32(__float_as_uint(v), c.rs, voff, soff, 16); }
DI gu64* flag_ptr(const Ctx& c, int line, int slot) { return (gu64*)(c.ws + WS_FLAG) + line * 32 + slot; }

DI void publish(const Ctx& c, int ctr, unsigned n, int line, int slot) {
  asm volatile("s_waitcnt vmcnt(0)" ::: "memory");
  if (c.lane == 0) {
    const unsigned old = __hip_atomic_fetch_add((LAS unsigned*)(c.lds + LD_CTR) + ctr, 1u, __ATOMIC_RELAXED, __HIP_MEMORY_SCOPE_WORKGROUP);
    if (old == n - 1) __hip_atomic_store(flag_ptr(c, line, slot), c.token, RLX_AGENT);
  }
}
DI void poll(Ctx& c, int line, int slot) {
  if (c.w == 0 && !c.dead) {
    gu64* p = flag_ptr(c, line, slot);
    unsigned spins = 0;
    while (!__all(__hip_atomic_load(p, RLX_AGENT) == c.token)) {
      if (++spins > (1u << 18)) { c.dead = true; break; }
    }
    __builtin_amdgcn_fence(__ATOMIC_ACQUIRE, "wavefront");
  }
}
template <int NQ> DI void help_dma(const Ctx& c, unsigned src, int dst) {
#pragma unroll
  for (int u = 0; u < NQ / 4; ++u) {
    const int p = (c.w & 3) + 4 * u;
    glds16x4(c.ws + src + p * 4096, c.voff, c.lbase + dst + p * 4096);
  }
}
DI void dma_wqkv(const Ctx& c) {
  help_dma<8>(c, WS_WQKV + c.nb * 32768, LD_W);
  help_dma<8>(c, WS_WQKV + (16 + c.nb) * 32768, LD_W + 32768);
  help_dma<8>(c, WS_WQKV + (32 + c.nb) * 32768, LD_W + 65536);
}
DI int w2_run(int run) { return run < 6 ? LD_W + run * 14336 : LD_W2E + (run - 6) * 14336; }
template <int P0, int P1> DI void dma_w2(const Ctx& c) {
#pragma unroll
  for (int u = 0; u < (P1 - P0 + 3) / 4; ++u) {
    const int p = P0 + (c.w & 3) + 4 * u;
    if (p < P1) {
      const int run = p / 7, kq = run >> 1, kk = 2 * (p % 7);
      glds16x2(c.ws + WS_W2 + ((2 * c.nb8 + (run & 1)) * KS_F + kq * 21 + (kq < 2 ? kq : 2) + kk) * 1024, c.voff,
               c.lbase + w2_run(run) + kk * 1024);
    }
  }
}
DI unsigned warm8k(const Ctx& c, unsigned off) { return __builtin_amdgcn_raw_buffer_load_b32(c.rs, c.lane * 128, off, 0); }

DI void store_x(const Ctx& c, int inst, const u32x4 (&res)[2]) {
  const int tt = 2 * c.tb64 + (c.w >> 1), nt = 2 * c.nb8 + (c.w & 1);
  st16(c, res[0], c.voff, WS_X + inst * (2 * MB) + (tt * 32 + 2 * nt) * 1024);
  st16(c, res[1], c.voff, WS_X + inst * (2 * MB) + (tt * 32 + 2 * nt + 1) * 1024);
  float s1 = 0.f, s2 = 0.f;
#pragma unroll
  for (int i = 0; i < 8; ++i) {
    const unsigned u = res[i >> 2][i & 3];
    const float a = bf_lo(u), b = bf_hi(u);
    s1 += a + b; s2 = fmaf(a, a, fmaf(b, b, s2));
  }
  auto r1 = __builtin_amdgcn_permlane32_swap(__float_as_uint(s1), __float_as_uint(s2), false, false);
  st4(c, __uint_as_float(r1[0]) + __uint_as_float(r1[1]), c.lane * 4, WS_ST + inst * (64 * 4096) + tt * 4096 + nt * 256);
}

DI void wfrag_load(const unsigned short* W, int N, int nt, int ks, int r, int h, unsigned (&e)[8]) {
#pragma unroll
  for (int j = 0; j < 8; ++j) e[j] = W[(size_t)(16 * ks + 8 * (j >> 2) + 4 * h + (j & 3)) * N + 32 * nt + r];
}
DI void wfrag_store(const Ctx& c, const unsigned (&e)[8], unsigned dst, bool docs, unsigned csdst) {
  u32x4 v;
#pragma unroll
  for (int j = 0; j < 4; ++j) v[j] = e[2 * j] | (e[2 * j + 1] << 16);
  st16(c, v, c.voff, dst);
  if (docs) {
    float s = 0.f;
#pragma unroll
    for (int j = 0; j < 8; ++j) s += __uint_as_float(e[j] << 16);
    s = swap_half_sum(s);
    if (c.h == 0) st4(c, s, c.r * 4, csdst);
  }
}

DI float mish_f(float hh) {
  const float u = __builtin_amdgcn_exp2f(fminf(hh, 30.f) * 1.4426950408889634f);
  const float n = u * (u + 2.f);
  return hh * n * __builtin_amdgcn_rcpf(n + 2.f);
}
template <int MODE>
DI u32x2 epi4(float a0, float a1, float a2, float a3, f32x4 cs, f32x4 bb, float mr, float rstd) {
  const float a[4] = {a0, a1, a2, a3};
  float v[4];
#pragma unroll
  for (int j = 0; j < 4; ++j) {
    if (MODE == 0) v[j] = fmaf(a[j], rstd, mr * cs[j]);
    else v[j] = mish_f(fmaf(a[j], rstd, fmaf(mr, cs[j], bb[j])));
  }
  return (u32x2){pk2(v[0], v[1]), pk2(v[2], v[3])};
}
template <int MODE>
DI u32x4 epi8(const f32x8& a, const f32x4* cs, const f32x4* bb, float mr, float rstd) {
  const u32x2 lo = epi4<MODE>(a[0], a[1], a[2], a[3], cs[0], bb[0], mr, rstd);
  const u32x2 hi = epi4<MODE>(a[4], a[5], a[6], a[7], cs[1], bb[1], mr, rstd);
  return (u32x4){lo[0], lo[1], hi[0], hi[1]};
}
#define LO8(s) __builtin_shufflevector(s, s, 0, 1, 2, 3, 4, 5, 6, 7)
#define HI8(s) __builtin_shufflevector(s, s, 8, 9, 10, 11, 12, 13, 14, 15)
DI f32x4 q4(const f32x8& v, int g) { return g ? (f32x4){v[4], v[5], v[6], v[7]} : (f32x4){v[0], v[1], v[2], v[3]}; }
DI f32x8 cat8(f32x4 a, f32x4 b) { return __builtin_shufflevector(a, b, 0, 1, 2, 3, 4, 5, 6, 7); }

struct LnPar { f32x4 cs[6], bb[6]; float csv; };
template <int MODE, int T> DI void ln_params(const Ctx& c, LnPar& p) {
  const LAS float* par = (const LAS float*)(c.lds + LD_PAR) + MODE * 96 + 4 * c.h;
#pragma unroll
  for (int g = 0; g < 6; ++g) {
    const int o = g < 4 ? 32 * T + 8 * g : 64 + 16 * T + 8 * (g - 4);
    if (MODE == 1 || g < 4) p.cs[g] = *(const LAS f32x4*)(par + o);
    if (MODE == 1) p.bb[g] = *(const LAS f32x4*)(par + 96 + o);
  }
  if (MODE == 0) p.csv = ((const LAS float*)(c.lds + LD_PAR))[64 + c.r];
}
template <int T> DI void ln_tokstats(const Ctx& c, f32x4 (&ts)[4]) {
  const LAS f32x2* st = (const LAS f32x2*)(c.lds + LD_ST) + (c.w & 3) * 32 + 16 * T + 4 * c.h;
#pragma unroll
  for (int g = 0; g < 4; ++g) ts[g] = *(const LAS f32x4*)(st + 8 * (g >> 1) + 2 * (g & 1));
}
template <int MODE, int T>
DI void ln_epilogue(Ctx& c, int l, const f32x16& a, const f32x8& ch, float mean, float rstd, const LnPar& p, const f32x4 (&ts)[4]) {
  const int tt = 4 * c.tb + (c.w & 3);
  const unsigned lay = WS_L + l * L_SZ;
  if (MODE == 0) {
    const float rs = T ? rstd : rstd * (0.25f * 1.4426950408889634f), mr = -mean * rs;
    const unsigned vo = T ? c.h * 512 + ((c.r & 19) | ((c.r & 4) << 1) | ((c.r & 8) >> 1)) * 16 : c.voff;
    const unsigned so = lay + (T ? L_K : L_Q) + (2 * c.nb * 64 + tt) * 1024;
    st16(c, epi8<0>(LO8(a), p.cs, p.cs, mr, rs), vo, so);
    st16(c, epi8<0>(HI8(a), p.cs + 2, p.cs, mr, rs), vo, so + 65536);
    unsigned q[4];
#pragma unroll
    for (int g = 0; g < 4; ++g) q[g] = pk2((ch[2 * g] - ts[g][0] * p.csv) * ts[g][1], (ch[2 * g + 1] - ts[g][2] * p.csv) * ts[g][3]);
    st16(c, (u32x4){q[0], q[1], q[2], q[3]}, (c.r >> 4) * 65536 + c.h * 256 + (c.r & 15) * 16, lay + L_V + (2 * c.nb * 128 + 2 * tt + T) * 512);
    publish(c, 4 + l, 8, FL_Q + l * 16 + c.nb, c.tb);
  } else {
    const float mr = -mean * rstd;
    const unsigned so = lay + L_H + (tt * KS_F + 2 * (c.nb < 11 ? 3 * c.nb : 33 + 2 * (c.nb - 11))) * 1024;
    st16(c, epi8<1>(LO8(a), p.cs, p.bb, mr, rstd), c.voff, so + T * 2048);
    st16(c, epi8<1>(HI8(a), p.cs + 2, p.bb + 2, mr, rstd), c.voff, so + T * 2048 + 1024);
    if (c.nb < 11) st16(c, epi8<1>(ch, p.cs + 4, p.bb + 4, mr, rstd), c.voff, so + 4096 + T * 1024);
    publish(c, 6 + l, 8, FL_H + l * 16 + c.tb, c.nb);
  }
}

DI void pin16(f32x16& v) { PIN(v); }
DI u32x4 wfrag(const LAS char* base, int ks) { return *(const LAS u32x4*)(base + ks * 1024); }

template <int MODE, bool FIRST>
DI void phase_ln(Ctx& c, int l, int inst, u32x4 (&w2r)[16]) {
  const int PH = (1 + 5 * l + (MODE ? 3 : 0)) * 10; (void)PH;
  const int tt = 4 * c.tb + c.w;
  const bool has3 = MODE == 0 || c.nb < 11;
  const unsigned xs = WS_X + inst * (2 * MB) + tt * 32768;
  constexpr int PD = 12;
  u32x4 xf[PD], sp[4];
#pragma unroll
  for (int k = 0; k < PD; ++k) xf[k] = ld16(c, c.voff, xs + k * 1024);
  SBAR();
  const LAS char* wl = c.lds + LD_W + c.lane * 16;
  const LAS char* wl2 = wl + 65536;
  asm volatile("" : "+v"(wl2));
  f32x16 aA, aB, aC;
#pragma unroll
  for (int i = 0; i < 16; ++i) { aA[i] = 0.f; aB[i] = 0.f; aC[i] = 0.f; }
  u32x4 fa[3], fb[3], fc[3];
  if (FIRST) BARRIER_LDS();
#pragma unroll
  for (int k = 0; k < 2; ++k) { fa[k] = wfrag(wl, k); fb[k] = wfrag(wl + 32768, k); fc[k] = wfrag(wl2, k); }
  float mean = 0.f, rstd = 0.f, sc4[4];
  LnPar par; f32x4 ts[4];
  LAS float* sc = (LAS float*)(c.lds + LD_SC) + c.w * 256;
  const int k0q = c.w * 21 + (c.w < 2 ? c.w : 2);
  const unsigned w2s = WS_W2 + ((2 * c.nb8 + (c.w & 1)) * KS_F + k0q + 14) * 1024;
  const unsigned w2o = WS_W2 + ((2 * c.nb8 + 1 - (c.w & 1)) * KS_F + k0q + 14) * 1024;
  static_for<0, 32>([&](auto kc) __attribute__((always_inline)) {
    constexpr int k = decltype(kc)::v;
    if constexpr (FIRST && (k & 7) == 6 && k < 24) asm volatile("s_barrier" ::: "memory");
    aA = MFMA(as_bf(fa[k % 3]), as_bf(xf[k % PD]), aA);
    pin16(aA); SBAR();
    if constexpr (k + 2 < 32) fa[(k + 2) % 3] = wfrag(wl, k + 2);
    if constexpr (k >= 1 && k - 1 + PD < 32) xf[(k - 1) % PD] = ld16(c, c.voff, xs + (k - 1 + PD) * 1024);
    if constexpr (k >= 21 && k < 23) {
      sp[2 * k - 42] = ld16(c, c.voff, WS_ST + inst * (64 * 4096) + tt * 4096 + (2 * k - 42) * 1024);
      sp[2 * k - 41] = ld16(c, c.voff, WS_ST + inst * (64 * 4096) + tt * 4096 + (2 * k - 41) * 1024);
    }
    SBAR();
    aB = MFMA(as_bf(fb[k % 3]), as_bf(xf[k % PD]), aB);
    pin16(aB); SBAR();
    if constexpr (k + 2 < 32) fb[(k + 2) % 3] = wfrag(wl + 32768, k + 2);
    if constexpr (MODE == 1 && k >= 23 && k < 31) {
      constexpr int i = k - 23;
      const int kk = (i == 7 && c.w >= 2) ? 6 : i;
      w2r[2 * i] = ld16(c, c.voff, w2s + kk * 1024); w2r[2 * i + 1] = ld16(c, c.voff, w2o + kk * 1024);
    }
    if constexpr (k == 28) {
      ((LAS f32x4*)sc)[c.lane] = (__builtin_bit_cast(f32x4, sp[0]) + __builtin_bit_cast(f32x4, sp[1])) +
                                 (__builtin_bit_cast(f32x4, sp[2]) + __builtin_bit_cast(f32x4, sp[3]));
#pragma unroll
      for (int i = 0; i < 4; ++i) sc4[i] = sc[i * 64 + c.lane];
    }
    if constexpr (k == 30) {
      const float s = (sc4[0] + sc4[1]) + (sc4[2] + sc4[3]);
      auto rr = __builtin_amdgcn_permlane32_swap(__float_as_uint(s), __float_as_uint(s), false, false);
      const float mu = __uint_as_float(rr[0]) * (1.f / D_);
      const float var = fmaxf(__uint_as_float(rr[1]) * (1.f / D_) - mu * mu, 0.f);
      mean = rbf(mu);
      rstd = __builtin_amdgcn_rcpf(rbf(__builtin_amdgcn_sqrtf(rbf(rbf(var) + 1e-5f))));
      ((LAS f32x2*)(c.lds + LD_ST))[c.w * 32 + c.r] = (f32x2){mean, rstd};
    }
    SBAR();
    if (has3) aC = MODE == 0 ? MFMA(as_bf(xf[k % PD]), as_bf(fc[k % 3]), aC) : MFMA(as_bf(fc[k % 3]), as_bf(xf[k % PD]), aC);
    pin16(aC); SBAR();
    if constexpr (k + 2 < 32) fc[(k + 2) % 3] = wfrag(wl2, k + 2);
    SBAR();
  });
  STAMP(PH + 2);
  if (MODE == 0) ln_tokstats<0>(c, ts);
  if (!FIRST) ln_params<MODE, 0>(c, par);
  SBAR();
  {
    LAS f32x4* ex = (LAS f32x4*)(c.lds + LD_EX) + c.w * 384 + c.lane;
#pragma unroll
    for (int g = 0; g < 4; ++g) ex[g * 64] = (f32x4){aB[4 * g], aB[4 * g + 1], aB[4 * g + 2], aB[4 * g + 3]};
    ex[256] = (f32x4){aC[8], aC[9], aC[10], aC[11]}; ex[320] = (f32x4){aC[12], aC[13], aC[14], aC[15]};
  }
  BARRIER_LDS();
  STAMP(PH + 4);
  if (FIRST) ln_params<MODE, 0>(c, par);
  ln_epilogue<MODE, 0>(c, l, aA, LO8(aC), mean, rstd, par, ts);
}
template <int MODE, bool FIRST>
DI void help_ln(Ctx& c, int l) {
  LnPar par; f32x4 ts[4];
  if (!FIRST) ln_params<MODE, 1>(c, par);
  BARRIER_LDS();
  if (FIRST) ln_params<MODE, 1>(c, par);
  const LAS f32x4* ex = (const LAS f32x4*)(c.lds + LD_EX) + (c.w & 3) * 384 + c.lane;
  const f32x2 ms = ((const LAS f32x2*)(c.lds + LD_ST))[(c.w & 3) * 32 + c.r];
  const f32x8 lo = cat8(ex[0], ex[64]), hi = cat8(ex[128], ex[192]), ch = cat8(ex[256], ex[320]);
  if (MODE == 0) ln_tokstats<1>(c, ts);
  SBAR();
  const f32x16 a = __builtin_shufflevector(lo, hi, 0, 1, 2, 3, 4, 5, 6, 7, 8, 9, 10, 11, 12, 13, 14, 15);
  ln_epilogue<MODE, 1>(c, l, a, ch, ms[0], ms[1], par, ts);
}
DI void help_colsum(const Ctx& c, int mode) {
  const int t = (c.w - 4) * 64 + c.lane;
  if (t < 96) {
    float v[32];
#pragma unroll
    for (int i = 0; i < 32; ++i) v[i] = ld4(c, t * 4, WS_CSP + ((mode * 16 + c.nb) * 32 + i) * 384);
    float s = 0.f;
#pragma unroll
    for (int i = 0; i < 32; i += 4) s += (v[i] + v[i + 1]) + (v[i + 2] + v[i + 3]);
    ((LAS float*)(c.lds + LD_PAR))[mode * 96 + t] = s;
  }
}

DI void reduce_res(Ctx& c, const f32x16 (&a)[4], u32x4 (&res)[2], int PH) {
  LAS f32x4* red = (LAS f32x4*)(c.lds + LD_EX) + c.lane;
#pragma unroll
  for (int j = 1; j < 4; ++j) {
    LAS f32x4* p = red + ((c.w ^ j) * 3 + j - 1) * 256;
#pragma unroll
    for (int g = 0; g < 4; ++g) p[g * 64] = (f32x4){a[j][4 * g], a[j][4 * g + 1], a[j][4 * g + 2], a[j][4 * g + 3]};
  }
  STAMP(PH + 3);
  BARRIER_LDS();
  STAMP(PH + 4);
  const LAS f32x4* q = red + c.w * 768;
#pragma unroll
  for (int g = 0; g < 4; ++g) {
    const f32x4 s = ((f32x4){a[0][4 * g], a[0][4 * g + 1], a[0][4 * g + 2], a[0][4 * g + 3]} + q[g * 64]) + (q[256 + g * 64] + q[512 + g * 64]);
    const unsigned u0 = res[g >> 1][2 * (g & 1)], u1 = res[g >> 1][2 * (g & 1) + 1];
    res[g >> 1][2 * (g & 1)] = pk2(bf_lo(u0) + rbf(s[0]), bf_hi(u0) + rbf(s[1]));
    res[g >> 1][2 * (g & 1) + 1] = pk2(bf_lo(u1) + rbf(s[2]), bf_hi(u1) + rbf(s[3]));
  }
}
#define MM4(w0, w1, x0, x1) do { a[0] = MFMA(as_bf(w0), as_bf(x0), a[0]); a[1] = MFMA(as_bf(w1), as_bf(x0), a[1]); \
                                 a[2] = MFMA(as_bf(w0), as_bf(x1), a[2]); a[3] = MFMA(as_bf(w1), as_bf(x1), a[3]); } while (0)

DI void phase_wo(Ctx& c, int l, const u32x4 (&wo)[16], u32x4 (&res)[2]) {
  const unsigned xs = WS_L + l * L_SZ + L_O + ((2 * c.tb64 + (c.w >> 1)) * 32 + 8 * c.w) * 1024;
  const unsigned xo = WS_L + l * L_SZ + L_O + ((2 * c.tb64 + 1 - (c.w >> 1)) * 32 + 8 * c.w) * 1024;
  u32x4 xf[16];
#pragma unroll
  for (int k = 0; k < 8; ++k) { xf[2 * k] = ld16(c, c.voff, xs + k * 1024); xf[2 * k + 1] = ld16(c, c.voff, xo + k * 1024); }
  SBAR();
  f32x16 a[4];
#pragma unroll
  for (int i = 0; i < 64; ++i) a[i >> 4][i & 15] = 0.f;
#pragma unroll
  for (int k = 0; k < 8; ++k) { MM4(wo[2 * k], wo[2 * k + 1], xf[2 * k], xf[2 * k + 1]); PIN(a[0]); PIN(a[1]); PIN(a[2]); PIN(a[3]); SBAR(); }
  STAMP((3 + 5 * l) * 10 + 2);
  reduce_res(c, a, res, (3 + 5 * l) * 10);
}

DI void phase_w2(Ctx& c, int l, const u32x4 (&w2r)[16], u32x4 (&res)[2]) {
  const int k0 = c.w * 21 + (c.w < 2 ? c.w : 2);
  const unsigned xs = WS_L + l * L_SZ + L_H + ((2 * c.tb64 + (c.w >> 1)) * KS_F + k0) * 1024;
  const unsigned xo = WS_L + l * L_SZ + L_H + ((2 * c.tb64 + 1 - (c.w >> 1)) * KS_F + k0) * 1024;
  constexpr int WIN = 10;
  u32x4 xf[2 * WIN];
#pragma unroll
  for (int k = 0; k < WIN; ++k) { xf[2 * k] = ld16(c, c.voff, xs + k * 1024); xf[2 * k + 1] = ld16(c, c.voff, xo + k * 1024); }
  SBAR();
  const LAS u32x4* w0 = (const LAS u32x4*)(c.lds + w2_run(2 * c.w + (c.w & 1))) + c.lane;
  const LAS u32x4* w1 = (const LAS u32x4*)(c.lds + w2_run(2 * c.w + 1 - (c.w & 1))) + c.lane;
  f32x16 a[4];
#pragma unroll
  for (int i = 0; i < 64; ++i) a[i >> 4][i & 15] = 0.f;
  u32x4 f0[2], f1[2];
  f0[0] = w0[0]; f1[0] = w1[0];
  static_for<0, 22>([&](auto kc) __attribute__((always_inline)) {
    constexpr int k = decltype(kc)::v;
    const u32x4 g0 = k < 14 ? f0[k & 1] : w2r[k < 14 ? 0 : 2 * (k - 14)], g1 = k < 14 ? f1[k & 1] : w2r[k < 14 ? 0 : 2 * (k - 14) + 1];
    const u32x4 x0 = xf[2 * (k % WIN)], x1 = xf[2 * (k % WIN) + 1];
    constexpr int kn = k - 1 + WIN;
    const int kk = (kn == 21 && c.w >= 2) ? 20 : kn;
    if (k < 21 || c.w < 2) {
      a[0] = MFMA(as_bf(g0), as_bf(x0), a[0]);
      pin16(a[0]); SBAR();
      if constexpr (k >= 1 && kn < 22) xf[2 * ((k - 1) % WIN)] = ld16(c, c.voff, xs + kk * 1024);
      SBAR();
      a[1] = MFMA(as_bf(g1), as_bf(x0), a[1]);
      pin16(a[1]); SBAR();
      if constexpr (k + 1 < 14) f0[(k + 1) & 1] = w0[(k + 1) * 64];
      SBAR();
      a[2] = MFMA(as_bf(g0), as_bf(x1), a[2]);
      pin16(a[2]); SBAR();
      if constexpr (k >= 1 && kn < 22) xf[2 * ((k - 1) % WIN) + 1] = ld16(c, c.voff, xo + kk * 1024);
      SBAR();
      a[3] = MFMA(as_bf(g1), as_bf(x1), a[3]);
      pin16(a[3]); SBAR();
      if constexpr (k + 1 < 14) f1[(k + 1) & 1] = w1[(k + 1) * 64];
      SBAR();
    }
  });
  STAMP((5 + 5 * l) * 10 + 2);
  BARRIER_LDS();
  reduce_res(c, a, res, (5 + 5 * l) * 10);
}

#define SMF(a, b8, c) __builtin_amdgcn_smfmac_f32_32x32x32_bf16((a), __builtin_bit_cast(bf16x16, b8), (c), sidx, 0, 0)
#define LDF(p) (*(const LAS bf16x8*)(p))
DI float max16(const f32x16& s) {
  float a = fmaxf(fmaxf(s[0], s[1]), s[2]), b = fmaxf(fmaxf(s[3], s[4]), s[5]);
  a = fmaxf(fmaxf(a, s[6]), s[7]); b = fmaxf(fmaxf(b, s[8]), s[9]);
  a = fmaxf(fmaxf(a, s[10]), s[11]); b = fmaxf(fmaxf(b, s[12]), s[13]);
  a = fmaxf(fmaxf(a, s[14]), s[15]);
  return swap_half_max(fmaxf(a, b));
}
DI void attn_slow(const LAS char* kp, const LAS char* vp, bool isv, bf16x8 q, f32x16& o, float& m) {
  const int sidx = 0xDDDDDDDD;
  f32x16 zero;
#pragma unroll
  for (int i = 0; i < 16; ++i) zero[i] = 0.f;
  o = zero; m = -3.0e38f;
  for (int t = 0; t < 32; ++t) {
    f32x16 s = MFMA(LDF(kp + t * 1024), q, zero);
    const float mn = fmaxf(m, max16(s));
    const float al = __builtin_amdgcn_exp2f(m - mn);
    m = mn;
#pragma unroll
    for (int i = 0; i < 16; ++i) { o[i] *= al; s[i] = __builtin_amdgcn_exp2f(s[i] - mn); }
    const int vo = isv ? t * 1024 : 0;
    o = SMF(LDF(vp + vo), LO8(s), o);
    o = SMF(LDF(vp + vo + 512), HI8(s), o);
  }
}

#define EX1(r) "v_exp_f32 v" #r ", v" #r "\n\t"
#define ASTEP(KOFF, VOFF, kc_, kn_, v0_, v1_, nv0_, pv1_)                                                                  \
  asm volatile("s_waitcnt lgkmcnt(0)\n\t" EX1(64) EX1(65)                                                                  \
               "v_smfmac_f32_32x32x32_bf16 %[o1], %[pv1], v[88:95], %[idx]\n\t"                                            \
               "ds_read_b128 %[nv0], %[vp] offset:%[vo]\n\tds_read_b128 %[pv1], %[vp] offset:%[vo1]\n\t"                   \
               EX1(66) EX1(67) EX1(68)                                                                                     \
               "v_mfma_f32_32x32x16_bf16 v[80:95], %[kc], %[q1], %[c1]\n\t"                                                \
               "ds_read_b128 %[kc], %[kp] offset:%[ko]\n\t"                                                                \
               EX1(69) EX1(70) EX1(71) EX1(72) EX1(73)                                                                     \
               "v_smfmac_f32_32x32x32_bf16 %[o0], %[v0], v[64:71], %[idx]\n\t"                                             \
               EX1(74) EX1(75) EX1(76) EX1(77) EX1(78) EX1(79) EX1(80) EX1(81)                                             \
               "v_smfmac_f32_32x32x32_bf16 %[o0], %[v1], v[72:79], %[idx]\n\t"                                             \
               EX1(82) EX1(83) EX1(84)                                                                                     \
               "v_mfma_f32_32x32x16_bf16 v[64:79], %[kn], %[q0], %[c0]\n\t"                                                \
               EX1(85) EX1(86) EX1(87) EX1(88) EX1(89)                                                                     \
               "v_smfmac_f32_32x32x32_bf16 %[o1], %[v0], v[80:87], %[idx]\n\t"                                             \
               EX1(90) EX1(91) EX1(92) EX1(93) EX1(94) "v_exp_f32 v95, v95"                                                \
               : "+{v[64:79]}"(sA), "+{v[80:95]}"(sB), [o0] "+v"(o0), [o1] "+v"(o1), [kc] "+v"(kc_), [nv0] "=&v"(nv0_),    \
                 [pv1] "+v"(pv1_)                                                                                          \
               : [kn] "v"(kn_), [v0] "v"(v0_), [v1] "v"(v1_), [q0] "v"(q0), [q1] "v"(q1), [c0] "v"(cin0), [c1] "v"(cin1),  \
                 [idx] "v"(sidx), [kp] "v"(kpa), [vp] "v"(vpa), [ko] "n"(KOFF), [vo] "n"(VOFF), [vo1] "n"((VOFF) + 512)     \
               : "memory")

template <int half>
DI void phase_attn(Ctx& c, int l, u32x4 (&wo)[16]) {
  const int qt = c.qb * 8 + (c.w & 3) * 2;
  const unsigned lay = WS_L + l * L_SZ;
  const char* kvg = c.ws + lay + ((c.w & 1) ? L_V : L_K) + c.head * 65536 + ((c.w >> 1) & 1) * 32768 + half * 4096;
  const unsigned kvd = c.lbase + ((c.w & 1) ? LD_V : LD_W) + ((c.w >> 1) & 1) * 32768 + half * 4096;
  u32x4 qv0, qv1;
  asm volatile("s_nop 4\n\tglobal_load_dwordx4 %0, %2, %3 sc1\n\tglobal_load_dwordx4 %1, %2, %3 offset:1024 sc1"
               : "=&v"(qv0), "=&v"(qv1) : "v"(c.voff), "s"(c.ws + lay + L_Q + (c.head * 64 + qt) * 1024) : "memory");
#define KV4(u) glds16x4(kvg + (u) * 8192, c.voff, kvd + (u) * 8192)
  glds16x4(kvg, c.voff, kvd);
  if (half == 0) {
    const unsigned o1 = (c.lane & 11) == 0 ? 0x3f803f80u : 0u;
    ((LAS u32x4*)(c.lds + LD_ONE))[c.w * 64 + c.lane] = (u32x4){o1, o1, o1, o1};
  }
  int sidx = 0xDDDDDDDD;
  const bool isv = c.r < 16;
  const LAS char* kp0 = c.lds + LD_W + half * 32768 + c.lane * 16;
  const LAS char* vp0 = c.lds + (isv ? LD_V + half * 32768 : LD_ONE) + c.h * 256 + (c.r & 15) * 16;
  const int vstep = isv ? 4096 : 0;
  if (half == 0) asm volatile("s_waitcnt vmcnt(0) lgkmcnt(0)\n\ts_barrier" : "+v"(qv0), "+v"(qv1)::"memory");
  else asm volatile("s_waitcnt vmcnt(4)\n\ts_barrier" : "+v"(qv0), "+v"(qv1)::"memory");
  STAMP((2 + 5 * l) * 10 + 1);
  const bf16x8 q0 = as_bf(qv0), q1 = as_bf(qv1);
  f32x16 zero;
#pragma unroll
  for (int i = 0; i < 16; ++i) zero[i] = 0.f;
  f32x16 cin0, cin1, o0 = zero, o1 = zero, sA, sB = zero;
  float m0, m1;
  bf16x8 ka = LDF(kp0), kb = LDF(kp0 + 1024), va0 = LDF(vp0), va1 = LDF(vp0 + 512), vb0, vb1 = as_bf((u32x4){0u, 0u, 0u, 0u});
  {
    const f32x16 s = MFMA(ka, q0, zero), s1 = MFMA(ka, q1, zero);
    m0 = max16(s); m1 = max16(s1);
#pragma unroll
    for (int i = 0; i < 16; ++i) { cin0[i] = -m0; cin1[i] = -m1; sA[i] = s[i] - m0; }
  }
  unsigned kpa = (unsigned)(size_t)kp0, vpa = (unsigned)(size_t)vp0;
  asm volatile("s_nop 1" : "+v"(cin0), "+v"(cin1), "+v"(sA), "+v"(sB), "+v"(o0), "+v"(o1), "+v"(sidx), "+v"(vb1));
  const unsigned wos = WS_WO + ((2 * c.nb8 + (c.w & 1)) * 32 + 8 * c.w) * 1024;
  const unsigned woo = WS_WO + ((2 * c.nb8 + 1 - (c.w & 1)) * 32 + 8 * c.w) * 1024;
  unsigned wm[6] = {0u, 0u, 0u, 0u, 0u, 0u};
  const unsigned wm1 = WS_W1 + (c.nb < 11 ? 3 * c.nb * 32768 + ((c.tb & 1) * 4 + (c.w & 3)) * 12288 : (33 + 2 * (c.nb - 11)) * 32768 + ((c.tb & 1) * 4 + (c.w & 3)) * 7168);
  const unsigned wm2 = WS_W2 + 2 * c.nb8 * KS_F * 1024 + ((c.tb64 & 3) * 4 + (c.w & 3)) * 11008;
#define QUAD(A, B, MID, C, D)                                                        \
  do {                                                                               \
    ASTEP(2048, 1024, ka, kb, va0, va1, vb0, vb1); A;                                \
    ASTEP(3072, 2048, kb, ka, vb0, vb1, va0, va1); B; MID;                           \
    ASTEP(4096, 3072, ka, kb, va0, va1, vb0, vb1); C;                                \
    vpa += vstep;                                                                    \
    ASTEP(5120, 0, kb, ka, vb0, vb1, va0, va1); D;                                   \
    kpa += 4096; QSTAMP();                                                           \
  } while (0)
#define NOP (void)0
#define QSTAMP()
#define WOL(k) do { if (half == 0) wo[k] = ld16(c, c.voff, (((k) & 1) ? woo : wos) + ((k) >> 1) * 1024);                    \
                    else if (((k) & 3) == 0) wm[(k) >> 2] = warm8k(c, (k) < 8 ? wm1 + ((k) >> 2) * 4096 : wm2 + (((k) >> 2) - 2) * 2816); \
                    SBAR(); } while (0)
#define WBAR(n) asm volatile("s_waitcnt vmcnt(" #n ")\n\ts_barrier" ::: "memory")
#define WQW(k) do { if (half == 1 && l == 0) wm[4 + (k)] = warm8k(c, WS_WQKV + c.nb * 32768 + (c.tb & 1) * 16384 + (c.w & 1) * 8192 + ((k) ? 32 : ((c.w >> 1) & 1) * 16) * 32768); SBAR(); } while (0)
#define PREPPUB() do { if (half == 1 && l == 0 && c.w == 4 && c.lane < 2) \
    __hip_atomic_store(c.lane ? flag_ptr(c, FL_W1 + c.nb, c.tb) : flag_ptr(c, FL_W2 + c.nb8, c.tb64), c.token, RLX_AGENT); } while (0)
#define PREPCHK() do { if (half == 0 && l == 0) poll(c, c.lane < 32 ? FL_W1 + c.nb : FL_W2 + c.nb8, c.lane < 32 ? (c.lane & 15) : (c.lane & 31)); } while (0)
  KV4(1);
  QUAD(KV4(2), NOP, WBAR(8), KV4(3), NOP);
  QUAD(NOP, NOP, WBAR(0), PREPPUB(), NOP);
  QUAD(NOP, NOP, NOP, NOP, NOP);
  QUAD(NOP, PREPCHK(), if (l == 0) WBAR(0), WOL(0); WOL(1), WOL(2); WOL(3));
  STAMP((2 + 5 * l) * 10 + 5);
  QUAD(WOL(4), WOL(5), NOP, WOL(6), WOL(7));
  QUAD(WOL(8), WOL(9), NOP, WOL(10), WOL(11));
  QUAD(WOL(12), WOL(13); WQW(0), NOP, WOL(14); WQW(1), WOL(15));
  QUAD(NOP, NOP, NOP, NOP, NOP);
#undef WBAR
#undef PREPCHK
#undef PREPPUB
#undef WQW
  asm volatile("s_nop 1\n\tv_smfmac_f32_32x32x32_bf16 %[o1], %[pv1], v[88:95], %[idx]\n\ts_waitcnt lgkmcnt(0)\n\ts_nop 7\n\ts_nop 7"
               : [o1] "+v"(o1), "+v"(o0), "+v"(ka), "+v"(kb), "+v"(va0), "+v"(va1) : [pv1] "v"(vb1), "{v[80:95]}"(sB), [idx] "v"(sidx));
#undef QUAD
#undef KV4
#undef WOL
  STAMP((2 + 5 * l) * 10 + 2);
  asm volatile("" ::"v"(wm[0]), "v"(wm[1]), "v"(wm[2]), "v"(wm[3]), "v"(wm[4]), "v"(wm[5]));
  if (__any(!(o0[8] < 1.0995116e12f))) attn_slow(kp0, vp0, isv, q0, o0, m0);
  if (__any(!(o1[8] < 1.0995116e12f))) attn_slow(kp0, vp0, isv, q1, o1, m1);
  LAS f32x4* cmb = (LAS f32x4*)(c.lds + LD_CMB) + (c.w & 3) * 320 + c.lane;
  if (half == 1) {
    cmb[0] = (f32x4){o0[0], o0[1], o0[2], o0[3]}; cmb[64] = (f32x4){o0[4], o0[5], o0[6], o0[7]};
    cmb[128] = (f32x4){o1[0], o1[1], o1[2], o1[3]}; cmb[192] = (f32x4){o1[4], o1[5], o1[6], o1[7]};
    cmb[256] = (f32x4){o0[8], m0, o1[8], m1};
  }
  STAMP((2 + 5 * l) * 10 + 3);
  BARRIER_LDS();
  STAMP((2 + 5 * l) * 10 + 4);
  if (half == 0) {
    const f32x4 lm = cmb[256];
#pragma unroll
    for (int u = 0; u < 2; ++u) {
      const f32x16& o = u ? o1 : o0; const float m = u ? m1 : m0;
      const f32x8 p2 = cat8(cmb[128 * u], cmb[128 * u + 64]);
      const float m2 = lm[2 * u + 1];
      const float mm = fmaxf(m, m2);
      const float f1 = __builtin_amdgcn_exp2f(m - mm), f2 = __builtin_amdgcn_exp2f(m2 - mm);
      const float inv = __builtin_amdgcn_rcpf(o[8] * f1 + lm[2 * u] * f2);
      const float g1 = f1 * inv, g2 = f2 * inv;
      unsigned p[4];
#pragma unroll
      for (int i = 0; i < 4; ++i) p[i] = pk2(o[2 * i] * g1 + p2[2 * i] * g2, o[2 * i + 1] * g1 + p2[2 * i + 1] * g2);
      st16(c, (u32x4){p[0], p[1], p[2], p[3]}, c.voff, lay + L_O + ((qt + u) * 32 + c.head) * 1024);
    }
    STAMP((2 + 5 * l) * 10 + 6);
    asm volatile("s_waitcnt vmcnt(0)" ::: "memory");
    if (c.lane == 0) __hip_atomic_store(flag_ptr(c, FL_O + l * 32 + c.qb * 4 + c.w, c.head), c.token, RLX_AGENT);
  } else {
    const unsigned src = WS_W1 + (c.nb < 11 ? 3 * c.nb : 33 + 2 * (c.nb - 11)) * 32768;
    help_dma<16>(c, src, LD_W);
    if (c.nb < 11) help_dma<8>(c, src + 65536, LD_W + 65536);
  }
}

struct In { const unsigned short *x, *wqkv, *wo, *w1, *b1, *w2; unsigned short* out; };
struct Jobs { unsigned e[2][8], dst[2], csd[2]; bool ok[2]; };
DI void jobs_load(const Ctx& c, const In& in, Jobs& jb) {
  const int t0 = c.nb < 11 ? 3 * c.nb : 33 + 2 * (c.nb - 11), nt1 = c.nb < 11 ? 3 : 2;
#pragma unroll
  for (int u = 0; u < 2; ++u) {
    const int j = c.w - 1 + 7 * u;
    const unsigned short* W; int N, nt, ks;
    if (j < 6) { W = in.w1; N = F_; jb.ok[u] = j >= 0 && (j >> 1) < nt1; nt = t0 + (jb.ok[u] ? j >> 1 : 0); ks = 2 * c.tb + (j & 1); jb.dst[u] = WS_W1 + (nt * 32 + ks) * 1024; }
    else if (j < 8) { W = in.wo; N = D_; jb.ok[u] = c.w != 0; nt = 2 * c.nb8 + (j & 1); ks = c.tb64; jb.dst[u] = WS_WO + (nt * 32 + ks) * 1024; }
    else { W = in.w2; N = D_; ks = c.tb64 + 32 * ((j - 8) >> 1); jb.ok[u] = ks < KS_F; if (!jb.ok[u]) ks = 0; nt = 2 * c.nb8 + (j & 1); jb.dst[u] = WS_W2 + (nt * KS_F + ks) * 1024; }
    jb.csd[u] = WS_CSP + ((16 + c.nb) * 32 + ks) * 384 + (j >> 1) * 128;
    if (c.w != 0) wfrag_load(W, N, nt, ks, c.r, c.h, jb.e[u]);
  }
}
DI void jobs_store(const Ctx& c, const Jobs& jb) {
#pragma unroll
  for (int u = 0; u < 2; ++u)
    if (jb.ok[u]) wfrag_store(c, jb.e[u], jb.dst[u], c.w - 1 + 7 * u < 6, jb.csd[u]);
}
DI u64 uniform64(u64 v) {
  return ((u64)__builtin_amdgcn_readfirstlane((unsigned)(v >> 32)) << 32) | (unsigned)__builtin_amdgcn_readfirstlane((unsigned)v);
}

DI void run_main(Ctx& c, const In& in) {
  const u64 epoch = __hip_atomic_load((gu64*)c.ws, RLX_AGENT);
  u32x4 res[2];
  {
    if (c.w == 0 && c.lane < 16) ((LAS unsigned*)(c.lds + LD_CTR))[c.lane] = 0u;
    const unsigned short* src = in.x + (size_t)((2 * c.tb64 + (c.w >> 1)) * 32 + c.r) * D_ + 32 * (2 * c.nb8 + (c.w & 1)) + 4 * c.h;
    u32x2 xr[4];
#pragma unroll
    for (int i = 0; i < 4; ++i) xr[i] = *(const u32x2*)(src + 8 * i);
    BARRIER_LDS();
    c.token = uniform64(epoch) + 1;
    res[0] = (u32x4){xr[0][0], xr[0][1], xr[1][0], xr[1][1]}; res[1] = (u32x4){xr[2][0], xr[2][1], xr[3][0], xr[3][1]};
    store_x(c, 0, res);
    publish(c, 0, 4, FL_X + c.tb, c.nb);
    STAMP(1);
    Jobs jb;
    jobs_load(c, in, jb);
    SBAR();
    poll(c, (c.lane & 16) ? FL_WQ + c.nb : FL_X + c.tb, c.lane & 15);
    STAMP(2);
    BARRIER_LDS();
    STAMP(11);
    u32x4 dummy[16];
    fresh(c);
    phase_ln<0, true>(c, 0, 0, dummy);
    jobs_store(c, jb);
  }
  for (int l = 0; l < 2; ++l) {
    u32x4 wo[16], w2r[16];
    if (l == 1) {
      STAMP((1 + 5 * l) * 10 + 1);
      fresh(c);
      u32x4 dummy[16];
      phase_ln<0, false>(c, l, 2 * l, dummy);
    }
    fresh(c);
    STAMP((1 + 5 * l) * 10 + 9);
    poll(c, FL_Q + l * 16 + (c.head >> 1), c.lane & 15);
    BARRIER_LDS();
    STAMP((2 + 5 * l) * 10);
    phase_attn<0>(c, l, wo);
    fresh(c);
    STAMP((2 + 5 * l) * 10 + 9);
    poll(c, FL_O + l * 32 + c.tb64, c.lane & 31);
    BARRIER_LDS();
    STAMP((3 + 5 * l) * 10 + 1);
    phase_wo(c, l, wo, res);
    store_x(c, 2 * l + 1, res);
    STAMP((3 + 5 * l) * 10 + 6);
    publish(c, 8 + l, 4, FL_X + (2 * l + 1) * 16 + c.tb, c.nb);
    fresh(c);
    STAMP((3 + 5 * l) * 10 + 9);
    poll(c, FL_X + (2 * l + 1) * 16 + c.tb, c.lane & 15);
    BARRIER_LDS();
    STAMP((4 + 5 * l) * 10 + 1);
    phase_ln<1, false>(c, l, 2 * l + 1, w2r);
    fresh(c);
    STAMP((4 + 5 * l) * 10 + 9);
    poll(c, FL_H + l * 16 + c.tb, c.lane & 15);
    STAMP((4 + 5 * l) * 10 + 8);
    BARRIER_LDS();
    STAMP((5 + 5 * l) * 10 + 1);
    phase_w2(c, l, w2r, res);
    fresh(c);
    if (l == 0) {
      store_x(c, 2, res);
      STAMP((5 + 5 * l) * 10 + 6);
      publish(c, 10, 4, FL_X + 2 * 16 + c.tb, c.nb);
      STAMP((5 + 5 * l) * 10 + 9);
      poll(c, FL_X + 2 * 16 + c.tb, c.lane & 15);
      STAMP((5 + 5 * l) * 10 + 8);
      BARRIER_LDS();
    }
  }
  unsigned short* op = in.out + (size_t)((2 * c.tb64 + (c.w >> 1)) * 32 + c.r) * D_ + 32 * (2 * c.nb8 + (c.w & 1)) + 4 * c.h;
#pragma unroll
  for (int i = 0; i < 4; ++i) *(u32x2*)(op + 8 * i) = (u32x2){res[i >> 1][2 * (i & 1)], res[i >> 1][2 * (i & 1) + 1]};
  STAMP(109);
  if (c.bid == 0 && c.w == 0 && c.lane == 0) __hip_atomic_store((gu64*)c.ws, c.token, RLX_AGENT);
}

DI void run_help(Ctx& c, const In& in) {
  const u64 epoch = __hip_atomic_load((gu64*)c.ws, RLX_AGENT);
  {
    unsigned e0[8], e1[8];
    const int qk = 2 * c.tb + (c.w & 1), qa = (c.w >> 1) & 1;
    wfrag_load(in.wqkv, 3 * D_, qa * 16 + c.nb, qk, c.r, c.h, e0);
    wfrag_load(in.wqkv, 3 * D_, 32 + c.nb, qk, c.r, c.h, e1);
    const int t = (c.w & 3) * 64 + c.lane;
    const int bcol = 32 * (c.nb < 11 ? 3 * c.nb : 33 + 2 * (c.nb - 11)) + t;
    const unsigned bv = in.b1[bcol < F_ ? bcol : 0];
    BARRIER_LDS();
    c.token = uniform64(epoch) + 1;
    wfrag_store(c, e0, WS_WQKV + ((qa * 16 + c.nb) * 32 + qk) * 1024, true, WS_CSP + (c.nb * 32 + qk) * 384 + qa * 128);
    if (qa == 0) wfrag_store(c, e1, WS_WQKV + ((32 + c.nb) * 32 + qk) * 1024, true, WS_CSP + (c.nb * 32 + qk) * 384 + 256);
    publish(c, 1, 4, FL_WQ + c.nb, c.tb);
    STAMP(1);
    Jobs jb;
    jobs_load(c, in, jb);
    SBAR();
    if (t < 96) ((LAS float*)(c.lds + LD_PAR))[192 + t] = __uint_as_float(bv << 16);
    BARRIER_LDS();
    STAMP(11);
    fresh(c);
#define WQC(q) _Pragma("unroll") for (int t_ = 0; t_ < 3; ++t_) \
    glds16x2(c.ws + WS_WQKV + ((16 * t_ + c.nb) * 32 + 8 * (q) + 2 * (c.w & 3)) * 1024, c.voff, c.lbase + LD_W + t_ * 32768 + (8 * (q) + 2 * (c.w & 3)) * 1024)
    WQC(0); WQC(1);
    asm volatile("s_waitcnt vmcnt(6)\n\ts_barrier" ::: "memory");
    WQC(2);
    asm volatile("s_waitcnt vmcnt(6)\n\ts_barrier" ::: "memory");
    WQC(3);
    asm volatile("s_waitcnt vmcnt(6)\n\ts_barrier" ::: "memory");
    asm volatile("s_waitcnt vmcnt(0)\n\ts_barrier" ::: "memory");
#undef WQC
    help_colsum(c, 0);
    STAMP(12);
    help_ln<0, true>(c, 0);
    jobs_store(c, jb);
  }
  for (int l = 0; l < 2; ++l) {
    u32x4 wo[16];
    fresh(c);
    if (l == 1) help_ln<0, false>(c, l);
    STAMP((1 + 5 * l) * 10 + 9);
    BARRIER_LDS();
    STAMP((2 + 5 * l) * 10);
    fresh(c);
    phase_attn<1>(c, l, wo);
    fresh(c);
    STAMP((2 + 5 * l) * 10 + 9);
    BARRIER_LDS();
    BARRIER_LDS();
    if (l == 0) help_colsum(c, 1);
    BARRIER_ALL();
    STAMP((4 + 5 * l) * 10 + 1);
    dma_w2<42, 56>(c);
    STAMP((4 + 5 * l) * 10 + 2);
    help_ln<1, false>(c, l);
    STAMP((4 + 5 * l) * 10 + 6);
    dma_w2<0, 42>(c);
    STAMP((4 + 5 * l) * 10 + 9);
    BARRIER_ALL();
    STAMP((5 + 5 * l) * 10 + 1);
    BARRIER_LDS();
    BARRIER_LDS();
    if (l == 0) {
      fresh(c);
      dma_wqkv(c);
      STAMP((5 + 5 * l) * 10 + 9);
      BARRIER_ALL();
    }
  }
}

__global__ __launch_bounds__(512, 2) void mega(const unsigned short* __restrict__ x, const unsigned short* __restrict__ wqkv,
                                               const unsigned short* __restrict__ wo_g, const unsigned short* __restrict__ w1,
                                               const unsigned short* __restrict__ b1, const unsigned short* __restrict__ w2,
                                               char* __restrict__ ws, unsigned short* __restrict__ out) {
  extern __shared__ __attribute__((aligned(16))) char lds_[];
  Ctx c;
  c.ws = ws; c.lds = (LAS char*)lds_; c.rs = __builtin_amdgcn_make_buffer_rsrc(ws, 0, WS_END, 0x00020000);
  c.lbase = (unsigned)(size_t)c.lds;
  c.bid = blockIdx.x; c.lane = threadIdx.x & 63; c.w = __builtin_amdgcn_readfirstlane(threadIdx.x >> 6); c.dead = false;
  fresh(c);
  STAMP(0);
  const In in = {x, wqkv, wo_g, w1, b1, w2, out};
  if (c.w < 4) run_main(c, in); else run_help(c, in);
}

extern "C" void kernel_launch(void* const* d_in, const int* in_sizes, int n_in, void* d_out, int out_size, void* d_ws,
                              size_t ws_size, hipStream_t stream) {
  (void)hipFuncSetAttribute((const void*)mega, hipFuncAttributeMaxDynamicSharedMemorySize, LDS_BYTES);
  mega<<<256, 512, LDS_BYTES, stream>>>((const unsigned short*)d_in[0], (const unsigned short*)d_in[1], (const unsigned short*)d_in[2],
                                        (const unsigned short*)d_in[4], (const unsigned short*)d_in[5], (const unsigned short*)d_in[6],
                                        (char*)d_ws, (unsigned short*)d_out);
}
```

```cpp
#include <hip/hip_runtime.h>
#include <hip/hip_bf16.h>

typedef __attribute__((ext_vector_type(2))) __bf16 bf16x2;
typedef __attribute__((ext_vector_type(8))) __bf16 bf16x8;
typedef __attribute__((ext_vector_type(16))) __bf16 bf16x16;
typedef __attribute__((ext_vector_type(16))) float f32x16;
typedef __attribute__((ext_vector_type(8))) float f32x8;
typedef __attribute__((ext_vector_type(4))) float f32x4;
typedef __attribute__((ext_vector_type(2))) float f32x2;
typedef __attribute__((ext_vector_type(4))) unsigned u32x4;
typedef __attribute__((ext_vector_type(2))) unsigned u32x2;
typedef unsigned long long u64;
#define LAS __attribute__((address_space(3)))
#define GAS __attribute__((address_space(1)))
typedef GAS u64 gu64;
#define DI __device__ __forceinline__
#define RLX_AGENT __ATOMIC_RELAXED, __HIP_MEMORY_SCOPE_AGENT

constexpr int S_ = 2048, D_ = 512, F_ = 1376;
constexpr int KS_F = F_ / 16;

constexpr unsigned MB = 1u << 20;
constexpr unsigned WS_FLAG = 0;
constexpr unsigned WS_WQKV = 1024 * 1024;
constexpr unsigned WS_WO = WS_WQKV + 48 * 32 * 1024;
constexpr unsigned WS_W1 = WS_WO + 16 * 32 * 1024;
constexpr unsigned WS_W2 = WS_W1 + 43 * 32 * 1024;
constexpr unsigned WS_CSP = WS_W2 + 16 * 86 * 1024;
constexpr unsigned WS_ST = WS_CSP + 2 * 16 * 32 * 384;
constexpr unsigned WS_X = WS_ST + 4 * 64 * 4096;
constexpr unsigned WS_L = WS_X + 8 * MB;
constexpr unsigned L_Q = 0, L_K = 2 * MB, L_V = 4 * MB, L_O = 6 * MB, L_H = 8 * MB, L_SZ = 14 * MB;
constexpr unsigned WS_END = WS_L + 2 * L_SZ;
constexpr int FL_X = 1;
constexpr int FL_H = FL_X + 1024;
constexpr int FL_Q = FL_H + 512;
constexpr int FL_O = FL_Q + 512;
constexpr int FL_WQ = FL_O + 512;
constexpr int FL_W1 = FL_WQ + 256;
constexpr int FL_W2 = FL_W1 + 16;
static_assert((FL_W2 + 8) * 256 <= WS_WQKV, "flag region");

constexpr int LD_W = 0;
constexpr int LD_V = 65536;
constexpr int LD_EX = 98304;
constexpr int LD_ONE = 131072;
constexpr int LD_CMB = 135168;
constexpr int LD_W2E = 122880;
constexpr int LD_SC = 151552;
constexpr int LD_ST = 155648;
constexpr int LD_PAR = 157696;
constexpr int LD_CTR = LD_PAR + 1280;
constexpr int LD_DBG = LD_CTR + 512;
constexpr int LDS_BYTES = 163840;

#define MFMA(a, b, c) __builtin_amdgcn_mfma_f32_32x32x16_bf16((a), (b), (c), 0, 0, 0)
#define SBAR() __builtin_amdgcn_sched_barrier(0)
#define PIN(x) asm volatile("" : "+v"(x))
#define BARRIER_LDS() asm volatile("s_waitcnt lgkmcnt(0)\n\ts_barrier" ::: "memory")
#define BARRIER_ALL() asm volatile("s_waitcnt vmcnt(0) lgkmcnt(0)\n\ts_barrier" ::: "memory")

#define STAMP(i)

DI unsigned pk2(float lo, float hi) {
  bf16x2 v; v[0] = (__bf16)lo; v[1] = (__bf16)hi;
  return __builtin_bit_cast(unsigned, v);
}
DI float bf_lo(unsigned u) { return __uint_as_float(u << 16); }
DI float bf_hi(unsigned u) { return __uint_as_float(u & 0xffff0000u); }
DI float rbf(float x) { return (float)(__bf16)x; }
DI bf16x8 as_bf(u32x4 v) { return __builtin_bit_cast(bf16x8, v); }
DI float swap_half_sum(float x) {
  auto r = __builtin_amdgcn_permlane32_swap(__float_as_uint(x), __float_as_uint(x), false, false);
  return __uint_as_float(r[0]) + __uint_as_float(r[1]);
}
DI float swap_half_max(float x) {
  auto r = __builtin_amdgcn_permlane32_swap(__float_as_uint(x), __float_as_uint(x), false, false);
  return fmaxf(__uint_as_float(r[0]), __uint_as_float(r[1]));
}
DI void glds16(const char* sbase, unsigned voff, unsigned lds_base) {
  unsigned sv;
  asm volatile("s_mov_b32 %0, m0\n\ts_mov_b32 m0, %3\n\ts_nop 2\n\tglobal_load_lds_dwordx4 %1, %2 sc1\n\ts_mov_b32 m0, %0"
               : "=&s"(sv) : "v"(voff), "s"(sbase), "s"(lds_base) : "memory");
}
DI void glds16x2(const char* sbase, unsigned voff, unsigned lds_base) {
  unsigned sv;
  asm volatile("s_mov_b32 %0, m0\n\ts_mov_b32 m0, %3\n\ts_nop 2\n\tglobal_load_lds_dwordx4 %1, %2 sc1\n\t"
               "global_load_lds_dwordx4 %1, %2 offset:1024 sc1\n\ts_mov_b32 m0, %0"
               : "=&s"(sv) : "v"(voff), "s"(sbase), "s"(lds_base) : "memory");
}
DI void glds16x4(const char* sbase, unsigned voff, unsigned lds_base) {
  unsigned sv;
  asm volatile("s_mov_b32 %0, m0\n\ts_mov_b32 m0, %3\n\ts_nop 2\n\tglobal_load_lds_dwordx4 %1, %2 sc1\n\t"
               "global_load_lds_dwordx4 %1, %2 offset:1024 sc1\n\tglobal_load_lds_dwordx4 %1, %2 offset:2048 sc1\n\t"
               "global_load_lds_dwordx4 %1, %2 offset:3072 sc1\n\ts_mov_b32 m0, %0"
               : "=&s"(sv) : "v"(voff), "s"(sbase), "s"(lds_base) : "memory");
}
template <int I> struct IC { static constexpr int v = I; };
template <int I, int N, class F> DI void static_for(F&& f) {
  if constexpr (I < N) { f(IC<I>{}); static_for<I + 1, N>(f); }
}

struct Ctx {
  char* ws; LAS char* lds; __amdgpu_buffer_rsrc_t rs;
  u64 token; unsigned lbase, voff;
  int bid, lane, w, r, h, tb, nb, tb64, nb8, head, qb;
  bool dead;
};
DI void fresh(Ctx& c) {
  asm volatile("" : "+s"(c.bid));
  asm volatile("" : "+s"(c.w));
  asm volatile("" : "+v"(c.lane));
  const int g = c.bid & 7, idx = c.bid >> 3;
  c.r = c.lane & 31; c.h = c.lane >> 5; c.voff = c.lane * 16;
  c.tb = 2 * g + (idx >> 4); c.nb = idx & 15; c.tb64 = 4 * g + (idx >> 3); c.nb8 = idx & 7;
  c.head = 4 * g + (idx & 3); c.qb = idx >> 2;
}
DI u32x4 ld16(const Ctx& c, unsigned voff, unsigned soff) { return __builtin_amdgcn_raw_buffer_load_b128(c.rs, voff, soff, 16); }
DI float ld4(const Ctx& c, unsigned voff, unsigned soff) { return __uint_as_float(__builtin_amdgcn_raw_buffer_load_b32(c.rs, voff, soff, 16)); }
DI void st16(const Ctx& c, u32x4 v, unsigned voff, unsigned soff) { __builtin_amdgcn_raw_buffer_store_b128(v, c.rs, voff + soff, 0, 16); }
DI void st4(const Ctx& c, float v, unsigned voff, unsigned soff) { __builtin_amdgcn_raw_buffer_store_b32(__float_as_uint(v), c.rs, voff, soff, 16); }
DI gu64* flag_ptr(const Ctx& c, int line, int slot) { return (gu64*)(c.ws + WS_FLAG) + line * 32 + slot; }

DI void publish(const Ctx& c, int ctr, unsigned n, int line0, int slot) {
  asm volatile("s_waitcnt vmcnt(0)" ::: "memory");
  unsigned old = 0;
  if (c.lane == 0) old = __hip_atomic_fetch_add((LAS unsigned*)(c.lds + LD_CTR) + ctr, 1u, __ATOMIC_RELAXED, __HIP_MEMORY_SCOPE_WORKGROUP);
  if (__builtin_amdgcn_readfirstlane(old) == n - 1 && c.lane < 16) __hip_atomic_store(flag_ptr(c, line0 + c.lane, slot), c.token, RLX_AGENT);
}
DI void poll_any(Ctx& c, int line, int slot) {
  if (!c.dead) {
    gu64* p = flag_ptr(c, line, slot);
    unsigned spins = 0;
    while (!__all(__hip_atomic_load(p, RLX_AGENT) == c.token)) {
      if (++spins > (1u << 18)) { c.dead = true; break; }
    }
    __builtin_amdgcn_fence(__ATOMIC_ACQUIRE, "wavefront");
  }
}
template <bool NAP = false> DI void wait4(const Ctx& c, int ctr) {
  while (__hip_atomic_load((LAS unsigned*)(c.lds + LD_CTR) + ctr, __ATOMIC_RELAXED, __HIP_MEMORY_SCOPE_WORKGROUP) < 4u)
    if (NAP) __builtin_amdgcn_s_sleep(2);
  asm volatile("" ::: "memory");
}
DI void arrive4(const Ctx& c, int ctr) {
  asm volatile("s_waitcnt lgkmcnt(0)" ::: "memory");
  if (c.lane == 0) __hip_atomic_fetch_add((LAS unsigned*)(c.lds + LD_CTR) + ctr, 1u, __ATOMIC_RELAXED, __HIP_MEMORY_SCOPE_WORKGROUP);
}
DI void poll(Ctx& c, int line, int slot) { if (c.w == 0) poll_any(c, line, slot); }
template <int NQ> DI void help_dma(const Ctx& c, unsigned src, int dst) {
#pragma unroll
  for (int u = 0; u < NQ / 4; ++u) {
    const int p = (c.w & 3) + 4 * u;
    glds16x4(c.ws + src + p * 4096, c.voff, c.lbase + dst + p * 4096);
  }
}
DI int w2_run(int run) { return run < 6 ? LD_W + run * 14336 : LD_W2E + (run - 6) * 14336; }
template <int U0, int U1> DI void w2_fetch(const Ctx& c, u32x4 (&h)[28]) {
#pragma unroll
  for (int u = U0; u < U1; ++u) {
    const int f = (c.w & 3) + 4 * u, run = f / 14, kq = run >> 1;
    h[u] = ld16(c, c.voff, WS_W2 + ((2 * c.nb8 + (run & 1)) * KS_F + kq * 21 + (kq < 2 ? kq : 2) + f % 14) * 1024);
  }
  SBAR();
}
template <int U0, int U1> DI void w2_put(const Ctx& c, const u32x4 (&h)[28]) {
#pragma unroll
  for (int u = U0; u < U1; ++u) {
    const int f = (c.w & 3) + 4 * u;
    *(LAS u32x4*)(c.lds + (u < 21 ? LD_W : LD_W2E - 84 * 1024) + f * 1024 + c.voff) = h[u];
  }
}

DI void store_x(const Ctx& c, int inst, const u32x4 (&res)[2]) {
  const int tt = 2 * c.tb64 + (c.w >> 1), nt = 2 * c.nb8 + (c.w & 1);
  st16(c, res[0], c.voff, WS_X + inst * (2 * MB) + (tt * 32 + 2 * nt) * 1024);
  st16(c, res[1], c.voff, WS_X + inst * (2 * MB) + (tt * 32 + 2 * nt + 1) * 1024);
  float s1 = 0.f, s2 = 0.f;
#pragma unroll
  for (int i = 0; i < 8; ++i) {
    const unsigned u = res[i >> 2][i & 3];
    const float a = bf_lo(u), b = bf_hi(u);
    s1 += a + b; s2 = fmaf(a, a, fmaf(b, b, s2));
  }
  auto r1 = __builtin_amdgcn_permlane32_swap(__float_as_uint(s1), __float_as_uint(s2), false, false);
  st4(c, __uint_as_float(r1[0]) + __uint_as_float(r1[1]), c.lane * 4, WS_ST + inst * (64 * 4096) + tt * 4096 + nt * 256);
}

DI void wfrag_load(const unsigned short* W, int N, int nt, int ks, int r, int h, unsigned (&e)[8]) {
#pragma unroll
  for (int j = 0; j < 8; ++j) e[j] = W[(size_t)(16 * ks + 8 * (j >> 2) + 4 * h + (j & 3)) * N + 32 * nt + r];
}
DI void wfrag_store(const Ctx& c, const unsigned (&e)[8], unsigned dst, bool docs, unsigned csdst) {
  u32x4 v;
#pragma unroll
  for (int j = 0; j < 4; ++j) v[j] = e[2 * j] | (e[2 * j + 1] << 16);
  st16(c, v, c.voff, dst);
  if (docs) {
    float s = 0.f;
#pragma unroll
    for (int j = 0; j < 8; ++j) s += __uint_as_float(e[j] << 16);
    s = swap_half_sum(s);
    if (c.h == 0) st4(c, s, c.r * 4, csdst);
  }
}

DI float mish_f(float hh) {
  const float u = __builtin_amdgcn_exp2f(fminf(hh, 30.f) * 1.4426950408889634f);
  const float n = u * (u + 2.f);
  return hh * n * __builtin_amdgcn_rcpf(n + 2.f);
}
template <int MODE>
DI u32x2 epi4(float a0, float a1, float a2, float a3, f32x4 cs, f32x4 bb, float mr, float rstd) {
  const float a[4] = {a0, a1, a2, a3};
  float v[4];
#pragma unroll
  for (int j = 0; j < 4; ++j) {
    if (MODE == 0) v[j] = fmaf(a[j], rstd, mr * cs[j]);
    else v[j] = mish_f(fmaf(a[j], rstd, fmaf(mr, cs[j], bb[j])));
  }
  return (u32x2){pk2(v[0], v[1]), pk2(v[2], v[3])};
}
template <int MODE>
DI u32x4 epi8(const f32x8& a, const f32x4* cs, const f32x4* bb, float mr, float rstd) {
  const u32x2 lo = epi4<MODE>(a[0], a[1], a[2], a[3], cs[0], bb[0], mr, rstd);
  const u32x2 hi = epi4<MODE>(a[4], a[5], a[6], a[7], cs[1], bb[1], mr, rstd);
  return (u32x4){lo[0], lo[1], hi[0], hi[1]};
}
#define LO8(s) __builtin_shufflevector(s, s, 0, 1, 2, 3, 4, 5, 6, 7)
#define HI8(s) __builtin_shufflevector(s, s, 8, 9, 10, 11, 12, 13, 14, 15)
DI f32x4 q4(const f32x8& v, int g) { return g ? (f32x4){v[4], v[5], v[6], v[7]} : (f32x4){v[0], v[1], v[2], v[3]}; }
DI f32x8 cat8(f32x4 a, f32x4 b) { return __builtin_shufflevector(a, b, 0, 1, 2, 3, 4, 5, 6, 7); }

struct LnPar { f32x4 cs[6], bb[6]; float csv; };
template <int MODE, int T> DI void ln_params(const Ctx& c, LnPar& p) {
  const LAS float* par = (const LAS float*)(c.lds + LD_PAR) + MODE * 96 + 4 * c.h;
#pragma unroll
  for (int g = 0; g < 6; ++g) {
    const int o = g < 4 ? 32 * T + 8 * g : 64 + 16 * T + 8 * (g - 4);
    if (MODE == 1 || g < 4) p.cs[g] = *(const LAS f32x4*)(par + o);
    if (MODE == 1) p.bb[g] = *(const LAS f32x4*)(par + 96 + o);
  }
  if (MODE == 0) p.csv = ((const LAS float*)(c.lds + LD_PAR))[64 + c.r];
}
template <int T> DI void ln_tokstats(const Ctx& c, f32x4 (&ts)[4]) {
  const LAS f32x2* st = (const LAS f32x2*)(c.lds + LD_ST) + (c.w & 3) * 32 + 16 * T + 4 * c.h;
#pragma unroll
  for (int g = 0; g < 4; ++g) ts[g] = *(const LAS f32x4*)(st + 8 * (g >> 1) + 2 * (g & 1));
}
template <int MODE, int T>
DI void ln_epilogue(Ctx& c, int l, const f32x16& a, const f32x8& ch, float mean, float rstd, const LnPar& p, const f32x4 (&ts)[4]) {
  const int tt = 4 * c.tb + (c.w & 3);
  const unsigned lay = WS_L + l * L_SZ;
  if (MODE == 0) {
    const float rs = T ? rstd : rstd * (0.25f * 1.4426950408889634f), mr = -mean * rs;
    const unsigned vo = T ? c.h * 512 + ((c.r & 19) | ((c.r & 4) << 1) | ((c.r & 8) >> 1)) * 16 : c.voff;
    const unsigned so = lay + (T ? L_K : L_Q) + (2 * c.nb * 64 + tt) * 1024;
    st16(c, epi8<0>(LO8(a), p.cs, p.cs, mr, rs), vo, so);
    st16(c, epi8<0>(HI8(a), p.cs + 2, p.cs, mr, rs), vo, so + 65536);
    unsigned q[4];
#pragma unroll
    for (int g = 0; g < 4; ++g) q[g] = pk2((ch[2 * g] - ts[g][0] * p.csv) * ts[g][1], (ch[2 * g + 1] - ts[g][2] * p.csv) * ts[g][3]);
    st16(c, (u32x4){q[0], q[1], q[2], q[3]}, (c.r >> 4) * 65536 + c.h * 256 + (c.r & 15) * 16, lay + L_V + (2 * c.nb * 128 + 2 * tt + T) * 512);
    publish(c, 4 + l, 8, FL_Q + l * 256 + c.nb * 16, c.tb);
  } else {
    const float mr = -mean * rstd;
    const unsigned so = lay + L_H + (tt * KS_F + 2 * (c.nb < 11 ? 3 * c.nb : 33 + 2 * (c.nb - 11))) * 1024;
    st16(c, epi8<1>(LO8(a), p.cs, p.bb, mr, rstd), c.voff, so + T * 2048);
    st16(c, epi8<1>(HI8(a), p.cs + 2, p.bb + 2, mr, rstd), c.voff, so + T * 2048 + 1024);
    if (c.nb < 11) st16(c, epi8<1>(ch, p.cs + 4, p.bb + 4, mr, rstd), c.voff, so + 4096 + T * 1024);
    publish(c, 6 + l, 8, FL_H + l * 256 + c.tb * 16, c.nb);
  }
}

DI void pin16(f32x16& v) { PIN(v); }
DI u32x4 wfrag(const LAS char* base, int ks) { return *(const LAS u32x4*)(base + ks * 1024); }

template <int MODE, bool FIRST>
DI void phase_ln(Ctx& c, int l, int inst, u32x4 (&w2r)[16]) {
  const int PH = (1 + 5 * l + (MODE ? 3 : 0)) * 10; (void)PH;
  const int tt = 4 * c.tb + c.w;
  const bool has3 = MODE == 0 || c.nb < 11;
  const unsigned xs = WS_X + inst * (2 * MB) + tt * 32768;
  constexpr int PD = 12;
  u32x4 xf[PD], sp[4];
#pragma unroll
  for (int k = 0; k < PD; ++k) xf[k] = ld16(c, c.voff, xs + k * 1024);
  SBAR();
  const LAS char* wl = c.lds + LD_W + c.lane * 16;
  const LAS char* wl2 = wl + 65536;
  asm volatile("" : "+v"(wl2));
  f32x16 aA, aB, aC;
#pragma unroll
  for (int i = 0; i < 16; ++i) { aA[i] = 0.f; aB[i] = 0.f; aC[i] = 0.f; }
  u32x4 fa[3], fb[3], fc[3];
  if (FIRST) BARRIER_LDS();
#pragma unroll
  for (int k = 0; k < 2; ++k) { fa[k] = wfrag(wl, k); fb[k] = wfrag(wl + 32768, k); fc[k] = wfrag(wl2, k); }
  float mean = 0.f, rstd = 0.f, sc4[4];
  LnPar par; f32x4 ts[4];
  LAS float* sc = (LAS float*)(c.lds + LD_SC) + c.w * 256;
  const int k0q = c.w * 21 + (c.w < 2 ? c.w : 2);
  const unsigned w2s = WS_W2 + ((2 * c.nb8 + (c.w & 1)) * KS_F + k0q + 14) * 1024;
  const unsigned w2o = WS_W2 + ((2 * c.nb8 + 1 - (c.w & 1)) * KS_F + k0q + 14) * 1024;
  static_for<0, 32>([&](auto kc) __attribute__((always_inline)) {
    constexpr int k = decltype(kc)::v;
    if constexpr (FIRST && (k & 7) == 6 && k < 24) asm volatile("s_barrier" ::: "memory");
    aA = MFMA(as_bf(fa[k % 3]), as_bf(xf[k % PD]), aA);
    pin16(aA); SBAR();
    if constexpr (k + 2 < 32) fa[(k + 2) % 3] = wfrag(wl, k + 2);
    if constexpr (k >= 1 && k - 1 + PD < 32) xf[(k - 1) % PD] = ld16(c, c.voff, xs + (k - 1 + PD) * 1024);
    if constexpr (k >= 21 && k < 23) {
      sp[2 * k - 42] = ld16(c, c.voff, WS_ST + inst * (64 * 4096) + tt * 4096 + (2 * k - 42) * 1024);
      sp[2 * k - 41] = ld16(c, c.voff, WS_ST + inst * (64 * 4096) + tt * 4096 + (2 * k - 41) * 1024);
    }
    SBAR();
    aB = MFMA(as_bf(fb[k % 3]), as_bf(xf[k % PD]), aB);
    pin16(aB); SBAR();
    if constexpr (k + 2 < 32) fb[(k + 2) % 3] = wfrag(wl + 32768, k + 2);
    if constexpr (MODE == 1 && k >= 23 && k < 31) {
      constexpr int i = k - 23;
      const int kk = (i == 7 && c.w >= 2) ? 6 : i;
      w2r[2 * i] = ld16(c, c.voff, w2s + kk * 1024); w2r[2 * i + 1] = ld16(c, c.voff, w2o + kk * 1024);
    }
    if constexpr (k == 28) {
      ((LAS f32x4*)sc)[c.lane] = (__builtin_bit_cast(f32x4, sp[0]) + __builtin_bit_cast(f32x4, sp[1])) +
                                 (__builtin_bit_cast(f32x4, sp[2]) + __builtin_bit_cast(f32x4, sp[3]));
#pragma unroll
      for (int i = 0; i < 4; ++i) sc4[i] = sc[i * 64 + c.lane];
    }
    if constexpr (k == 30) {
      const float s = (sc4[0] + sc4[1]) + (sc4[2] + sc4[3]);
      auto rr = __builtin_amdgcn_permlane32_swap(__float_as_uint(s), __float_as_uint(s), false, false);
      const float mu = __uint_as_float(rr[0]) * (1.f / D_);
      const float var = fmaxf(__uint_as_float(rr[1]) * (1.f / D_) - mu * mu, 0.f);
      mean = rbf(mu);
      rstd = __builtin_amdgcn_rcpf(rbf(__builtin_amdgcn_sqrtf(rbf(rbf(var) + 1e-5f))));
      ((LAS f32x2*)(c.lds + LD_ST))[c.w * 32 + c.r] = (f32x2){mean, rstd};
    }
    SBAR();
    if (has3) aC = MODE == 0 ? MFMA(as_bf(xf[k % PD]), as_bf(fc[k % 3]), aC) : MFMA(as_bf(fc[k % 3]), as_bf(xf[k % PD]), aC);
    pin16(aC); SBAR();
    if constexpr (k + 2 < 32) fc[(k + 2) % 3] = wfrag(wl2, k + 2);
    SBAR();
  });
  STAMP(PH + 2);
  if (MODE == 0) ln_tokstats<0>(c, ts);
  if (!FIRST) ln_params<MODE, 0>(c, par);
  SBAR();
  {
    LAS f32x4* ex = (LAS f32x4*)(c.lds + LD_EX) + c.w * 384 + c.lane;
#pragma unroll
    for (int g = 0; g < 4; ++g) ex[g * 64] = (f32x4){aB[4 * g], aB[4 * g + 1], aB[4 * g + 2], aB[4 * g + 3]};
    ex[256] = (f32x4){aC[8], aC[9], aC[10], aC[11]}; ex[320] = (f32x4){aC[12], aC[13], aC[14], aC[15]};
  }
  BARRIER_LDS();
  STAMP(PH + 4);
  if (FIRST) ln_params<MODE, 0>(c, par);
  ln_epilogue<MODE, 0>(c, l, aA, LO8(aC), mean, rstd, par, ts);
}
template <int MODE, bool FIRST>
DI void help_ln(Ctx& c, int l) {
  LnPar par; f32x4 ts[4];
  if (!FIRST) ln_params<MODE, 1>(c, par);
  BARRIER_LDS();
  if (FIRST) ln_params<MODE, 1>(c, par);
  const LAS f32x4* ex = (const LAS f32x4*)(c.lds + LD_EX) + (c.w & 3) * 384 + c.lane;
  const f32x2 ms = ((const LAS f32x2*)(c.lds + LD_ST))[(c.w & 3) * 32 + c.r];
  const f32x8 lo = cat8(ex[0], ex[64]), hi = cat8(ex[128], ex[192]), ch = cat8(ex[256], ex[320]);
  if (MODE == 0) ln_tokstats<1>(c, ts);
  SBAR();
  const f32x16 a = __builtin_shufflevector(lo, hi, 0, 1, 2, 3, 4, 5, 6, 7, 8, 9, 10, 11, 12, 13, 14, 15);
  ln_epilogue<MODE, 1>(c, l, a, ch, ms[0], ms[1], par, ts);
}
DI void help_colsum(const Ctx& c, int mode) {
  const int t = (c.w - 4) * 64 + c.lane;
  if (t < 96) {
    float v[32];
#pragma unroll
    for (int i = 0; i < 32; ++i) v[i] = ld4(c, t * 4, WS_CSP + ((mode * 16 + c.nb) * 32 + i) * 384);
    float s = 0.f;
#pragma unroll
    for (int i = 0; i < 32; i += 4) s += (v[i] + v[i + 1]) + (v[i + 2] + v[i + 3]);
    ((LAS float*)(c.lds + LD_PAR))[mode * 96 + t] = s;
  }
}

DI void reduce_res(Ctx& c, const f32x16 (&a)[4], u32x4 (&res)[2], int ctr, int PH) {
  LAS f32x4* red = (LAS f32x4*)(c.lds + LD_EX) + c.lane;
#pragma unroll
  for (int j = 1; j < 4; ++j) {
    LAS f32x4* p = red + ((c.w ^ j) * 3 + j - 1) * 256;
#pragma unroll
    for (int g = 0; g < 4; ++g) p[g * 64] = (f32x4){a[j][4 * g], a[j][4 * g + 1], a[j][4 * g + 2], a[j][4 * g + 3]};
  }
  STAMP(PH + 3);
  if (ctr >= 0) { arrive4(c, ctr); wait4(c, ctr); } else BARRIER_LDS();
  STAMP(PH + 4);
  const LAS f32x4* q = red + c.w * 768;
#pragma unroll
  for (int g = 0; g < 4; ++g) {
    const f32x4 s = ((f32x4){a[0][4 * g], a[0][4 * g + 1], a[0][4 * g + 2], a[0][4 * g + 3]} + q[g * 64]) + (q[256 + g * 64] + q[512 + g * 64]);
    const unsigned u0 = res[g >> 1][2 * (g & 1)], u1 = res[g >> 1][2 * (g & 1) + 1];
    res[g >> 1][2 * (g & 1)] = pk2(bf_lo(u0) + rbf(s[0]), bf_hi(u0) + rbf(s[1]));
    res[g >> 1][2 * (g & 1) + 1] = pk2(bf_lo(u1) + rbf(s[2]), bf_hi(u1) + rbf(s[3]));
  }
}
#define MM4(w0, w1, x0, x1) do { a[0] = MFMA(as_bf(w0), as_bf(x0), a[0]); a[1] = MFMA(as_bf(w1), as_bf(x0), a[1]); \
                                 a[2] = MFMA(as_bf(w0), as_bf(x1), a[2]); a[3] = MFMA(as_bf(w1), as_bf(x1), a[3]); } while (0)

DI void phase_wo(Ctx& c, int l, const u32x4 (&wo)[16], u32x4 (&res)[2]) {
  const unsigned xs = WS_L + l * L_SZ + L_O + ((2 * c.tb64 + (c.w >> 1)) * 32 + 8 * c.w) * 1024;
  const unsigned xo = WS_L + l * L_SZ + L_O + ((2 * c.tb64 + 1 - (c.w >> 1)) * 32 + 8 * c.w) * 1024;
  u32x4 xf[16];
#pragma unroll
  for (int k = 0; k < 8; ++k) { xf[2 * k] = ld16(c, c.voff, xs + k * 1024); xf[2 * k + 1] = ld16(c, c.voff, xo + k * 1024); }
  asm volatile("s_barrier" ::: "memory");
  f32x16 a[4];
#pragma unroll
  for (int i = 0; i < 64; ++i) a[i >> 4][i & 15] = 0.f;
#pragma unroll
  for (int k = 0; k < 8; ++k) { MM4(wo[2 * k], wo[2 * k + 1], xf[2 * k], xf[2 * k + 1]); PIN(a[0]); PIN(a[1]); PIN(a[2]); PIN(a[3]); SBAR(); }
  STAMP((3 + 5 * l) * 10 + 2);
  reduce_res(c, a, res, -1, (3 + 5 * l) * 10);
}

DI void phase_w2(Ctx& c, int l, const u32x4 (&w2r)[16], u32x4 (&res)[2]) {
  const int k0 = c.w * 21 + (c.w < 2 ? c.w : 2);
  const unsigned xs = WS_L + l * L_SZ + L_H + ((2 * c.tb64 + (c.w >> 1)) * KS_F + k0) * 1024;
  const unsigned xo = WS_L + l * L_SZ + L_H + ((2 * c.tb64 + 1 - (c.w >> 1)) * KS_F + k0) * 1024;
  constexpr int WIN = 10;
  u32x4 xf[2 * WIN];
#pragma unroll
  for (int k = 0; k < WIN; ++k) { xf[2 * k] = ld16(c, c.voff, xs + k * 1024); xf[2 * k + 1] = ld16(c, c.voff, xo + k * 1024); }
  SBAR();
  const LAS u32x4* w0 = (const LAS u32x4*)(c.lds + w2_run(2 * c.w + (c.w & 1))) + c.lane;
  const LAS u32x4* w1 = (const LAS u32x4*)(c.lds + w2_run(2 * c.w + 1 - (c.w & 1))) + c.lane;
  f32x16 a[4];
#pragma unroll
  for (int i = 0; i < 64; ++i) a[i >> 4][i & 15] = 0.f;
  u32x4 f0[2], f1[2];
  f0[0] = w0[0]; f1[0] = w1[0];
  static_for<0, 22>([&](auto kc) __attribute__((always_inline)) {
    constexpr int k = decltype(kc)::v;
    const u32x4 g0 = k < 14 ? f0[k & 1] : w2r[k < 14 ? 0 : 2 * (k - 14)], g1 = k < 14 ? f1[k & 1] : w2r[k < 14 ? 0 : 2 * (k - 14) + 1];
    const u32x4 x0 = xf[2 * (k % WIN)], x1 = xf[2 * (k % WIN) + 1];
    constexpr int kn = k - 1 + WIN;
    const int kk = (kn == 21 && c.w >= 2) ? 20 : kn;
    if constexpr (kn == 22) asm volatile("s_barrier" ::: "memory");
    if (k < 21 || c.w < 2) {
      a[0] = MFMA(as_bf(g0), as_bf(x0), a[0]);
      pin16(a[0]); SBAR();
      if constexpr (k >= 1 && kn < 22) xf[2 * ((k - 1) % WIN)] = ld16(c, c.voff, xs + kk * 1024);
      SBAR();
      a[1] = MFMA(as_bf(g1), as_bf(x0), a[1]);
      pin16(a[1]); SBAR();
      if constexpr (k + 1 < 14) f0[(k + 1) & 1] = w0[(k + 1) * 64];
      SBAR();
      a[2] = MFMA(as_bf(g0), as_bf(x1), a[2]);
      pin16(a[2]); SBAR();
      if constexpr (k >= 1 && kn < 22) xf[2 * ((k - 1) % WIN) + 1] = ld16(c, c.voff, xo + kk * 1024);
      SBAR();
      a[3] = MFMA(as_bf(g1), as_bf(x1), a[3]);
      pin16(a[3]); SBAR();
      if constexpr (k + 1 < 14) f1[(k + 1) & 1] = w1[(k + 1) * 64];
      SBAR();
    }
  });
  STAMP((5 + 5 * l) * 10 + 2);
  if (l == 0) { arrive4(c, 16); wait4(c, 16); } else BARRIER_LDS();
  reduce_res(c, a, res, l == 0 ? 17 : -1, (5 + 5 * l) * 10);
}

#define SMF(a, b8, c) __builtin_amdgcn_smfmac_f32_32x32x32_bf16((a), __builtin_bit_cast(bf16x16, b8), (c), sidx, 0, 0)
#define LDF(p) (*(const LAS bf16x8*)(p))
DI float max16(const f32x16& s) {
  float a = fmaxf(fmaxf(s[0], s[1]), s[2]), b = fmaxf(fmaxf(s[3], s[4]), s[5]);
  a = fmaxf(fmaxf(a, s[6]), s[7]); b = fmaxf(fmaxf(b, s[8]), s[9]);
  a = fmaxf(fmaxf(a, s[10]), s[11]); b = fmaxf(fmaxf(b, s[12]), s[13]);
  a = fmaxf(fmaxf(a, s[14]), s[15]);
  return swap_half_max(fmaxf(a, b));
}
DI void attn_slow(const LAS char* kp, const LAS char* vp, bool isv, bf16x8 q, f32x16& o, float& m) {
  const int sidx = 0xDDDDDDDD;
  f32x16 zero;
#pragma unroll
  for (int i = 0; i < 16; ++i) zero[i] = 0.f;
  o = zero; m = -3.0e38f;
  for (int t = 0; t < 32; ++t) {
    f32x16 s = MFMA(LDF(kp + t * 1024), q, zero);
    const float mn = fmaxf(m, max16(s));
    const float al = __builtin_amdgcn_exp2f(m - mn);
    m = mn;
#pragma unroll
    for (int i = 0; i < 16; ++i) { o[i] *= al; s[i] = __builtin_amdgcn_exp2f(s[i] - mn); }
    const int vo = isv ? t * 1024 : 0;
    o = SMF(LDF(vp + vo), LO8(s), o);
    o = SMF(LDF(vp + vo + 512), HI8(s), o);
  }
}

struct In { const unsigned short *x, *wqkv, *wo, *w1, *b1, *w2; unsigned short* out; };
struct Jobs { unsigned e[2][8], dst[2], csd[2]; bool ok[2]; };
DI void jobs_load(const Ctx& c, const In& in, Jobs& jb) {
  const int t0 = c.nb < 11 ? 3 * c.nb : 33 + 2 * (c.nb - 11), nt1 = c.nb < 11 ? 3 : 2;
#pragma unroll
  for (int u = 0; u < 2; ++u) {
    const int j = c.w - 1 + 7 * u;
    const unsigned short* W; int N, nt, ks;
    if (j < 6) { W = in.w1; N = F_; jb.ok[u] = j >= 0 && (j >> 1) < nt1; nt = t0 + (jb.ok[u] ? j >> 1 : 0); ks = 2 * c.tb + (j & 1); jb.dst[u] = WS_W1 + (nt * 32 + ks) * 1024; }
    else if (j < 8) { W = in.wo; N = D_; jb.ok[u] = c.w != 0; nt = 2 * c.nb8 + (j & 1); ks = c.tb64; jb.dst[u] = WS_WO + (nt * 32 + ks) * 1024; }
    else { W = in.w2; N = D_; ks = c.tb64 + 32 * ((j - 8) >> 1); jb.ok[u] = ks < KS_F; if (!jb.ok[u]) ks = 0; nt = 2 * c.nb8 + (j & 1); jb.dst[u] = WS_W2 + (nt * KS_F + ks) * 1024; }
    jb.csd[u] = WS_CSP + ((16 + c.nb) * 32 + ks) * 384 + (j >> 1) * 128;
    wfrag_load(W, N, nt, ks, c.r, c.h, jb.e[u]);
  }
}
DI void jobs_store(const Ctx& c, Jobs& jb) {
#pragma unroll
  for (int i = 0; i < 16; ++i) PIN(jb.e[i >> 3][i & 7]);
#pragma unroll
  for (int u = 0; u < 2; ++u)
    if (jb.ok[u]) wfrag_store(c, jb.e[u], jb.dst[u], c.w - 1 + 7 * u < 6, jb.csd[u]);
}

#define EX1(r) "v_exp_f32 v" #r ", v" #r "\n\t"
#define ASTEP(KOFF, VOFF, kc_, kn_, v0_, v1_, nv0_, pv1_)                                                                  \
  asm volatile("s_waitcnt lgkmcnt(0)\n\t" EX1(64) EX1(65)                                                                  \
               "v_smfmac_f32_32x32x32_bf16 %[o1], %[pv1], v[88:95], %[idx]\n\t"                                            \
               "ds_read_b128 %[nv0], %[vp] offset:%[vo]\n\tds_read_b128 %[pv1], %[vp] offset:%[vo1]\n\t"                   \
               EX1(66) EX1(67) EX1(68)                                                                                     \
               "v_mfma_f32_32x32x16_bf16 v[80:95], %[kc], %[q1], %[c1]\n\t"                                                \
               "ds_read_b128 %[kc], %[kp] offset:%[ko]\n\t"                                                                \
               EX1(69) EX1(70) EX1(71) EX1(72) EX1(73)                                                                     \
               "v_smfmac_f32_32x32x32_bf16 %[o0], %[v0], v[64:71], %[idx]\n\t"                                             \
               EX1(74) EX1(75) EX1(76) EX1(77) EX1(78) EX1(79) EX1(80) EX1(81)                                             \
               "v_smfmac_f32_32x32x32_bf16 %[o0], %[v1], v[72:79], %[idx]\n\t"                                             \
               EX1(82) EX1(83) EX1(84)                                                                                     \
               "v_mfma_f32_32x32x16_bf16 v[64:79], %[kn], %[q0], %[c0]\n\t"                                                \
               EX1(85) EX1(86) EX1(87) EX1(88) EX1(89)                                                                     \
               "v_smfmac_f32_32x32x32_bf16 %[o1], %[v0], v[80:87], %[idx]\n\t"                                             \
               EX1(90) EX1(91) EX1(92) EX1(93) EX1(94) "v_exp_f32 v95, v95"                                                \
               : "+{v[64:79]}"(sA), "+{v[80:95]}"(sB), [o0] "+v"(o0), [o1] "+v"(o1), [kc] "+v"(kc_), [nv0] "=&v"(nv0_),    \
                 [pv1] "+v"(pv1_)                                                                                          \
               : [kn] "v"(kn_), [v0] "v"(v0_), [v1] "v"(v1_), [q0] "v"(q0), [q1] "v"(q1), [c0] "v"(cin0), [c1] "v"(cin1),  \
                 [idx] "v"(sidx), [kp] "v"(kpa), [vp] "v"(vpa), [ko] "n"(KOFF), [vo] "n"(VOFF), [vo1] "n"((VOFF) + 512)     \
               : "memory")

template <int half, int l>
DI void phase_attn(Ctx& c, const In& in, u32x4 (&wo)[16], u32x4 (&hw)[28]) {
  const int qt = c.qb * 8 + (c.w & 3) * 2;
  const unsigned lay = WS_L + l * L_SZ;
  const char* kvg = c.ws + lay + ((c.w & 1) ? L_V : L_K) + c.head * 65536 + ((c.w >> 1) & 1) * 32768 + half * 4096;
  const unsigned kvd = c.lbase + ((c.w & 1) ? LD_V : LD_W) + ((c.w >> 1) & 1) * 32768 + half * 4096;
  u32x4 qv0, qv1;
  asm volatile("s_nop 4\n\tglobal_load_dwordx4 %0, %2, %3 sc1\n\tglobal_load_dwordx4 %1, %2, %3 offset:1024 sc1"
               : "=&v"(qv0), "=&v"(qv1) : "v"(c.voff), "s"(c.ws + lay + L_Q + (c.head * 64 + qt) * 1024) : "memory");
#define KV4(u) glds16x4(kvg + (u) * 8192, c.voff, kvd + (u) * 8192)
  glds16x4(kvg, c.voff, kvd);
  if (half == 0) {
    const unsigned o1 = (c.lane & 11) == 0 ? 0x3f803f80u : 0u;
    ((LAS u32x4*)(c.lds + LD_ONE))[c.w * 64 + c.lane] = (u32x4){o1, o1, o1, o1};
  }
  int sidx = 0xDDDDDDDD;
  const bool isv = c.r < 16;
  const LAS char* kp0 = c.lds + LD_W + half * 32768 + c.lane * 16;
  const LAS char* vp0 = c.lds + (isv ? LD_V + half * 32768 : LD_ONE) + c.h * 256 + (c.r & 15) * 16;
  const int vstep = isv ? 4096 : 0;
  if (half == 0) asm volatile("s_waitcnt vmcnt(0) lgkmcnt(0)\n\ts_barrier" : "+v"(qv0), "+v"(qv1)::"memory");
  else asm volatile("s_waitcnt vmcnt(4)\n\ts_barrier" : "+v"(qv0), "+v"(qv1)::"memory");
  STAMP((2 + 5 * l) * 10 + 1);
  if (half == 0) __builtin_amdgcn_s_setprio(1);
  const bf16x8 q0 = as_bf(qv0), q1 = as_bf(qv1);
  f32x16 zero;
#pragma unroll
  for (int i = 0; i < 16; ++i) zero[i] = 0.f;
  f32x16 cin0, cin1, o0 = zero, o1 = zero, sA, sB = zero;
  float m0, m1;
  bf16x8 ka = LDF(kp0), kb = LDF(kp0 + 1024), va0 = LDF(vp0), va1 = LDF(vp0 + 512), vb0, vb1 = as_bf((u32x4){0u, 0u, 0u, 0u});
  {
    const f32x16 s = MFMA(ka, q0, zero), s1 = MFMA(ka, q1, zero);
    m0 = max16(s); m1 = max16(s1);
#pragma unroll
    for (int i = 0; i < 16; ++i) { cin0[i] = -m0; cin1[i] = -m1; sA[i] = s[i] - m0; }
  }
  unsigned kpa = (unsigned)(size_t)kp0, vpa = (unsigned)(size_t)vp0;
  asm volatile("s_nop 1" : "+v"(cin0), "+v"(cin1), "+v"(sA), "+v"(sB), "+v"(o0), "+v"(o1), "+v"(sidx), "+v"(vb1));
#define QUAD(A, B, MID, C, D)                                                        \
  do {                                                                               \
    ASTEP(2048, 1024, ka, kb, va0, va1, vb0, vb1); A;                                \
    ASTEP(3072, 2048, kb, ka, vb0, vb1, va0, va1); B; MID;                           \
    ASTEP(4096, 3072, ka, kb, va0, va1, vb0, vb1); C;                                \
    vpa += vstep;                                                                    \
    ASTEP(5120, 0, kb, ka, vb0, vb1, va0, va1); D;                                   \
    kpa += 4096; QSTAMP();                                                           \
  } while (0)
#define NOP (void)0
#define QSTAMP()
#define WBAR(n) asm volatile("s_waitcnt vmcnt(" #n ")\n\ts_barrier" ::: "memory")
#define PREPPUB() do { if (half == 1 && c.w == 4 && c.lane < 2) \
    __hip_atomic_store(c.lane ? flag_ptr(c, FL_W1 + c.nb, c.tb) : flag_ptr(c, FL_W2 + c.nb8, c.tb64), c.token, RLX_AGENT); } while (0)
  Jobs jb;
  KV4(1);
  QUAD(KV4(2), NOP, WBAR(8), KV4(3), if (l == 0) { jobs_load(c, in, jb); SBAR(); });
  QUAD(NOP, NOP, WBAR(0); if (half == 1 && l == 1) (w2_fetch<0, 21>(c, hw)), if (l == 0) { jobs_store(c, jb); SBAR(); }, NOP);
  QUAD(NOP, NOP, if (l == 0) { WBAR(0); PREPPUB(); if (half == 1) { poll_any(c, FL_W2 + c.nb8, c.lane & 31); (w2_fetch<0, 21>(c, hw)); } }, NOP, NOP);
  STAMP((2 + 5 * l) * 10 + 5);
  for (int it = 3; it < 8; ++it) QUAD(NOP, NOP, NOP, NOP, NOP);
#undef WBAR
#undef PREPPUB
  asm volatile("s_nop 1\n\tv_smfmac_f32_32x32x32_bf16 %[o1], %[pv1], v[88:95], %[idx]\n\ts_waitcnt lgkmcnt(0)\n\ts_nop 7\n\ts_nop 7"
               : [o1] "+v"(o1), "+v"(o0), "+v"(ka), "+v"(kb), "+v"(va0), "+v"(va1) : [pv1] "v"(vb1), "{v[80:95]}"(sB), [idx] "v"(sidx));
#undef QUAD
#undef KV4
  STAMP((2 + 5 * l) * 10 + 2);
  if (__any(!(o0[8] < 1.0995116e12f))) attn_slow(kp0, vp0, isv, q0, o0, m0);
  if (__any(!(o1[8] < 1.0995116e12f))) attn_slow(kp0, vp0, isv, q1, o1, m1);
  LAS f32x4* cmb = (LAS f32x4*)(c.lds + LD_CMB) + (c.w & 3) * 320 + c.lane;
  const unsigned w1src = WS_W1 + (c.nb < 11 ? 3 * c.nb : 33 + 2 * (c.nb - 11)) * 32768;
  if (half == 0) {
    __builtin_amdgcn_s_setprio(0);
    cmb[0] = (f32x4){o0[0], o0[1], o0[2], o0[3]}; cmb[64] = (f32x4){o0[4], o0[5], o0[6], o0[7]};
    cmb[128] = (f32x4){o1[0], o1[1], o1[2], o1[3]}; cmb[192] = (f32x4){o1[4], o1[5], o1[6], o1[7]};
    cmb[256] = (f32x4){o0[8], m0, o1[8], m1};
    arrive4(c, 12 + l);
    if (l == 0) poll_any(c, c.lane < 32 ? FL_W1 + c.nb : FL_W2 + c.nb8, c.lane < 32 ? (c.lane & 15) : (c.lane & 31));
    const unsigned wos = WS_WO + ((2 * c.nb8 + (c.w & 1)) * 32 + 8 * c.w) * 1024;
    const unsigned woo = WS_WO + ((2 * c.nb8 + 1 - (c.w & 1)) * 32 + 8 * c.w) * 1024;
#pragma unroll
    for (int k = 0; k < 8; ++k) { wo[2 * k] = ld16(c, c.voff, wos + k * 1024); wo[2 * k + 1] = ld16(c, c.voff, woo + k * 1024); }
    SBAR();
    wait4<true>(c, 12 + l);
    help_dma<8>(c, w1src, LD_W);
    if (c.nb < 11) help_dma<8>(c, w1src + 65536, LD_W + 65536);
  }
  STAMP((2 + 5 * l) * 10 + 3);
  BARRIER_LDS();
  STAMP((2 + 5 * l) * 10 + 4);
  if (half == 1) {
    const f32x4 lm = cmb[256];
#pragma unroll
    for (int u = 0; u < 2; ++u) {
      const f32x16& o = u ? o1 : o0; const float m = u ? m1 : m0;
      const f32x8 p2 = cat8(cmb[128 * u], cmb[128 * u + 64]);
      const float m2 = lm[2 * u + 1];
      const float mm = fmaxf(m, m2);
      const float f1 = __builtin_amdgcn_exp2f(m - mm), f2 = __builtin_amdgcn_exp2f(m2 - mm);
      const float inv = __builtin_amdgcn_rcpf(o[8] * f1 + lm[2 * u] * f2);
      const float g1 = f1 * inv, g2 = f2 * inv;
      unsigned p[4];
#pragma unroll
      for (int i = 0; i < 4; ++i) p[i] = pk2(o[2 * i] * g1 + p2[2 * i] * g2, o[2 * i + 1] * g1 + p2[2 * i + 1] * g2);
      st16(c, (u32x4){p[0], p[1], p[2], p[3]}, c.voff, lay + L_O + ((qt + u) * 32 + c.head) * 1024);
    }
    STAMP((2 + 5 * l) * 10 + 6);
    asm volatile("s_waitcnt vmcnt(0)" ::: "memory");
    if (c.lane < 8) __hip_atomic_store(flag_ptr(c, FL_O + l * 256 + (c.qb * 4 + (c.w & 3)) * 8 + c.lane, c.head), c.token, RLX_AGENT);
  }
}

DI u64 uniform64(u64 v) {
  return ((u64)__builtin_amdgcn_readfirstlane((unsigned)(v >> 32)) << 32) | (unsigned)__builtin_amdgcn_readfirstlane((unsigned)v);
}

DI void run_main(Ctx& c, const In& in) {
  const u64 epoch = __hip_atomic_load((gu64*)c.ws, RLX_AGENT);
  u32x4 res[2];
  {
    if (c.w == 0 && c.lane < 32) ((LAS unsigned*)(c.lds + LD_CTR))[c.lane] = 0u;
    const unsigned short* src = in.x + (size_t)((2 * c.tb64 + (c.w >> 1)) * 32 + c.r) * D_ + 32 * (2 * c.nb8 + (c.w & 1)) + 4 * c.h;
    u32x2 xr[4];
#pragma unroll
    for (int i = 0; i < 4; ++i) xr[i] = *(const u32x2*)(src + 8 * i);
    BARRIER_LDS();
    c.token = uniform64(epoch) + 1;
    res[0] = (u32x4){xr[0][0], xr[0][1], xr[1][0], xr[1][1]}; res[1] = (u32x4){xr[2][0], xr[2][1], xr[3][0], xr[3][1]};
    store_x(c, 0, res);
    publish(c, 0, 4, FL_X + c.tb * 16, c.nb);
    STAMP(1);
    poll(c, (c.lane & 16) ? FL_WQ + c.nb * 16 + c.tb : FL_X + c.tb * 16 + c.nb, c.lane & 15);
    STAMP(2);
    BARRIER_LDS();
    STAMP(11);
    u32x4 dummy[16];
    fresh(c);
    phase_ln<0, true>(c, 0, 0, dummy);
  }
  for (int l = 0; l < 2; ++l) {
    u32x4 wo[16], w2r[16];
    if (l == 1) {
      STAMP((1 + 5 * l) * 10 + 1);
      fresh(c);
      u32x4 dummy[16];
      phase_ln<0, false>(c, l, 2 * l, dummy);
    }
    fresh(c);
    STAMP((1 + 5 * l) * 10 + 9);
    poll(c, FL_Q + l * 256 + c.head * 8 + c.qb, c.lane & 15);
    BARRIER_LDS();
    STAMP((2 + 5 * l) * 10);
    { u32x4 unused[28]; if (l == 0) phase_attn<0, 0>(c, in, wo, unused); else phase_attn<0, 1>(c, in, wo, unused); }
    fresh(c);
    STAMP((2 + 5 * l) * 10 + 9);
    poll(c, FL_O + l * 256 + c.tb64 * 8 + c.nb8, c.lane & 31);
    BARRIER_LDS();
    STAMP((3 + 5 * l) * 10 + 1);
    phase_wo(c, l, wo, res);
    store_x(c, 2 * l + 1, res);
    STAMP((3 + 5 * l) * 10 + 6);
    publish(c, 8 + l, 4, FL_X + (2 * l + 1) * 256 + c.tb * 16, c.nb);
    fresh(c);
    STAMP((3 + 5 * l) * 10 + 9);
    poll(c, FL_X + (2 * l + 1) * 256 + c.tb * 16 + c.nb, c.lane & 15);
    BARRIER_ALL();
    STAMP((4 + 5 * l) * 10 + 1);
    phase_ln<1, false>(c, l, 2 * l + 1, w2r);
    fresh(c);
    STAMP((4 + 5 * l) * 10 + 9);
    poll(c, FL_H + l * 256 + c.tb * 16 + c.nb, c.lane & 15);
    STAMP((4 + 5 * l) * 10 + 8);
    BARRIER_LDS();
    STAMP((5 + 5 * l) * 10 + 1);
    phase_w2(c, l, w2r, res);
    fresh(c);
    if (l == 0) {
      store_x(c, 2, res);
      STAMP((5 + 5 * l) * 10 + 6);
      publish(c, 10, 4, FL_X + 2 * 256 + c.tb * 16, c.nb);
      STAMP((5 + 5 * l) * 10 + 9);
      poll(c, FL_X + 2 * 256 + c.tb * 16 + c.nb, c.lane & 15);
      STAMP((5 + 5 * l) * 10 + 8);
      BARRIER_LDS();
    }
  }
  unsigned short* op = in.out + (size_t)((2 * c.tb64 + (c.w >> 1)) * 32 + c.r) * D_ + 32 * (2 * c.nb8 + (c.w & 1)) + 4 * c.h;
#pragma unroll
  for (int i = 0; i < 4; ++i) *(u32x2*)(op + 8 * i) = (u32x2){res[i >> 1][2 * (i & 1)], res[i >> 1][2 * (i & 1) + 1]};
  STAMP(109);
  if (c.bid == 0 && c.w == 0 && c.lane == 0) __hip_atomic_store((gu64*)c.ws, c.token, RLX_AGENT);
}

DI void run_help(Ctx& c, const In& in) {
  const u64 epoch = __hip_atomic_load((gu64*)c.ws, RLX_AGENT);
  {
    unsigned e0[8], e1[8];
    const int qk = 2 * c.tb + (c.w & 1), qa = (c.w >> 1) & 1;
    wfrag_load(in.wqkv, 3 * D_, qa * 16 + c.nb, qk, c.r, c.h, e0);
    wfrag_load(in.wqkv, 3 * D_, 32 + c.nb, qk, c.r, c.h, e1);
    const int t = (c.w & 3) * 64 + c.lane;
    const int bcol = 32 * (c.nb < 11 ? 3 * c.nb : 33 + 2 * (c.nb - 11)) + t;
    const unsigned bv = in.b1[bcol < F_ ? bcol : 0];
    BARRIER_LDS();
    c.token = uniform64(epoch) + 1;
    wfrag_store(c, e0, WS_WQKV + ((qa * 16 + c.nb) * 32 + qk) * 1024, true, WS_CSP + (c.nb * 32 + qk) * 384 + qa * 128);
    if (qa == 0) wfrag_store(c, e1, WS_WQKV + ((32 + c.nb) * 32 + qk) * 1024, true, WS_CSP + (c.nb * 32 + qk) * 384 + 256);
    publish(c, 1, 4, FL_WQ + c.nb * 16, c.tb);
    STAMP(1);
    if (t < 96) ((LAS float*)(c.lds + LD_PAR))[192 + t] = __uint_as_float(bv << 16);
    BARRIER_LDS();
    STAMP(11);
    fresh(c);
#define WQC(q) _Pragma("unroll") for (int t_ = 0; t_ < 3; ++t_) \
    glds16x2(c.ws + WS_WQKV + ((16 * t_ + c.nb) * 32 + 8 * (q) + 2 * (c.w & 3)) * 1024, c.voff, c.lbase + LD_W + t_ * 32768 + (8 * (q) + 2 * (c.w & 3)) * 1024)
    WQC(0); WQC(1);
    asm volatile("s_waitcnt vmcnt(6)\n\ts_barrier" ::: "memory");
    WQC(2);
    asm volatile("s_waitcnt vmcnt(6)\n\ts_barrier" ::: "memory");
    WQC(3);
    asm volatile("s_waitcnt vmcnt(6)\n\ts_barrier" ::: "memory");
    asm volatile("s_waitcnt vmcnt(0)\n\ts_barrier" ::: "memory");
#undef WQC
    help_colsum(c, 0);
    STAMP(12);
    help_ln<0, true>(c, 0);
  }
  for (int l = 0; l < 2; ++l) {
    u32x4 wo[16];
    fresh(c);
    if (l == 1) help_ln<0, false>(c, l);
    STAMP((1 + 5 * l) * 10 + 9);
    BARRIER_LDS();
    STAMP((2 + 5 * l) * 10);
    fresh(c);
    u32x4 hw[28];
    if (l == 0) phase_attn<1, 0>(c, in, wo, hw); else phase_attn<1, 1>(c, in, wo, hw);
    fresh(c);
    STAMP((2 + 5 * l) * 10 + 9);
    BARRIER_LDS();
    asm volatile("s_barrier" ::: "memory");
    help_dma<8>(c, WS_W1 + (c.nb < 11 ? 3 * c.nb + 1 : 34 + 2 * (c.nb - 11)) * 32768, LD_W + 32768);
    w2_fetch<21, 28>(c, hw);
    asm volatile("s_barrier" ::: "memory");
    if (l == 0) help_colsum(c, 1);
    BARRIER_ALL();
    STAMP((4 + 5 * l) * 10 + 1);
    w2_put<21, 28>(c, hw);
    STAMP((4 + 5 * l) * 10 + 2);
    help_ln<1, false>(c, l);
    STAMP((4 + 5 * l) * 10 + 6);
    w2_put<0, 21>(c, hw);
    STAMP((4 + 5 * l) * 10 + 9);
    BARRIER_LDS();
    STAMP((5 + 5 * l) * 10 + 1);
    asm volatile("s_barrier" ::: "memory");
    if (l == 0) {
      fresh(c);
#pragma unroll
      for (int u = 0; u < 24; ++u) hw[u] = ld16(c, c.voff, WS_WQKV + (((u >> 3) * 16 + c.nb) * 32 + (u & 7) * 4 + (c.w & 3)) * 1024);
      SBAR();
      STAMP((5 + 5 * l) * 10 + 2);
    }
    if (l == 0) {
      wait4<true>(c, 16);
#pragma unroll
      for (int u = 0; u < 24; ++u) *(LAS u32x4*)(c.lds + LD_W + (u * 4 + (c.w & 3)) * 1024 + c.voff) = hw[u];
      STAMP((5 + 5 * l) * 10 + 9);
      BARRIER_LDS();
    } else {
      asm volatile("s_barrier\n\ts_barrier" ::: "memory");
    }
  }
}

__global__ __launch_bounds__(512, 2) void mega(const unsigned short* __restrict__ x, const unsigned short* __restrict__ wqkv,
                                               const unsigned short* __restrict__ wo_g, const unsigned short* __restrict__ w1,
                                               const unsigned short* __restrict__ b1, const unsigned short* __restrict__ w2,
                                               char* __restrict__ ws, unsigned short* __restrict__ out) {
  extern __shared__ __attribute__((aligned(16))) char lds_[];
  Ctx c;
  c.ws = ws; c.lds = (LAS char*)lds_; c.rs = __builtin_amdgcn_make_buffer_rsrc(ws, 0, WS_END, 0x00020000);
  c.lbase = (unsigned)(size_t)c.lds;
  c.bid = blockIdx.x; c.lane = threadIdx.x & 63; c.w = __builtin_amdgcn_readfirstlane(threadIdx.x >> 6); c.dead = false;
  fresh(c);
  STAMP(0);
  const In in = {x, wqkv, wo_g, w1, b1, w2, out};
  if (c.w < 4) run_main(c, in); else run_help(c, in);
}

extern "C" void kernel_launch(void* const* d_in, const int* in_sizes, int n_in, void* d_out, int out_size, void* d_ws,
                              size_t ws_size, hipStream_t stream) {
  (void)hipFuncSetAttribute((const void*)mega, hipFuncAttributeMaxDynamicSharedMemorySize, LDS_BYTES);
  mega<<<256, 512, LDS_BYTES, stream>>>((const unsigned short*)d_in[0], (const unsigned short*)d_in[1], (const unsigned short*)d_in[2],
                                        (const unsigned short*)d_in[4], (const unsigned short*)d_in[5], (const unsigned short*)d_in[6],
                                        (char*)d_ws, (unsigned short*)d_out);
}
```

```cpp
#include <hip/hip_runtime.h>
#include <hip/hip_bf16.h>
#define KV_G2 0
#define KV_PD 12
#define KV_WIN 10

typedef __attribute__((ext_vector_type(2))) __bf16 bf16x2;
typedef __attribute__((ext_vector_type(8))) __bf16 bf16x8;
typedef __attribute__((ext_vector_type(16))) __bf16 bf16x16;
typedef __attribute__((ext_vector_type(16))) float f32x16;
typedef __attribute__((ext_vector_type(8))) float f32x8;
typedef __attribute__((ext_vector_type(4))) float f32x4;
typedef __attribute__((ext_vector_type(2))) float f32x2;
typedef __attribute__((ext_vector_type(4))) unsigned u32x4;
typedef __attribute__((ext_vector_type(2))) unsigned u32x2;
typedef unsigned long long u64;
#define LAS __attribute__((address_space(3)))
#define GAS __attribute__((address_space(1)))
typedef GAS u64 gu64;
#define DI __device__ __forceinline__
#define RLX_AGENT __ATOMIC_RELAXED, __HIP_MEMORY_SCOPE_AGENT

constexpr int S_ = 2048, D_ = 512, F_ = 1376;
constexpr int KS_F = F_ / 16;

constexpr unsigned MB = 1u << 20;
constexpr unsigned WS_FLAG = 0;
constexpr unsigned WS_WQKV = 1024 * 1024;
constexpr unsigned WS_WO = WS_WQKV + 48 * 32 * 1024;
constexpr unsigned WS_W1 = WS_WO + 16 * 32 * 1024;
constexpr unsigned WS_W2 = WS_W1 + 43 * 32 * 1024;
constexpr unsigned WS_CSP = WS_W2 + 16 * 86 * 1024;
constexpr unsigned WS_ST = WS_CSP + 2 * 16 * 32 * 384;
constexpr unsigned WS_X = WS_ST + 4 * 64 * 4096;
constexpr unsigned WS_L = WS_X + 8 * MB;
constexpr unsigned L_Q = 0, L_K = 2 * MB, L_V = 4 * MB, L_O = 6 * MB, L_H = 8 * MB, L_SZ = 14 * MB;
constexpr unsigned WS_END = WS_L + 2 * L_SZ;
constexpr int FL_X = 1;
constexpr int FL_H = FL_X + 1024;
constexpr int FL_Q = FL_H + 512;
constexpr int FL_O = FL_Q + 512;
constexpr int FL_WQ = FL_O + 512;
constexpr int FL_W1 = FL_WQ + 256;
constexpr int FL_W2 = FL_W1 + 16;
static_assert((FL_W2 + 8) * 256 <= WS_WQKV, "flag region");

constexpr int LD_W = 0;
constexpr int LD_V = 65536;
constexpr int LD_EX = 98304;
constexpr int LD_ONE = 131072;
constexpr int LD_CMB = 135168;
constexpr int LD_W2E = 122880;
constexpr int LD_SC = 151552;
constexpr int LD_ST = 155648;
constexpr int LD_PAR = 157696;
constexpr int LD_CTR = LD_PAR + 1280;
constexpr int LD_DBG = LD_CTR + 512;
constexpr int LDS_BYTES = 163840;

#define STORE16(p, v) __builtin_nontemporal_store((v), (u32x4*)(p))
#define MFMA(a, b, c) __builtin_amdgcn_mfma_f32_32x32x16_bf16((a), (b), (c), 0, 0, 0)
#define SBAR() __builtin_amdgcn_sched_barrier(0)
#define PIN(x) asm volatile("" : "+v"(x))
#define BARRIER_LDS() asm volatile("s_waitcnt lgkmcnt(0)\n\ts_barrier" ::: "memory")
#define BARRIER_ALL() asm volatile("s_waitcnt vmcnt(0) lgkmcnt(0)\n\ts_barrier" ::: "memory")

#define STAMP(i)

DI unsigned pk2(float lo, float hi) {
  bf16x2 v; v[0] = (__bf16)lo; v[1] = (__bf16)hi;
  return __builtin_bit_cast(unsigned, v);
}
DI float bf_lo(unsigned u) { return __uint_as_float(u << 16); }
DI float bf_hi(unsigned u) { return __uint_as_float(u & 0xffff0000u); }
DI float rbf(float x) { return (float)(__bf16)x; }
DI bf16x8 as_bf(u32x4 v) { return __builtin_bit_cast(bf16x8, v); }
DI float swap_half_sum(float x) {
  auto r = __builtin_amdgcn_permlane32_swap(__float_as_uint(x), __float_as_uint(x), false, false);
  return __uint_as_float(r[0]) + __uint_as_float(r[1]);
}
DI float swap_half_max(float x) {
  auto r = __builtin_amdgcn_permlane32_swap(__float_as_uint(x), __float_as_uint(x), false, false);
  return fmaxf(__uint_as_float(r[0]), __uint_as_float(r[1]));
}
DI void glds16(const char* sbase, unsigned voff, unsigned lds_base) {
  unsigned sv;
  asm volatile("s_mov_b32 %0, m0\n\ts_mov_b32 m0, %3\n\ts_nop 2\n\tglobal_load_lds_dwordx4 %1, %2 sc1\n\ts_mov_b32 m0, %0"
               : "=&s"(sv) : "v"(voff), "s"(sbase), "s"(lds_base) : "memory");
}
DI void glds16x2(const char* sbase, unsigned voff, unsigned lds_base) {
  unsigned sv;
  asm volatile("s_mov_b32 %0, m0\n\ts_mov_b32 m0, %3\n\ts_nop 2\n\tglobal_load_lds_dwordx4 %1, %2 sc1\n\t"
               "global_load_lds_dwordx4 %1, %2 offset:1024 sc1\n\ts_mov_b32 m0, %0"
               : "=&s"(sv) : "v"(voff), "s"(sbase), "s"(lds_base) : "memory");
}
DI void glds16x4(const char* sbase, unsigned voff, unsigned lds_base) {
  unsigned sv;
  asm volatile("s_mov_b32 %0, m0\n\ts_mov_b32 m0, %3\n\ts_nop 2\n\tglobal_load_lds_dwordx4 %1, %2 sc1\n\t"
               "global_load_lds_dwordx4 %1, %2 offset:1024 sc1\n\tglobal_load_lds_dwordx4 %1, %2 offset:2048 sc1\n\t"
               "global_load_lds_dwordx4 %1, %2 offset:3072 sc1\n\ts_mov_b32 m0, %0"
               : "=&s"(sv) : "v"(voff), "s"(sbase), "s"(lds_base) : "memory");
}
template <int I> struct IC { static constexpr int v = I; };
template <int I, int N, class F> DI void static_for(F&& f) {
  if constexpr (I < N) { f(IC<I>{}); static_for<I + 1, N>(f); }
}

struct Ctx {
  char* ws; LAS char* lds; __amdgpu_buffer_rsrc_t rs;
  u64 token; unsigned lbase, voff;
  int bid, lane, w, r, h, tb, nb, tb64, nb8, head, qb;
  bool dead;
};
DI void fresh(Ctx& c) {
  asm volatile("" : "+s"(c.bid));
  asm volatile("" : "+s"(c.w));
  asm volatile("" : "+v"(c.lane));
  const int g = c.bid & 7, idx = c.bid >> 3;
  c.r = c.lane & 31; c.h = c.lane >> 5; c.voff = c.lane * 16;
  c.tb = 2 * g + (idx >> 4); c.nb = idx & 15; c.tb64 = 4 * g + (idx >> 3); c.nb8 = idx & 7;
  c.head = 4 * g + (idx & 3); c.qb = idx >> 2;
}
DI u32x4 ld16(const Ctx& c, unsigned voff, unsigned soff) { return __builtin_amdgcn_raw_buffer_load_b128(c.rs, voff, soff, 16); }
DI float ld4(const Ctx& c, unsigned voff, unsigned soff) { return __uint_as_float(__builtin_amdgcn_raw_buffer_load_b32(c.rs, voff, soff, 16)); }
DI void st16(const Ctx& c, u32x4 v, unsigned voff, unsigned soff) { __builtin_amdgcn_raw_buffer_store_b128(v, c.rs, voff + soff, 0, 16); }
DI void st4(const Ctx& c, float v, unsigned voff, unsigned soff) { __builtin_amdgcn_raw_buffer_store_b32(__float_as_uint(v), c.rs, voff, soff, 16); }
DI gu64* flag_ptr(const Ctx& c, int line, int slot) { return (gu64*)(c.ws + WS_FLAG) + line * 32 + slot; }

DI void publish(const Ctx& c, int ctr, unsigned n, int line0, int slot) {
  asm volatile("s_waitcnt vmcnt(0)" ::: "memory");
  unsigned old = 0;
  if (c.lane == 0) old = __hip_atomic_fetch_add((LAS unsigned*)(c.lds + LD_CTR) + ctr, 1u, __ATOMIC_RELAXED, __HIP_MEMORY_SCOPE_WORKGROUP);
  if (__builtin_amdgcn_readfirstlane(old) == n - 1 && c.lane < 16) __hip_atomic_store(flag_ptr(c, line0 + c.lane, slot), c.token, RLX_AGENT);
}
DI void poll_any(Ctx& c, int line, int slot) {
  if (!c.dead) {
    gu64* p = flag_ptr(c, line, slot);
    unsigned spins = 0;
    while (!__all(__hip_atomic_load(p, RLX_AGENT) == c.token)) {
      if (++spins > (1u << 18)) { c.dead = true; break; }
    }
    __builtin_amdgcn_fence(__ATOMIC_ACQUIRE, "wavefront");
  }
}
template <bool NAP = false> DI void wait4(const Ctx& c, int ctr) {
  while (__hip_atomic_load((LAS unsigned*)(c.lds + LD_CTR) + ctr, __ATOMIC_RELAXED, __HIP_MEMORY_SCOPE_WORKGROUP) < 4u)
    if (NAP) __builtin_amdgcn_s_sleep(2);
  asm volatile("" ::: "memory");
}
DI void arrive4(const Ctx& c, int ctr) {
  asm volatile("s_waitcnt lgkmcnt(0)" ::: "memory");
  if (c.lane == 0) __hip_atomic_fetch_add((LAS unsigned*)(c.lds + LD_CTR) + ctr, 1u, __ATOMIC_RELAXED, __HIP_MEMORY_SCOPE_WORKGROUP);
}
DI void poll(Ctx& c, int line, int slot) { if (c.w == 0) poll_any(c, line, slot); }
template <int NQ> DI void help_dma(const Ctx& c, unsigned src, int dst) {
#pragma unroll
  for (int u = 0; u < NQ / 4; ++u) {
    const int p = (c.w & 3) + 4 * u;
    glds16x4(c.ws + src + p * 4096, c.voff, c.lbase + dst + p * 4096);
  }
}
DI int w2_run(int run) { return run < 6 ? LD_W + run * 14336 : LD_W2E + (run - 6) * 14336; }
template <int U0, int U1> DI void w2_fetch(const Ctx& c, u32x4 (&h)[28]) {
#pragma unroll
  for (int u = U0; u < U1; ++u) {
    const int f = (c.w & 3) + 4 * u, run = f / 14, kq = run >> 1;
    h[u] = ld16(c, c.voff, WS_W2 + ((2 * c.nb8 + (run & 1)) * KS_F + kq * 21 + (kq < 2 ? kq : 2) + f % 14) * 1024);
  }
  SBAR();
}
template <int U0, int U1> DI void w2_put(const Ctx& c, const u32x4 (&h)[28]) {
#pragma unroll
  for (int u = U0; u < U1; ++u) {
    const int f = (c.w & 3) + 4 * u;
    *(LAS u32x4*)(c.lds + (u < 21 ? LD_W : LD_W2E - 84 * 1024) + f * 1024 + c.voff) = h[u];
  }
}

DI void store_x(const Ctx& c, int inst, const u32x4 (&res)[2]) {
  const int tt = 2 * c.tb64 + (c.w >> 1), nt = 2 * c.nb8 + (c.w & 1);
  st16(c, res[0], c.voff, WS_X + inst * (2 * MB) + (tt * 32 + 2 * nt) * 1024);
  st16(c, res[1], c.voff, WS_X + inst * (2 * MB) + (tt * 32 + 2 * nt + 1) * 1024);
  float s1 = 0.f, s2 = 0.f;
#pragma unroll
  for (int i = 0; i < 8; ++i) {
    const unsigned u = res[i >> 2][i & 3];
    const float a = bf_lo(u), b = bf_hi(u);
    s1 += a + b; s2 = fmaf(a, a, fmaf(b, b, s2));
  }
  auto r1 = __builtin_amdgcn_permlane32_swap(__float_as_uint(s1), __float_as_uint(s2), false, false);
  st4(c, __uint_as_float(r1[0]) + __uint_as_float(r1[1]), c.lane * 4, WS_ST + inst * (64 * 4096) + tt * 4096 + nt * 256);
}

DI void wfrag_load(const unsigned short* W, int N, int nt, int ks, int r, int h, unsigned (&e)[8]) {
#pragma unroll
  for (int j = 0; j < 8; ++j) e[j] = W[(size_t)(16 * ks + 8 * (j >> 2) + 4 * h + (j & 3)) * N + 32 * nt + r];
}
DI void wfrag_store(const Ctx& c, const unsigned (&e)[8], unsigned dst, bool docs, unsigned csdst) {
  u32x4 v;
#pragma unroll
  for (int j = 0; j < 4; ++j) v[j] = e[2 * j] | (e[2 * j + 1] << 16);
  st16(c, v, c.voff, dst);
  if (docs) {
    float s = 0.f;
#pragma unroll
    for (int j = 0; j < 8; ++j) s += __uint_as_float(e[j] << 16);
    s = swap_half_sum(s);
    if (c.h == 0) st4(c, s, c.r * 4, csdst);
  }
}

DI float mish_f(float hh) {
  const float u = __builtin_amdgcn_exp2f(fminf(hh, 30.f) * 1.4426950408889634f);
  const float n = u * (u + 2.f);
  return hh * n * __builtin_amdgcn_rcpf(n + 2.f);
}
template <int MODE>
DI u32x2 epi4(float a0, float a1, float a2, float a3, f32x4 cs, f32x4 bb, float mr, float rstd) {
  const float a[4] = {a0, a1, a2, a3};
  float v[4];
#pragma unroll
  for (int j = 0; j < 4; ++j) {
    if (MODE == 0) v[j] = fmaf(a[j], rstd, mr * cs[j]);
    else v[j] = mish_f(fmaf(a[j], rstd, fmaf(mr, cs[j], bb[j])));
  }
  return (u32x2){pk2(v[0], v[1]), pk2(v[2], v[3])};
}
template <int MODE>
DI u32x4 epi8(const f32x8& a, const f32x4* cs, const f32x4* bb, float mr, float rstd) {
  const u32x2 lo = epi4<MODE>(a[0], a[1], a[2], a[3], cs[0], bb[0], mr, rstd);
  const u32x2 hi = epi4<MODE>(a[4], a[5], a[6], a[7], cs[1], bb[1], mr, rstd);
  return (u32x4){lo[0], lo[1], hi[0], hi[1]};
}
#define LO8(s) __builtin_shufflevector(s, s, 0, 1, 2, 3, 4, 5, 6, 7)
#define HI8(s) __builtin_shufflevector(s, s, 8, 9, 10, 11, 12, 13, 14, 15)
DI f32x4 q4(const f32x8& v, int g) { return g ? (f32x4){v[4], v[5], v[6], v[7]} : (f32x4){v[0], v[1], v[2], v[3]}; }
DI f32x8 cat8(f32x4 a, f32x4 b) { return __builtin_shufflevector(a, b, 0, 1, 2, 3, 4, 5, 6, 7); }

struct LnPar { f32x4 cs[6], bb[6]; float csv; };
template <int MODE, int T> DI void ln_params(const Ctx& c, LnPar& p) {
  const LAS float* par = (const LAS float*)(c.lds + LD_PAR) + MODE * 96 + 4 * c.h;
#pragma unroll
  for (int g = 0; g < 6; ++g) {
    const int o = g < 4 ? 32 * T + 8 * g : 64 + 16 * T + 8 * (g - 4);
    if (MODE == 1 || g < 4) p.cs[g] = *(const LAS f32x4*)(par + o);
    if (MODE == 1) p.bb[g] = *(const LAS f32x4*)(par + 96 + o);
  }
  if (MODE == 0) p.csv = ((const LAS float*)(c.lds + LD_PAR))[64 + c.r];
}
template <int T> DI void ln_tokstats(const Ctx& c, f32x4 (&ts)[4]) {
  const LAS f32x2* st = (const LAS f32x2*)(c.lds + LD_ST) + (c.w & 3) * 32 + 16 * T + 4 * c.h;
#pragma unroll
  for (int g = 0; g < 4; ++g) ts[g] = *(const LAS f32x4*)(st + 8 * (g >> 1) + 2 * (g & 1));
}
template <int MODE, int T>
DI void ln_epilogue(Ctx& c, int l, const f32x16& a, const f32x8& ch, float mean, float rstd, const LnPar& p, const f32x4 (&ts)[4]) {
  const int tt = 4 * c.tb + (c.w & 3);
  const unsigned lay = WS_L + l * L_SZ;
  if (MODE == 0) {
    const float rs = T ? rstd : rstd * (0.25f * 1.4426950408889634f), mr = -mean * rs;
    const unsigned vo = T ? c.h * 512 + ((c.r & 19) | ((c.r & 4) << 1) | ((c.r & 8) >> 1)) * 16 : c.voff;
    const unsigned so = lay + (T ? L_K : L_Q) + (2 * c.nb * 64 + tt) * 1024;
    st16(c, epi8<0>(LO8(a), p.cs, p.cs, mr, rs), vo, so);
    st16(c, epi8<0>(HI8(a), p.cs + 2, p.cs, mr, rs), vo, so + 65536);
    unsigned q[4];
#pragma unroll
    for (int g = 0; g < 4; ++g) q[g] = pk2((ch[2 * g] - ts[g][0] * p.csv) * ts[g][1], (ch[2 * g + 1] - ts[g][2] * p.csv) * ts[g][3]);
    st16(c, (u32x4){q[0], q[1], q[2], q[3]}, (c.r >> 4) * 65536 + c.h * 256 + (c.r & 15) * 16, lay + L_V + (2 * c.nb * 128 + 2 * tt + T) * 512);
    publish(c, 4 + l, 8, FL_Q + l * 256 + c.nb * 16, c.tb);
  } else {
    const float mr = -mean * rstd;
    const unsigned so = lay + L_H + (tt * KS_F + 2 * (c.nb < 11 ? 3 * c.nb : 33 + 2 * (c.nb - 11))) * 1024;
    st16(c, epi8<1>(LO8(a), p.cs, p.bb, mr, rstd), c.voff, so + T * 2048);
    st16(c, epi8<1>(HI8(a), p.cs + 2, p.bb + 2, mr, rstd), c.voff, so + T * 2048 + 1024);
    if (c.nb < 11) st16(c, epi8<1>(ch, p.cs + 4, p.bb + 4, mr, rstd), c.voff, so + 4096 + T * 1024);
    publish(c, 6 + l, 8, FL_H + l * 256 + c.tb * 16, c.nb);
  }
}

DI void pin16(f32x16& v) { PIN(v); }
DI u32x4 wfrag(const LAS char* base, int ks) { return *(const LAS u32x4*)(base + ks * 1024); }

template <int MODE, bool FIRST>
DI void phase_ln(Ctx& c, int l, int inst, u32x4 (&w2r)[16]) {
  const int PH = (1 + 5 * l + (MODE ? 3 : 0)) * 10; (void)PH;
  const int tt = 4 * c.tb + c.w;
  const bool has3 = MODE == 0 || c.nb < 11;
  const unsigned xs = WS_X + inst * (2 * MB) + tt * 32768;
  constexpr int PD = KV_PD;
  u32x4 xf[PD], sp[4];
#pragma unroll
  for (int k = 0; k < PD; ++k) xf[k] = ld16(c, c.voff, xs + k * 1024);
  SBAR();
  const LAS char* wl = c.lds + LD_W + c.lane * 16;
  const LAS char* wl2 = wl + 65536;
  asm volatile("" : "+v"(wl2));
  f32x16 aA, aB, aC;
#pragma unroll
  for (int i = 0; i < 16; ++i) { aA[i] = 0.f; aB[i] = 0.f; aC[i] = 0.f; }
  u32x4 fa[3], fb[3], fc[3];
  if (FIRST) BARRIER_LDS();
#pragma unroll
  for (int k = 0; k < 2; ++k) { fa[k] = wfrag(wl, k); fb[k] = wfrag(wl + 32768, k); fc[k] = wfrag(wl2, k); }
  float mean = 0.f, rstd = 0.f, sc4[4];
  LnPar par; f32x4 ts[4];
  LAS float* sc = (LAS float*)(c.lds + LD_SC) + c.w * 256;
  const int k0q = c.w * 21 + (c.w < 2 ? c.w : 2);
  const unsigned w2s = WS_W2 + ((2 * c.nb8 + (c.w & 1)) * KS_F + k0q + 14) * 1024;
  const unsigned w2o = WS_W2 + ((2 * c.nb8 + 1 - (c.w & 1)) * KS_F + k0q + 14) * 1024;
  static_for<0, 32>([&](auto kc) __attribute__((always_inline)) {
    constexpr int k = decltype(kc)::v;
    if constexpr (FIRST && (k & 7) == 6 && k < 24) asm volatile("s_barrier" ::: "memory");
    aA = MFMA(as_bf(fa[k % 3]), as_bf(xf[k % PD]), aA);
    pin16(aA); SBAR();
    if constexpr (k + 2 < 32) fa[(k + 2) % 3] = wfrag(wl, k + 2);
    if constexpr (k >= 1 && k - 1 + PD < 32) xf[(k - 1) % PD] = ld16(c, c.voff, xs + (k - 1 + PD) * 1024);
    if constexpr (k >= 33 - PD && k < 35 - PD) {
      sp[2 * (k + PD - 33)] = ld16(c, c.voff, WS_ST + inst * (64 * 4096) + tt * 4096 + 2 * (k + PD - 33) * 1024);
      sp[2 * (k + PD - 33) + 1] = ld16(c, c.voff, WS_ST + inst * (64 * 4096) + tt * 4096 + (2 * (k + PD - 33) + 1) * 1024);
    }
    SBAR();
    aB = MFMA(as_bf(fb[k % 3]), as_bf(xf[k % PD]), aB);
    pin16(aB); SBAR();
    if constexpr (k + 2 < 32) fb[(k + 2) % 3] = wfrag(wl + 32768, k + 2);
    if constexpr (MODE == 1 && k >= 23 && k < 31) {
      constexpr int i = k - 23;
      const int kk = (i == 7 && c.w >= 2) ? 6 : i;
      w2r[2 * i] = ld16(c, c.voff, w2s + kk * 1024); w2r[2 * i + 1] = ld16(c, c.voff, w2o + kk * 1024);
    }
    if constexpr (k == 28) {
      ((LAS f32x4*)sc)[c.lane] = (__builtin_bit_cast(f32x4, sp[0]) + __builtin_bit_cast(f32x4, sp[1])) +
                                 (__builtin_bit_cast(f32x4, sp[2]) + __builtin_bit_cast(f32x4, sp[3]));
#pragma unroll
      for (int i = 0; i < 4; ++i) sc4[i] = sc[i * 64 + c.lane];
    }
    if constexpr (k == 30) {
      const float s = (sc4[0] + sc4[1]) + (sc4[2] + sc4[3]);
      auto rr = __builtin_amdgcn_permlane32_swap(__float_as_uint(s), __float_as_uint(s), false, false);
      const float mu = __uint_as_float(rr[0]) * (1.f / D_);
      const float var = fmaxf(__uint_as_float(rr[1]) * (1.f / D_) - mu * mu, 0.f);
      mean = rbf(mu);
      rstd = __builtin_amdgcn_rcpf(rbf(__builtin_amdgcn_sqrtf(rbf(rbf(var) + 1e-5f))));
      ((LAS f32x2*)(c.lds + LD_ST))[c.w * 32 + c.r] = (f32x2){mean, rstd};
    }
    SBAR();
    if (has3) aC = MODE == 0 ? MFMA(as_bf(xf[k % PD]), as_bf(fc[k % 3]), aC) : MFMA(as_bf(fc[k % 3]), as_bf(xf[k % PD]), aC);
    pin16(aC); SBAR();
    if constexpr (k + 2 < 32) fc[(k + 2) % 3] = wfrag(wl2, k + 2);
    SBAR();
  });
  STAMP(PH + 2);
  if (MODE == 0) ln_tokstats<0>(c, ts);
  if (!FIRST) ln_params<MODE, 0>(c, par);
  SBAR();
  {
    LAS f32x4* ex = (LAS f32x4*)(c.lds + LD_EX) + c.w * 384 + c.lane;
#pragma unroll
    for (int g = 0; g < 4; ++g) ex[g * 64] = (f32x4){aB[4 * g], aB[4 * g + 1], aB[4 * g + 2], aB[4 * g + 3]};
    ex[256] = (f32x4){aC[8], aC[9], aC[10], aC[11]}; ex[320] = (f32x4){aC[12], aC[13], aC[14], aC[15]};
  }
  BARRIER_LDS();
  STAMP(PH + 4);
  if (FIRST) ln_params<MODE, 0>(c, par);
  ln_epilogue<MODE, 0>(c, l, aA, LO8(aC), mean, rstd, par, ts);
}
template <int MODE, bool FIRST>
DI void help_ln(Ctx& c, int l) {
  LnPar par; f32x4 ts[4];
  if (!FIRST) ln_params<MODE, 1>(c, par);
  BARRIER_LDS();
  if (FIRST) ln_params<MODE, 1>(c, par);
  const LAS f32x4* ex = (const LAS f32x4*)(c.lds + LD_EX) + (c.w & 3) * 384 + c.lane;
  const f32x2 ms = ((const LAS f32x2*)(c.lds + LD_ST))[(c.w & 3) * 32 + c.r];
  const f32x8 lo = cat8(ex[0], ex[64]), hi = cat8(ex[128], ex[192]), ch = cat8(ex[256], ex[320]);
  if (MODE == 0) ln_tokstats<1>(c, ts);
  SBAR();
  const f32x16 a = __builtin_shufflevector(lo, hi, 0, 1, 2, 3, 4, 5, 6, 7, 8, 9, 10, 11, 12, 13, 14, 15);
  ln_epilogue<MODE, 1>(c, l, a, ch, ms[0], ms[1], par, ts);
}
DI void help_colsum(const Ctx& c, int mode) {
  const int t = (c.w - 4) * 64 + c.lane;
  if (t < 96) {
    float v[32];
#pragma unroll
    for (int i = 0; i < 32; ++i) v[i] = ld4(c, t * 4, WS_CSP + ((mode * 16 + c.nb) * 32 + i) * 384);
    float s = 0.f;
#pragma unroll
    for (int i = 0; i < 32; i += 4) s += (v[i] + v[i + 1]) + (v[i + 2] + v[i + 3]);
    ((LAS float*)(c.lds + LD_PAR))[mode * 96 + t] = s;
  }
}

DI void reduce_res(Ctx& c, const f32x16 (&a)[4], u32x4 (&res)[2], int ctr, int PH) {
  LAS f32x4* red = (LAS f32x4*)(c.lds + LD_EX) + c.lane;
#pragma unroll
  for (int j = 1; j < 4; ++j) {
    LAS f32x4* p = red + ((c.w ^ j) * 3 + j - 1) * 256;
#pragma unroll
    for (int g = 0; g < 4; ++g) p[g * 64] = (f32x4){a[j][4 * g], a[j][4 * g + 1], a[j][4 * g + 2], a[j][4 * g + 3]};
  }
  STAMP(PH + 3);
  if (ctr >= 0) { arrive4(c, ctr); wait4(c, ctr); } else BARRIER_LDS();
  STAMP(PH + 4);
  const LAS f32x4* q = red + c.w * 768;
#pragma unroll
  for (int g = 0; g < 4; ++g) {
    const f32x4 s = ((f32x4){a[0][4 * g], a[0][4 * g + 1], a[0][4 * g + 2], a[0][4 * g + 3]} + q[g * 64]) + (q[256 + g * 64] + q[512 + g * 64]);
    const unsigned u0 = res[g >> 1][2 * (g & 1)], u1 = res[g >> 1][2 * (g & 1) + 1];
    res[g >> 1][2 * (g & 1)] = pk2(bf_lo(u0) + rbf(s[0]), bf_hi(u0) + rbf(s[1]));
    res[g >> 1][2 * (g & 1) + 1] = pk2(bf_lo(u1) + rbf(s[2]), bf_hi(u1) + rbf(s[3]));
  }
}
#define MM4(w0, w1, x0, x1) do { a[0] = MFMA(as_bf(w0), as_bf(x0), a[0]); a[1] = MFMA(as_bf(w1), as_bf(x0), a[1]); \
                                 a[2] = MFMA(as_bf(w0), as_bf(x1), a[2]); a[3] = MFMA(as_bf(w1), as_bf(x1), a[3]); } while (0)

DI void phase_wo(Ctx& c, int l, const u32x4 (&wo)[16], u32x4 (&res)[2]) {
  const unsigned xs = WS_L + l * L_SZ + L_O + ((2 * c.tb64 + (c.w >> 1)) * 32 + 8 * c.w) * 1024;
  const unsigned xo = WS_L + l * L_SZ + L_O + ((2 * c.tb64 + 1 - (c.w >> 1)) * 32 + 8 * c.w) * 1024;
  u32x4 xf[16];
#pragma unroll
  for (int k = 0; k < 8; ++k) { xf[2 * k] = ld16(c, c.voff, xs + k * 1024); xf[2 * k + 1] = ld16(c, c.voff, xo + k * 1024); }
  if (KV_G2 == 0) asm volatile("s_barrier" ::: "memory");
  SBAR();
  f32x16 a[4];
#pragma unroll
  for (int i = 0; i < 64; ++i) a[i >> 4][i & 15] = 0.f;
#pragma unroll
  for (int k = 0; k < 8; ++k) { MM4(wo[2 * k], wo[2 * k + 1], xf[2 * k], xf[2 * k + 1]); PIN(a[0]); PIN(a[1]); PIN(a[2]); PIN(a[3]); SBAR(); }
  if (KV_G2 == 1) asm volatile("s_barrier" ::: "memory");
  STAMP((3 + 5 * l) * 10 + 2);
  reduce_res(c, a, res, -1, (3 + 5 * l) * 10);
}

DI void phase_w2(Ctx& c, int l, const u32x4 (&w2r)[16], u32x4 (&res)[2]) {
  const int k0 = c.w * 21 + (c.w < 2 ? c.w : 2);
  const unsigned xs = WS_L + l * L_SZ + L_H + ((2 * c.tb64 + (c.w >> 1)) * KS_F + k0) * 1024;
  const unsigned xo = WS_L + l * L_SZ + L_H + ((2 * c.tb64 + 1 - (c.w >> 1)) * KS_F + k0) * 1024;
  constexpr int WIN = KV_WIN;
  u32x4 xf[2 * WIN];
#pragma unroll
  for (int k = 0; k < WIN; ++k) { xf[2 * k] = ld16(c, c.voff, xs + k * 1024); xf[2 * k + 1] = ld16(c, c.voff, xo + k * 1024); }
  SBAR();
  const LAS u32x4* w0 = (const LAS u32x4*)(c.lds + w2_run(2 * c.w + (c.w & 1))) + c.lane;
  const LAS u32x4* w1 = (const LAS u32x4*)(c.lds + w2_run(2 * c.w + 1 - (c.w & 1))) + c.lane;
  f32x16 a[4];
#pragma unroll
  for (int i = 0; i < 64; ++i) a[i >> 4][i & 15] = 0.f;
  u32x4 f0[2], f1[2];
  f0[0] = w0[0]; f1[0] = w1[0];
  static_for<0, 22>([&](auto kc) __attribute__((always_inline)) {
    constexpr int k = decltype(kc)::v;
    const u32x4 g0 = k < 14 ? f0[k & 1] : w2r[k < 14 ? 0 : 2 * (k - 14)], g1 = k < 14 ? f1[k & 1] : w2r[k < 14 ? 0 : 2 * (k - 14) + 1];
    const u32x4 x0 = xf[2 * (k % WIN)], x1 = xf[2 * (k % WIN) + 1];
    constexpr int kn = k - 1 + WIN;
    const int kk = (kn == 21 && c.w >= 2) ? 20 : kn;
    if constexpr (kn == 22) asm volatile("s_barrier" ::: "memory");
    if (k < 21 || c.w < 2) {
      a[0] = MFMA(as_bf(g0), as_bf(x0), a[0]);
      pin16(a[0]); SBAR();
      if constexpr (k >= 1 && kn < 22) xf[2 * ((k - 1) % WIN)] = ld16(c, c.voff, xs + kk * 1024);
      SBAR();
      a[1] = MFMA(as_bf(g1), as_bf(x0), a[1]);
      pin16(a[1]); SBAR();
      if constexpr (k + 1 < 14) f0[(k + 1) & 1] = w0[(k + 1) * 64];
      SBAR();
      a[2] = MFMA(as_bf(g0), as_bf(x1), a[2]);
      pin16(a[2]); SBAR();
      if constexpr (k >= 1 && kn < 22) xf[2 * ((k - 1) % WIN) + 1] = ld16(c, c.voff, xo + kk * 1024);
      SBAR();
      a[3] = MFMA(as_bf(g1), as_bf(x1), a[3]);
      pin16(a[3]); SBAR();
      if constexpr (k + 1 < 14) f1[(k + 1) & 1] = w1[(k + 1) * 64];
      SBAR();
    }
  });
  STAMP((5 + 5 * l) * 10 + 2);
  if (l == 0) { arrive4(c, 16); wait4(c, 16); } else BARRIER_LDS();
  reduce_res(c, a, res, l == 0 ? 17 : -1, (5 + 5 * l) * 10);
}

#define SMF(a, b8, c) __builtin_amdgcn_smfmac_f32_32x32x32_bf16((a), __builtin_bit_cast(bf16x16, b8), (c), sidx, 0, 0)
#define LDF(p) (*(const LAS bf16x8*)(p))
DI float max16(const f32x16& s) {
  float a = fmaxf(fmaxf(s[0], s[1]), s[2]), b = fmaxf(fmaxf(s[3], s[4]), s[5]);
  a = fmaxf(fmaxf(a, s[6]), s[7]); b = fmaxf(fmaxf(b, s[8]), s[9]);
  a = fmaxf(fmaxf(a, s[10]), s[11]); b = fmaxf(fmaxf(b, s[12]), s[13]);
  a = fmaxf(fmaxf(a, s[14]), s[15]);
  return swap_half_max(fmaxf(a, b));
}
DI void attn_slow(const LAS char* kp, const LAS char* vp, bool isv, bf16x8 q, f32x16& o, float& m) {
  const int sidx = 0xDDDDDDDD;
  f32x16 zero;
#pragma unroll
  for (int i = 0; i < 16; ++i) zero[i] = 0.f;
  o = zero; m = -3.0e38f;
  for (int t = 0; t < 32; ++t) {
    f32x16 s = MFMA(LDF(kp + t * 1024), q, zero);
    const float mn = fmaxf(m, max16(s));
    const float al = __builtin_amdgcn_exp2f(m - mn);
    m = mn;
#pragma unroll
    for (int i = 0; i < 16; ++i) { o[i] *= al; s[i] = __builtin_amdgcn_exp2f(s[i] - mn); }
    const int vo = isv ? t * 1024 : 0;
    o = SMF(LDF(vp + vo), LO8(s), o);
    o = SMF(LDF(vp + vo + 512), HI8(s), o);
  }
}

struct In { const unsigned short *x, *wqkv, *wo, *w1, *b1, *w2; unsigned short* out; };
struct Jobs { unsigned e[2][8], dst[2], csd[2]; bool ok[2]; };
DI void jobs_load(const Ctx& c, const In& in, Jobs& jb) {
  const int t0 = c.nb < 11 ? 3 * c.nb : 33 + 2 * (c.nb - 11), nt1 = c.nb < 11 ? 3 : 2;
#pragma unroll
  for (int u = 0; u < 2; ++u) {
    const int j = c.w - 1 + 7 * u;
    const unsigned short* W; int N, nt, ks;
    if (j < 6) { W = in.w1; N = F_; jb.ok[u] = j >= 0 && (j >> 1) < nt1; nt = t0 + (jb.ok[u] ? j >> 1 : 0); ks = 2 * c.tb + (j & 1); jb.dst[u] = WS_W1 + (nt * 32 + ks) * 1024; }
    else if (j < 8) { W = in.wo; N = D_; jb.ok[u] = c.w != 0; nt = 2 * c.nb8 + (j & 1); ks = c.tb64; jb.dst[u] = WS_WO + (nt * 32 + ks) * 1024; }
    else { W = in.w2; N = D_; ks = c.tb64 + 32 * ((j - 8) >> 1); jb.ok[u] = ks < KS_F; if (!jb.ok[u]) ks = 0; nt = 2 * c.nb8 + (j & 1); jb.dst[u] = WS_W2 + (nt * KS_F + ks) * 1024; }
    jb.csd[u] = WS_CSP + ((16 + c.nb) * 32 + ks) * 384 + (j >> 1) * 128;
    wfrag_load(W, N, nt, ks, c.r, c.h, jb.e[u]);
  }
}
DI void jobs_store(const Ctx& c, Jobs& jb) {
#pragma unroll
  for (int i = 0; i < 16; ++i) PIN(jb.e[i >> 3][i & 7]);
#pragma unroll
  for (int u = 0; u < 2; ++u)
    if (jb.ok[u]) wfrag_store(c, jb.e[u], jb.dst[u], c.w - 1 + 7 * u < 6, jb.csd[u]);
}

#define EX1(r) "v_exp_f32 v" #r ", v" #r "\n\t"
#define ASTEP(KOFF, VOFF, kc_, kn_, v0_, v1_, nv0_, pv1_)                                                                  \
  asm volatile("s_waitcnt lgkmcnt(0)\n\t" EX1(64) EX1(65)                                                                  \
               "v_smfmac_f32_32x32x32_bf16 %[o1], %[pv1], v[88:95], %[idx]\n\t"                                            \
               "ds_read_b128 %[nv0], %[vp] offset:%[vo]\n\tds_read_b128 %[pv1], %[vp] offset:%[vo1]\n\t"                   \
               EX1(66) EX1(67) EX1(68)                                                                                     \
               "v_mfma_f32_32x32x16_bf16 v[80:95], %[kc], %[q1], %[c1]\n\t"                                                \
               "ds_read_b128 %[kc], %[kp] offset:%[ko]\n\t"                                                                \
               EX1(69) EX1(70) EX1(71) EX1(72) EX1(73)                                                                     \
               "v_smfmac_f32_32x32x32_bf16 %[o0], %[v0], v[64:71], %[idx]\n\t"                                             \
               EX1(74) EX1(75) EX1(76) EX1(77) EX1(78) EX1(79) EX1(80) EX1(81)                                             \
               "v_smfmac_f32_32x32x32_bf16 %[o0], %[v1], v[72:79], %[idx]\n\t"                                             \
               EX1(82) EX1(83) EX1(84)                                                                                     \
               "v_mfma_f32_32x32x16_bf16 v[64:79], %[kn], %[q0], %[c0]\n\t"                                                \
               EX1(85) EX1(86) EX1(87) EX1(88) EX1(89)                                                                     \
               "v_smfmac_f32_32x32x32_bf16 %[o1], %[v0], v[80:87], %[idx]\n\t"                                             \
               EX1(90) EX1(91) EX1(92) EX1(93) EX1(94) "v_exp_f32 v95, v95"                                                \
               : "+{v[64:79]}"(sA), "+{v[80:95]}"(sB), [o0] "+v"(o0), [o1] "+v"(o1), [kc] "+v"(kc_), [nv0] "=&v"(nv0_),    \
                 [pv1] "+v"(pv1_)                                                                                          \
               : [kn] "v"(kn_), [v0] "v"(v0_), [v1] "v"(v1_), [q0] "v"(q0), [q1] "v"(q1), [c0] "v"(cin0), [c1] "v"(cin1),  \
                 [idx] "v"(sidx), [kp] "v"(kpa), [vp] "v"(vpa), [ko] "n"(KOFF), [vo] "n"(VOFF), [vo1] "n"((VOFF) + 512)     \
               : "memory")

template <int half, int l>
DI void phase_attn(Ctx& c, const In& in, u32x4 (&wo)[16], u32x4 (&hw)[28]) {
  const int qt = c.qb * 8 + (c.w & 3) * 2;
  const unsigned lay = WS_L + l * L_SZ;
  const char* kvg = c.ws + lay + ((c.w & 1) ? L_V : L_K) + c.head * 65536 + ((c.w >> 1) & 1) * 32768 + half * 4096;
  const unsigned kvd = c.lbase + ((c.w & 1) ? LD_V : LD_W) + ((c.w >> 1) & 1) * 32768 + half * 4096;
  u32x4 qv0, qv1;
  asm volatile("s_nop 4\n\tglobal_load_dwordx4 %0, %2, %3 sc1\n\tglobal_load_dwordx4 %1, %2, %3 offset:1024 sc1"
               : "=&v"(qv0), "=&v"(qv1) : "v"(c.voff), "s"(c.ws + lay + L_Q + (c.head * 64 + qt) * 1024) : "memory");
#define KV4(u) glds16x4(kvg + (u) * 8192, c.voff, kvd + (u) * 8192)
  glds16x4(kvg, c.voff, kvd);
  if (half == 0) {
    const unsigned o1 = (c.lane & 11) == 0 ? 0x3f803f80u : 0u;
    ((LAS u32x4*)(c.lds + LD_ONE))[c.w * 64 + c.lane] = (u32x4){o1, o1, o1, o1};
  }
  int sidx = 0xDDDDDDDD;
  const bool isv = c.r < 16;
  const LAS char* kp0 = c.lds + LD_W + half * 32768 + c.lane * 16;
  const LAS char* vp0 = c.lds + (isv ? LD_V + half * 32768 : LD_ONE) + c.h * 256 + (c.r & 15) * 16;
  const int vstep = isv ? 4096 : 0;
  if (half == 0) asm volatile("s_waitcnt vmcnt(0) lgkmcnt(0)\n\ts_barrier" : "+v"(qv0), "+v"(qv1)::"memory");
  else asm volatile("s_waitcnt vmcnt(4)\n\ts_barrier" : "+v"(qv0), "+v"(qv1)::"memory");
  STAMP((2 + 5 * l) * 10 + 1);
  if (half == 0) __builtin_amdgcn_s_setprio(1);
  const bf16x8 q0 = as_bf(qv0), q1 = as_bf(qv1);
  f32x16 zero;
#pragma unroll
  for (int i = 0; i < 16; ++i) zero[i] = 0.f;
  f32x16 cin0, cin1, o0 = zero, o1 = zero, sA, sB = zero;
  float m0, m1;
  bf16x8 ka = LDF(kp0), kb = LDF(kp0 + 1024), va0 = LDF(vp0), va1 = LDF(vp0 + 512), vb0, vb1 = as_bf((u32x4){0u, 0u, 0u, 0u});
  {
    const f32x16 s = MFMA(ka, q0, zero), s1 = MFMA(ka, q1, zero);
    m0 = max16(s); m1 = max16(s1);
#pragma unroll
    for (int i = 0; i < 16; ++i) { cin0[i] = -m0; cin1[i] = -m1; sA[i] = s[i] - m0; }
  }
  unsigned kpa = (unsigned)(size_t)kp0, vpa = (unsigned)(size_t)vp0;
  asm volatile("s_nop 1" : "+v"(cin0), "+v"(cin1), "+v"(sA), "+v"(sB), "+v"(o0), "+v"(o1), "+v"(sidx), "+v"(vb1));
#define QUAD(A, B, MID, C, D)                                                        \
  do {                                                                               \
    ASTEP(2048, 1024, ka, kb, va0, va1, vb0, vb1); A;                                \
    ASTEP(3072, 2048, kb, ka, vb0, vb1, va0, va1); B; MID;                           \
    ASTEP(4096, 3072, ka, kb, va0, va1, vb0, vb1); C;                                \
    vpa += vstep;                                                                    \
    ASTEP(5120, 0, kb, ka, vb0, vb1, va0, va1); D;                                   \
    kpa += 4096; QSTAMP();                                                           \
  } while (0)
#define NOP (void)0
#define QSTAMP()
#define WBAR(n) asm volatile("s_waitcnt vmcnt(" #n ")\n\ts_barrier" ::: "memory")
#define PREPPUB() do { if (half == 1 && c.w == 4 && c.lane < 2) \
    __hip_atomic_store(c.lane ? flag_ptr(c, FL_W1 + c.nb, c.tb) : flag_ptr(c, FL_W2 + c.nb8, c.tb64), c.token, RLX_AGENT); } while (0)
  Jobs jb;
  KV4(1);
  QUAD(KV4(2), NOP, WBAR(8), KV4(3), if (l == 0) { jobs_load(c, in, jb); SBAR(); });
  QUAD(NOP, NOP, WBAR(0); if (half == 1 && l == 1) (w2_fetch<0, 21>(c, hw)), if (l == 0) { jobs_store(c, jb); SBAR(); }, NOP);
  QUAD(NOP, NOP, if (l == 0) { WBAR(0); PREPPUB(); if (half == 1) { poll_any(c, FL_W2 + c.nb8, c.lane & 31); (w2_fetch<0, 21>(c, hw)); } }, NOP, NOP);
  STAMP((2 + 5 * l) * 10 + 5);
  for (int it = 3; it < 8; ++it) QUAD(NOP, NOP, NOP, NOP, NOP);
#undef WBAR
#undef PREPPUB
  asm volatile("s_nop 1\n\tv_smfmac_f32_32x32x32_bf16 %[o1], %[pv1], v[88:95], %[idx]\n\ts_waitcnt lgkmcnt(0)\n\ts_nop 7\n\ts_nop 7"
               : [o1] "+v"(o1), "+v"(o0), "+v"(ka), "+v"(kb), "+v"(va0), "+v"(va1) : [pv1] "v"(vb1), "{v[80:95]}"(sB), [idx] "v"(sidx));
#undef QUAD
#undef KV4
  STAMP((2 + 5 * l) * 10 + 2);
  if (__any(!(o0[8] < 1.0995116e12f))) attn_slow(kp0, vp0, isv, q0, o0, m0);
  if (__any(!(o1[8] < 1.0995116e12f))) attn_slow(kp0, vp0, isv, q1, o1, m1);
  LAS f32x4* cmb = (LAS f32x4*)(c.lds + LD_CMB) + (c.w & 3) * 320 + c.lane;
  const unsigned w1src = WS_W1 + (c.nb < 11 ? 3 * c.nb : 33 + 2 * (c.nb - 11)) * 32768;
  if (half == 0) {
    __builtin_amdgcn_s_setprio(0);
    cmb[0] = (f32x4){o0[0], o0[1], o0[2], o0[3]}; cmb[64] = (f32x4){o0[4], o0[5], o0[6], o0[7]};
    cmb[128] = (f32x4){o1[0], o1[1], o1[2], o1[3]}; cmb[192] = (f32x4){o1[4], o1[5], o1[6], o1[7]};
    cmb[256] = (f32x4){o0[8], m0, o1[8], m1};
    arrive4(c, 12 + l);
    if (l == 0) poll_any(c, c.lane < 32 ? FL_W1 + c.nb : FL_W2 + c.nb8, c.lane < 32 ? (c.lane & 15) : (c.lane & 31));
    const unsigned wos = WS_WO + ((2 * c.nb8 + (c.w & 1)) * 32 + 8 * c.w) * 1024;
    const unsigned woo = WS_WO + ((2 * c.nb8 + 1 - (c.w & 1)) * 32 + 8 * c.w) * 1024;
#pragma unroll
    for (int k = 0; k < 8; ++k) { wo[2 * k] = ld16(c, c.voff, wos + k * 1024); wo[2 * k + 1] = ld16(c, c.voff, woo + k * 1024); }
    SBAR();
    wait4<true>(c, 12 + l);
    help_dma<8>(c, w1src, LD_W);
    if (c.nb < 11) help_dma<8>(c, w1src + 65536, LD_W + 65536);
  }
  STAMP((2 + 5 * l) * 10 + 3);
  BARRIER_LDS();
  STAMP((2 + 5 * l) * 10 + 4);
  if (half == 1) {
    const f32x4 lm = cmb[256];
#pragma unroll
    for (int u = 0; u < 2; ++u) {
      const f32x16& o = u ? o1 : o0; const float m = u ? m1 : m0;
      const f32x8 p2 = cat8(cmb[128 * u], cmb[128 * u + 64]);
      const float m2 = lm[2 * u + 1];
      const float mm = fmaxf(m, m2);
      const float f1 = __builtin_amdgcn_exp2f(m - mm), f2 = __builtin_amdgcn_exp2f(m2 - mm);
      const float inv = __builtin_amdgcn_rcpf(o[8] * f1 + lm[2 * u] * f2);
      const float g1 = f1 * inv, g2 = f2 * inv;
      unsigned p[4];
#pragma unroll
      for (int i = 0; i < 4; ++i) p[i] = pk2(o[2 * i] * g1 + p2[2 * i] * g2, o[2 * i + 1] * g1 + p2[2 * i + 1] * g2);
      st16(c, (u32x4){p[0], p[1], p[2], p[3]}, c.voff, lay + L_O + ((qt + u) * 32 + c.head) * 1024);
    }
    STAMP((2 + 5 * l) * 10 + 6);
    asm volatile("s_waitcnt vmcnt(0)" ::: "memory");
    if (c.lane < 8) __hip_atomic_store(flag_ptr(c, FL_O + l * 256 + (c.qb * 4 + (c.w & 3)) * 8 + c.lane, c.head), c.token, RLX_AGENT);
  }
}

DI u64 uniform64(u64 v) {
  return ((u64)__builtin_amdgcn_readfirstlane((unsigned)(v >> 32)) << 32) | (unsigned)__builtin_amdgcn_readfirstlane((unsigned)v);
}

DI void run_main(Ctx& c, const In& in) {
  const u64 epoch = __hip_atomic_load((gu64*)c.ws, RLX_AGENT);
  u32x4 res[2];
  {
    if (c.w == 0 && c.lane < 32) ((LAS unsigned*)(c.lds + LD_CTR))[c.lane] = 0u;
    const unsigned short* src = in.x + (size_t)((2 * c.tb64 + (c.w >> 1)) * 32 + c.r) * D_ + 32 * (2 * c.nb8 + (c.w & 1)) + 4 * c.h;
    u32x2 xr[4];
#pragma unroll
    for (int i = 0; i < 4; ++i) xr[i] = *(const u32x2*)(src + 8 * i);
    BARRIER_LDS();
    c.token = uniform64(epoch) + 1;
    res[0] = (u32x4){xr[0][0], xr[0][1], xr[1][0], xr[1][1]}; res[1] = (u32x4){xr[2][0], xr[2][1], xr[3][0], xr[3][1]};
    store_x(c, 0, res);
    publish(c, 0, 4, FL_X + c.tb * 16, c.nb);
    STAMP(1);
    poll(c, (c.lane & 16) ? FL_WQ + c.nb * 16 + c.tb : FL_X + c.tb * 16 + c.nb, c.lane & 15);
    STAMP(2);
    BARRIER_LDS();
    STAMP(11);
    u32x4 dummy[16];
    fresh(c);
    phase_ln<0, true>(c, 0, 0, dummy);
  }
  for (int l = 0; l < 2; ++l) {
    u32x4 wo[16], w2r[16];
    if (l == 1) {
      STAMP((1 + 5 * l) * 10 + 1);
      fresh(c);
      u32x4 dummy[16];
      phase_ln<0, false>(c, l, 2 * l, dummy);
    }
    fresh(c);
    STAMP((1 + 5 * l) * 10 + 9);
    poll(c, FL_Q + l * 256 + c.head * 8 + c.qb, c.lane & 15);
    BARRIER_LDS();
    STAMP((2 + 5 * l) * 10);
    { u32x4 unused[28]; if (l == 0) phase_attn<0, 0>(c, in, wo, unused); else phase_attn<0, 1>(c, in, wo, unused); }
    fresh(c);
    STAMP((2 + 5 * l) * 10 + 9);
    poll(c, FL_O + l * 256 + c.tb64 * 8 + c.nb8, c.lane & 31);
    BARRIER_LDS();
    STAMP((3 + 5 * l) * 10 + 1);
    phase_wo(c, l, wo, res);
    store_x(c, 2 * l + 1, res);
    STAMP((3 + 5 * l) * 10 + 6);
    publish(c, 8 + l, 4, FL_X + (2 * l + 1) * 256 + c.tb * 16, c.nb);
    fresh(c);
    STAMP((3 + 5 * l) * 10 + 9);
    poll(c, FL_X + (2 * l + 1) * 256 + c.tb * 16 + c.nb, c.lane & 15);
    BARRIER_ALL();
    STAMP((4 + 5 * l) * 10 + 1);
    phase_ln<1, false>(c, l, 2 * l + 1, w2r);
    fresh(c);
    STAMP((4 + 5 * l) * 10 + 9);
    poll(c, FL_H + l * 256 + c.tb * 16 + c.nb, c.lane & 15);
    STAMP((4 + 5 * l) * 10 + 8);
    BARRIER_LDS();
    STAMP((5 + 5 * l) * 10 + 1);
    phase_w2(c, l, w2r, res);
    fresh(c);
    if (l == 0) {
      store_x(c, 2, res);
      STAMP((5 + 5 * l) * 10 + 6);
      publish(c, 10, 4, FL_X + 2 * 256 + c.tb * 16, c.nb);
      STAMP((5 + 5 * l) * 10 + 9);
      poll(c, FL_X + 2 * 256 + c.tb * 16 + c.nb, c.lane & 15);
      STAMP((5 + 5 * l) * 10 + 8);
      BARRIER_LDS();
    }
  }
  {
    auto s0 = __builtin_amdgcn_permlane32_swap(res[0][0], res[0][2], false, false), s1 = __builtin_amdgcn_permlane32_swap(res[0][1], res[0][3], false, false);
    auto s2 = __builtin_amdgcn_permlane32_swap(res[1][0], res[1][2], false, false), s3 = __builtin_amdgcn_permlane32_swap(res[1][1], res[1][3], false, false);
    unsigned short* op = in.out + (size_t)((2 * c.tb64 + (c.w >> 1)) * 32 + c.r) * D_ + 32 * (2 * c.nb8 + (c.w & 1)) + 8 * c.h;
    STORE16(op, ((u32x4){s0[0], s1[0], s0[1], s1[1]}));
    STORE16(op + 16, ((u32x4){s2[0], s3[0], s2[1], s3[1]}));
  }
  STAMP(109);
  if (c.bid == 0 && c.w == 0 && c.lane == 0) __hip_atomic_store((gu64*)c.ws, c.token, RLX_AGENT);
}

DI void run_help(Ctx& c, const In& in) {
  const u64 epoch = __hip_atomic_load((gu64*)c.ws, RLX_AGENT);
  {
    unsigned e0[8], e1[8];
    const int qk = 2 * c.tb + (c.w & 1), qa = (c.w >> 1) & 1;
    wfrag_load(in.wqkv, 3 * D_, qa * 16 + c.nb, qk, c.r, c.h, e0);
    wfrag_load(in.wqkv, 3 * D_, 32 + c.nb, qk, c.r, c.h, e1);
    const int t = (c.w & 3) * 64 + c.lane;
    const int bcol = 32 * (c.nb < 11 ? 3 * c.nb : 33 + 2 * (c.nb - 11)) + t;
    const unsigned bv = in.b1[bcol < F_ ? bcol : 0];
    BARRIER_LDS();
    c.token = uniform64(epoch) + 1;
    wfrag_store(c, e0, WS_WQKV + ((qa * 16 + c.nb) * 32 + qk) * 1024, true, WS_CSP + (c.nb * 32 + qk) * 384 + qa * 128);
    if (qa == 0) wfrag_store(c, e1, WS_WQKV + ((32 + c.nb) * 32 + qk) * 1024, true, WS_CSP + (c.nb * 32 + qk) * 384 + 256);
    publish(c, 1, 4, FL_WQ + c.nb * 16, c.tb);
    STAMP(1);
    if (t < 96) ((LAS float*)(c.lds + LD_PAR))[192 + t] = __uint_as_float(bv << 16);
    BARRIER_LDS();
    STAMP(11);
    fresh(c);
#define WQC(q) _Pragma("unroll") for (int t_ = 0; t_ < 3; ++t_) \
    glds16x2(c.ws + WS_WQKV + ((16 * t_ + c.nb) * 32 + 8 * (q) + 2 * (c.w & 3)) * 1024, c.voff, c.lbase + LD_W + t_ * 32768 + (8 * (q) + 2 * (c.w & 3)) * 1024)
    WQC(0); WQC(1);
    asm volatile("s_waitcnt vmcnt(6)\n\ts_barrier" ::: "memory");
    WQC(2);
    asm volatile("s_waitcnt vmcnt(6)\n\ts_barrier" ::: "memory");
    WQC(3);
    asm volatile("s_waitcnt vmcnt(6)\n\ts_barrier" ::: "memory");
    asm volatile("s_waitcnt vmcnt(0)\n\ts_barrier" ::: "memory");
#undef WQC
    help_colsum(c, 0);
    STAMP(12);
    help_ln<0, true>(c, 0);
  }
  for (int l = 0; l < 2; ++l) {
    u32x4 wo[16];
    fresh(c);
    if (l == 1) help_ln<0, false>(c, l);
    STAMP((1 + 5 * l) * 10 + 9);
    BARRIER_LDS();
    STAMP((2 + 5 * l) * 10);
    fresh(c);
    u32x4 hw[28];
    if (l == 0) phase_attn<1, 0>(c, in, wo, hw); else phase_attn<1, 1>(c, in, wo, hw);
    fresh(c);
    STAMP((2 + 5 * l) * 10 + 9);
    BARRIER_LDS();
    asm volatile("s_barrier" ::: "memory");
    help_dma<8>(c, WS_W1 + (c.nb < 11 ? 3 * c.nb + 1 : 34 + 2 * (c.nb - 11)) * 32768, LD_W + 32768);
    if (KV_G2 == 1) asm volatile("s_barrier" ::: "memory");
    w2_fetch<21, 28>(c, hw);
    if (KV_G2 == 0) asm volatile("s_barrier" ::: "memory");
    if (l == 0) help_colsum(c, 1);
    BARRIER_ALL();
    STAMP((4 + 5 * l) * 10 + 1);
    w2_put<21, 28>(c, hw);
    STAMP((4 + 5 * l) * 10 + 2);
    help_ln<1, false>(c, l);
    STAMP((4 + 5 * l) * 10 + 6);
    w2_put<0, 21>(c, hw);
    STAMP((4 + 5 * l) * 10 + 9);
    BARRIER_LDS();
    STAMP((5 + 5 * l) * 10 + 1);
    asm volatile("s_barrier" ::: "memory");
    if (l == 0) {
      fresh(c);
#pragma unroll
      for (int u = 0; u < 24; ++u) hw[u] = ld16(c, c.voff, WS_WQKV + (((u >> 3) * 16 + c.nb) * 32 + (u & 7) * 4 + (c.w & 3)) * 1024);
      SBAR();
      STAMP((5 + 5 * l) * 10 + 2);
    }
    if (l == 0) {
      wait4<true>(c, 16);
#pragma unroll
      for (int u = 0; u < 24; ++u) *(LAS u32x4*)(c.lds + LD_W + (u * 4 + (c.w & 3)) * 1024 + c.voff) = hw[u];
      STAMP((5 + 5 * l) * 10 + 9);
      BARRIER_LDS();
    } else {
      asm volatile("s_barrier\n\ts_barrier" ::: "memory");
    }
  }
}

__global__ __launch_bounds__(512, 2) void mega(const unsigned short* __restrict__ x, const unsigned short* __restrict__ wqkv,
                                               const unsigned short* __restrict__ wo_g, const unsigned short* __restrict__ w1,
                                               const unsigned short* __restrict__ b1, const unsigned short* __restrict__ w2,
                                               char* __restrict__ ws, unsigned short* __restrict__ out) {
  extern __shared__ __attribute__((aligned(16))) char lds_[];
  Ctx c;
  c.ws = ws; c.lds = (LAS char*)lds_; c.rs = __builtin_amdgcn_make_buffer_rsrc(ws, 0, WS_END, 0x00020000);
  c.lbase = (unsigned)(size_t)c.lds;
  c.bid = blockIdx.x; c.lane = threadIdx.x & 63; c.w = __builtin_amdgcn_readfirstlane(threadIdx.x >> 6); c.dead = false;
  fresh(c);
  STAMP(0);
  const In in = {x, wqkv, wo_g, w1, b1, w2, out};
  if (c.w < 4) run_main(c, in); else run_help(c, in);
}

extern "C" void kernel_launch(void* const* d_in, const int* in_sizes, int n_in, void* d_out, int out_size, void* d_ws,
                              size_t ws_size, hipStream_t stream) {
  (void)hipFuncSetAttribute((const void*)mega, hipFuncAttributeMaxDynamicSharedMemorySize, LDS_BYTES);
  mega<<<256, 512, LDS_BYTES, stream>>>((const unsigned short*)d_in[0], (const unsigned short*)d_in[1], (const unsigned short*)d_in[2],
                                        (const unsigned short*)d_in[4], (const unsigned short*)d_in[5], (const unsigned short*)d_in[6],
                                        (char*)d_ws, (unsigned short*)d_out);
}
```

```cpp
#include <hip/hip_runtime.h>
#include <hip/hip_bf16.h>

typedef __attribute__((ext_vector_type(2))) __bf16 bf16x2;
typedef __attribute__((ext_vector_type(8))) __bf16 bf16x8;
typedef __attribute__((ext_vector_type(16))) __bf16 bf16x16;
typedef __attribute__((ext_vector_type(16))) float f32x16;
typedef __attribute__((ext_vector_type(8))) float f32x8;
typedef __attribute__((ext_vector_type(4))) float f32x4;
typedef __attribute__((ext_vector_type(2))) float f32x2;
typedef __attribute__((ext_vector_type(4))) unsigned u32x4;
typedef __attribute__((ext_vector_type(2))) unsigned u32x2;
typedef unsigned long long u64;
#define LAS __attribute__((address_space(3)))
#define GAS __attribute__((address_space(1)))
typedef GAS u64 gu64;
#define DI __device__ __forceinline__
#define RLX_AGENT __ATOMIC_RELAXED, __HIP_MEMORY_SCOPE_AGENT

constexpr int S_ = 2048, D_ = 512, F_ = 1376;
constexpr int KS_F = F_ / 16;

constexpr unsigned MB = 1u << 20;
constexpr unsigned WS_FLAG = 0;
constexpr unsigned WS_WQKV = 1024 * 1024;
constexpr unsigned WS_WO = WS_WQKV + 48 * 32 * 1024;
constexpr unsigned WS_W1 = WS_WO + 16 * 32 * 1024;
constexpr unsigned WS_W2 = WS_W1 + 43 * 32 * 1024;
constexpr unsigned WS_CSP = WS_W2 + 16 * 86 * 1024;
constexpr unsigned WS_ST = WS_CSP + 2 * 16 * 32 * 384;
constexpr unsigned WS_X = WS_ST + 4 * 64 * 4096;
constexpr unsigned WS_L = WS_X + 8 * MB;
constexpr unsigned L_Q = 0, L_K = 2 * MB, L_V = 4 * MB, L_O = 6 * MB, L_H = 8 * MB, L_SZ = 14 * MB;
constexpr unsigned WS_END = WS_L + 2 * L_SZ;
constexpr int FL_X = 1;
constexpr int FL_H = FL_X + 1024;
constexpr int FL_Q = FL_H + 512;
constexpr int FL_O = FL_Q + 512;
constexpr int FL_WQ = FL_O + 512;
constexpr int FL_W1 = FL_WQ + 256;
constexpr int FL_W2 = FL_W1 + 16;
static_assert((FL_W2 + 8) * 256 <= WS_WQKV, "flag region");

constexpr int LD_W = 0;
constexpr int LD_V = 65536;
constexpr int LD_EX = 98304;
constexpr int LD_ONE = 131072;
constexpr int LD_CMB = 135168;
constexpr int LD_W2E = 122880;
constexpr int LD_SC = 151552;
constexpr int LD_ST = 155648;
constexpr int LD_PAR = 157696;
constexpr int LD_CTR = LD_PAR + 1280;
constexpr int LD_DBG = LD_CTR + 512;
constexpr int LDS_BYTES = 163840;

#define MFMA(a, b, c) __builtin_amdgcn_mfma_f32_32x32x16_bf16((a), (b), (c), 0, 0, 0)
#define SBAR() __builtin_amdgcn_sched_barrier(0)
#define PIN(x) asm volatile("" : "+v"(x))
#define BARRIER_LDS() asm volatile("s_waitcnt lgkmcnt(0)\n\ts_barrier" ::: "memory")
#define BARRIER_ALL() asm volatile("s_waitcnt vmcnt(0) lgkmcnt(0)\n\ts_barrier" ::: "memory")

#define STAMP(i)

DI unsigned pk2(float lo, float hi) {
  bf16x2 v; v[0] = (__bf16)lo; v[1] = (__bf16)hi;
  return __builtin_bit_cast(unsigned, v);
}
DI float bf_lo(unsigned u) { return __uint_as_float(u << 16); }
DI float bf_hi(unsigned u) { return __uint_as_float(u & 0xffff0000u); }
DI float rbf(float x) { return (float)(__bf16)x; }
DI bf16x8 as_bf(u32x4 v) { return __builtin_bit_cast(bf16x8, v); }
DI float swap_half_sum(float x) {
  auto r = __builtin_amdgcn_permlane32_swap(__float_as_uint(x), __float_as_uint(x), false, false);
  return __uint_as_float(r[0]) + __uint_as_float(r[1]);
}
DI float swap_half_max(float x) {
  auto r = __builtin_amdgcn_permlane32_swap(__float_as_uint(x), __float_as_uint(x), false, false);
  return fmaxf(__uint_as_float(r[0]), __uint_as_float(r[1]));
}
DI void glds16(const char* sbase, unsigned voff, unsigned lds_base) {
  unsigned sv;
  asm volatile("s_mov_b32 %0, m0\n\ts_mov_b32 m0, %3\n\ts_nop 2\n\tglobal_load_lds_dwordx4 %1, %2 sc1\n\ts_mov_b32 m0, %0"
               : "=&s"(sv) : "v"(voff), "s"(sbase), "s"(lds_base) : "memory");
}
DI void glds16x2(const char* sbase, unsigned voff, unsigned lds_base) {
  unsigned sv;
  asm volatile("s_mov_b32 %0, m0\n\ts_mov_b32 m0, %3\n\ts_nop 2\n\tglobal_load_lds_dwordx4 %1, %2 sc1\n\t"
               "global_load_lds_dwordx4 %1, %2 offset:1024 sc1\n\ts_mov_b32 m0, %0"
               : "=&s"(sv) : "v"(voff), "s"(sbase), "s"(lds_base) : "memory");
}
DI void glds16x4(const char* sbase, unsigned voff, unsigned lds_base) {
  unsigned sv;
  asm volatile("s_mov_b32 %0, m0\n\ts_mov_b32 m0, %3\n\ts_nop 2\n\tglobal_load_lds_dwordx4 %1, %2 sc1\n\t"
               "global_load_lds_dwordx4 %1, %2 offset:1024 sc1\n\tglobal_load_lds_dwordx4 %1, %2 offset:2048 sc1\n\t"
               "global_load_lds_dwordx4 %1, %2 offset:3072 sc1\n\ts_mov_b32 m0, %0"
               : "=&s"(sv) : "v"(voff), "s"(sbase), "s"(lds_base) : "memory");
}
template <int I> struct IC { static constexpr int v = I; };
template <int I, int N, class F> DI void static_for(F&& f) {
  if constexpr (I < N) { f(IC<I>{}); static_for<I + 1, N>(f); }
}

struct Ctx {
  char* ws; LAS char* lds; __amdgpu_buffer_rsrc_t rs;
  u64 token; unsigned lbase, voff;
  int bid, lane, w, r, h, tb, nb, tb64, nb8, head, qb;
  bool dead;
};
DI void fresh(Ctx& c) {
  asm volatile("" : "+s"(c.bid));
  asm volatile("" : "+s"(c.w));
  asm volatile("" : "+v"(c.lane));
  const int g = c.bid & 7, idx = c.bid >> 3;
  c.r = c.lane & 31; c.h = c.lane >> 5; c.voff = c.lane * 16;
  c.tb = 2 * g + (idx >> 4); c.nb = idx & 15; c.tb64 = 4 * g + (idx >> 3); c.nb8 = idx & 7;
  c.head = 4 * g + (idx & 3); c.qb = idx >> 2;
}
DI u32x4 ld16(const Ctx& c, unsigned voff, unsigned soff) { return __builtin_amdgcn_raw_buffer_load_b128(c.rs, voff, soff, 16); }
DI float ld4(const Ctx& c, unsigned voff, unsigned soff) { return __uint_as_float(__builtin_amdgcn_raw_buffer_load_b32(c.rs, voff, soff, 16)); }
DI void st16(const Ctx& c, u32x4 v, unsigned voff, unsigned soff) { __builtin_amdgcn_raw_buffer_store_b128(v, c.rs, voff + soff, 0, 16); }
DI void st4(const Ctx& c, float v, unsigned voff, unsigned soff) { __builtin_amdgcn_raw_buffer_store_b32(__float_as_uint(v), c.rs, voff, soff, 16); }
DI gu64* flag_ptr(const Ctx& c, int line, int slot) { return (gu64*)(c.ws + WS_FLAG) + line * 32 + slot; }

DI void publish(const Ctx& c, int ctr, unsigned n, int line0, int slot) {
  asm volatile("s_waitcnt vmcnt(0)" ::: "memory");
  unsigned old = 0;
  if (c.lane == 0) old = __hip_atomic_fetch_add((LAS unsigned*)(c.lds + LD_CTR) + ctr, 1u, __ATOMIC_RELAXED, __HIP_MEMORY_SCOPE_WORKGROUP);
  if (__builtin_amdgcn_readfirstlane(old) == n - 1 && c.lane < 16) __hip_atomic_store(flag_ptr(c, line0 + c.lane, slot), c.token, RLX_AGENT);
}
DI void poll_any(Ctx& c, int line, int slot) {
  if (!c.dead) {
    gu64* p = flag_ptr(c, line, slot);
    unsigned spins = 0;
    while (!__all(__hip_atomic_load(p, RLX_AGENT) == c.token)) {
      if (++spins > (1u << 18)) { c.dead = true; break; }
    }
    __builtin_amdgcn_fence(__ATOMIC_ACQUIRE, "wavefront");
  }
}
template <bool NAP = false> DI void wait4(const Ctx& c, int ctr) {
  while (__hip_atomic_load((LAS unsigned*)(c.lds + LD_CTR) + ctr, __ATOMIC_RELAXED, __HIP_MEMORY_SCOPE_WORKGROUP) < 4u)
    if (NAP) __builtin_amdgcn_s_sleep(2);
  asm volatile("" ::: "memory");
}
DI void arrive4(const Ctx& c, int ctr) {
  asm volatile("s_waitcnt lgkmcnt(0)" ::: "memory");
  if (c.lane == 0) __hip_atomic_fetch_add((LAS unsigned*)(c.lds + LD_CTR) + ctr, 1u, __ATOMIC_RELAXED, __HIP_MEMORY_SCOPE_WORKGROUP);
}
DI void poll(Ctx& c, int line, int slot) { if (c.w == 0) poll_any(c, line, slot); }
template <int NQ> DI void help_dma(const Ctx& c, unsigned src, int dst) {
#pragma unroll
  for (int u = 0; u < NQ / 4; ++u) {
    const int p = (c.w & 3) + 4 * u;
    glds16x4(c.ws + src + p * 4096, c.voff, c.lbase + dst + p * 4096);
  }
}
DI int w2_run(int run) { return run < 6 ? LD_W + run * 14336 : LD_W2E + (run - 6) * 14336; }
template <int U0, int U1> DI void w2_fetch(const Ctx& c, u32x4 (&h)[28]) {
#pragma unroll
  for (int u = U0; u < U1; ++u) {
    const int f = (c.w & 3) + 4 * u, run = f / 14, kq = run >> 1;
    h[u] = ld16(c, c.voff, WS_W2 + ((2 * c.nb8 + (run & 1)) * KS_F + kq * 21 + (kq < 2 ? kq : 2) + f % 14) * 1024);
  }
  SBAR();
}
template <int U0, int U1> DI void w2_put(const Ctx& c, const u32x4 (&h)[28]) {
#pragma unroll
  for (int u = U0; u < U1; ++u) {
    const int f = (c.w & 3) + 4 * u;
    *(LAS u32x4*)(c.lds + (u < 21 ? LD_W : LD_W2E - 84 * 1024) + f * 1024 + c.voff) = h[u];
  }
}

DI void store_x(const Ctx& c, int inst, u32x4 (&res)[2]) {
  const int tt = 2 * c.tb64 + (c.w >> 1), nt = 2 * c.nb8 + (c.w & 1);
  st16(c, res[0], c.voff, WS_X + inst * (2 * MB) + (tt * 32 + 2 * nt) * 1024);
  st16(c, res[1], c.voff, WS_X + inst * (2 * MB) + (tt * 32 + 2 * nt + 1) * 1024);
  float s1 = 0.f, s2 = 0.f;
#pragma unroll
  for (int i = 0; i < 8; ++i) {
    const unsigned u = res[i >> 2][i & 3];
    const float a = bf_lo(u), b = bf_hi(u);
    s1 += a + b; s2 = fmaf(a, a, fmaf(b, b, s2));
  }
  auto r1 = __builtin_amdgcn_permlane32_swap(__float_as_uint(s1), __float_as_uint(s2), false, false);
  st4(c, __uint_as_float(r1[0]) + __uint_as_float(r1[1]), c.lane * 4, WS_ST + inst * (64 * 4096) + tt * 4096 + nt * 256);
  PIN(res[0]); PIN(res[1]);
}

DI void wfrag_load(const unsigned short* W, int N, int nt, int ks, int r, int h, unsigned (&e)[8]) {
#pragma unroll
  for (int j = 0; j < 8; ++j) e[j] = W[(size_t)(16 * ks + 8 * (j >> 2) + 4 * h + (j & 3)) * N + 32 * nt + r];
}
DI void wfrag_store(const Ctx& c, const unsigned (&e)[8], unsigned dst, bool docs, unsigned csdst) {
  u32x4 v;
#pragma unroll
  for (int j = 0; j < 4; ++j) v[j] = e[2 * j] | (e[2 * j + 1] << 16);
  st16(c, v, c.voff, dst);
  if (docs) {
    float s = 0.f;
#pragma unroll
    for (int j = 0; j < 8; ++j) s += __uint_as_float(e[j] << 16);
    s = swap_half_sum(s);
    if (c.h == 0) st4(c, s, c.r * 4, csdst);
  }
}

DI float mish_f(float hh) {
  const float u = __builtin_amdgcn_exp2f(hh * 1.4426950408889634f);
  const float r = __builtin_amdgcn_rcpf(fmaf(u, u + 2.f, 2.f));
  return hh * fmaf(-2.f, r, 1.f);
}
template <int MODE>
DI u32x2 epi4(float a0, float a1, float a2, float a3, f32x4 cs, f32x4 bb, float mr, float rstd) {
  const float a[4] = {a0, a1, a2, a3};
  float v[4];
#pragma unroll
  for (int j = 0; j < 4; ++j) {
    if (MODE == 0) v[j] = fmaf(a[j], rstd, mr * cs[j]);
    else v[j] = mish_f(fmaf(a[j], rstd, fmaf(mr, cs[j], bb[j])));
  }
  return (u32x2){pk2(v[0], v[1]), pk2(v[2], v[3])};
}
template <int MODE>
DI u32x4 epi8(const f32x8& a, const f32x4* cs, const f32x4* bb, float mr, float rstd) {
  const u32x2 lo = epi4<MODE>(a[0], a[1], a[2], a[3], cs[0], bb[0], mr, rstd);
  const u32x2 hi = epi4<MODE>(a[4], a[5], a[6], a[7], cs[1], bb[1], mr, rstd);
  return (u32x4){lo[0], lo[1], hi[0], hi[1]};
}
#define LO8(s) __builtin_shufflevector(s, s, 0, 1, 2, 3, 4, 5, 6, 7)
#define HI8(s) __builtin_shufflevector(s, s, 8, 9, 10, 11, 12, 13, 14, 15)
DI f32x4 q4(const f32x8& v, int g) { return g ? (f32x4){v[4], v[5], v[6], v[7]} : (f32x4){v[0], v[1], v[2], v[3]}; }
DI f32x8 cat8(f32x4 a, f32x4 b) { return __builtin_shufflevector(a, b, 0, 1, 2, 3, 4, 5, 6, 7); }

struct LnPar { f32x4 cs[6], bb[6]; float csv; };
template <int MODE, int T> DI void ln_params(const Ctx& c, LnPar& p) {
  const LAS float* par = (const LAS float*)(c.lds + LD_PAR) + MODE * 96 + 4 * c.h;
#pragma unroll
  for (int g = 0; g < 6; ++g) {
    const int o = g < 4 ? 32 * T + 8 * g : 64 + 16 * T + 8 * (g - 4);
    if (MODE == 1 || g < 4) p.cs[g] = *(const LAS f32x4*)(par + o);
    if (MODE == 1) p.bb[g] = *(const LAS f32x4*)(par + 96 + o);
  }
  if (MODE == 0) p.csv = ((const LAS float*)(c.lds + LD_PAR))[64 + c.r];
}
template <int T> DI void ln_tokstats(const Ctx& c, f32x4 (&ts)[4]) {
  const LAS f32x2* st = (const LAS f32x2*)(c.lds + LD_ST) + (c.w & 3) * 32 + 16 * T + 4 * c.h;
#pragma unroll
  for (int g = 0; g < 4; ++g) ts[g] = *(const LAS f32x4*)(st + 8 * (g >> 1) + 2 * (g & 1));
}
template <int MODE, int T>
DI void ln_epilogue(Ctx& c, int l, const f32x16& a, const f32x8& ch, float mean, float rstd, const LnPar& p, const f32x4 (&ts)[4]) {
  const int tt = 4 * c.tb + (c.w & 3);
  const unsigned lay = WS_L + l * L_SZ;
  if (MODE == 0) {
    const float rs = T ? rstd : rstd * (0.25f * 1.4426950408889634f), mr = -mean * rs;
    const unsigned vo = T ? c.h * 512 + ((c.r & 19) | ((c.r & 4) << 1) | ((c.r & 8) >> 1)) * 16 : c.voff;
    const unsigned so = lay + (T ? L_K : L_Q) + (2 * c.nb * 64 + tt) * 1024;
    st16(c, epi8<0>(LO8(a), p.cs, p.cs, mr, rs), vo, so);
    st16(c, epi8<0>(HI8(a), p.cs + 2, p.cs, mr, rs), vo, so + 65536);
    unsigned q[4];
#pragma unroll
    for (int g = 0; g < 4; ++g) q[g] = pk2((ch[2 * g] - ts[g][0] * p.csv) * ts[g][1], (ch[2 * g + 1] - ts[g][2] * p.csv) * ts[g][3]);
    st16(c, (u32x4){q[0], q[1], q[2], q[3]}, (c.r >> 4) * 65536 + c.h * 256 + (c.r & 15) * 16, lay + L_V + (2 * c.nb * 128 + 2 * tt + T) * 512);
    publish(c, 4 + l, 8, FL_Q + l * 256 + c.nb * 16, c.tb);
  } else {
    const float mr = -mean * rstd;
    const unsigned so = lay + L_H + (tt * KS_F + 2 * (c.nb < 11 ? 3 * c.nb : 33 + 2 * (c.nb - 11))) * 1024;
    st16(c, epi8<1>(LO8(a), p.cs, p.bb, mr, rstd), c.voff, so + T * 2048);
    st16(c, epi8<1>(HI8(a), p.cs + 2, p.bb + 2, mr, rstd), c.voff, so + T * 2048 + 1024);
    if (c.nb < 11) st16(c, epi8<1>(ch, p.cs + 4, p.bb + 4, mr, rstd), c.voff, so + 4096 + T * 1024);
    publish(c, 6 + l, 8, FL_H + l * 256 + c.tb * 16, c.nb);
  }
}

DI void pin16(f32x16& v) { PIN(v); }
DI u32x4 wfrag(const LAS char* base, int ks) { return *(const LAS u32x4*)(base + ks * 1024); }

template <int MODE, bool FIRST>
DI void phase_ln(Ctx& c, int l, int inst, u32x4 (&w2r)[16]) {
  const int PH = (1 + 5 * l + (MODE ? 3 : 0)) * 10; (void)PH;
  const int tt = 4 * c.tb + c.w;
  const bool has3 = MODE == 0 || c.nb < 11;
  const unsigned xs = WS_X + inst * (2 * MB) + tt * 32768;
  constexpr int PD = 12;
  u32x4 xf[PD], sp[4];
#pragma unroll
  for (int k = 0; k < PD; ++k) xf[k] = ld16(c, c.voff, xs + k * 1024);
  SBAR();
  const LAS char* wl = c.lds + LD_W + c.lane * 16;
  const LAS char* wl2 = wl + 65536;
  asm volatile("" : "+v"(wl2));
  f32x16 aA, aB, aC;
#pragma unroll
  for (int i = 0; i < 16; ++i) { aA[i] = 0.f; aB[i] = 0.f; aC[i] = 0.f; }
  u32x4 fa[3], fb[3], fc[3];
  if (FIRST) BARRIER_LDS();
#pragma unroll
  for (int k = 0; k < 2; ++k) { fa[k] = wfrag(wl, k); fb[k] = wfrag(wl + 32768, k); fc[k] = wfrag(wl2, k); }
  float mean = 0.f, rstd = 0.f, sc4[4];
  LnPar par; f32x4 ts[4];
  LAS float* sc = (LAS float*)(c.lds + LD_SC) + c.w * 256;
  const int k0q = c.w * 21 + (c.w < 2 ? c.w : 2);
  const unsigned w2s = WS_W2 + ((2 * c.nb8 + (c.w & 1)) * KS_F + k0q + 14) * 1024;
  const unsigned w2o = WS_W2 + ((2 * c.nb8 + 1 - (c.w & 1)) * KS_F + k0q + 14) * 1024;
  static_for<0, 32>([&](auto kc) __attribute__((always_inline)) {
    constexpr int k = decltype(kc)::v;
    if constexpr (FIRST && (k & 7) == 6 && k < 24) asm volatile("s_barrier" ::: "memory");
    aA = MFMA(as_bf(fa[k % 3]), as_bf(xf[k % PD]), aA);
    pin16(aA); SBAR();
    if constexpr (k + 2 < 32) fa[(k + 2) % 3] = wfrag(wl, k + 2);
    if constexpr (k >= 1 && k - 1 + PD < 32) xf[(k - 1) % PD] = ld16(c, c.voff, xs + (k - 1 + PD) * 1024);
    if constexpr (k >= 33 - PD && k < 35 - PD) {
      sp[2 * (k + PD - 33)] = ld16(c, c.voff, WS_ST + inst * (64 * 4096) + tt * 4096 + 2 * (k + PD - 33) * 1024);
      sp[2 * (k + PD - 33) + 1] = ld16(c, c.voff, WS_ST + inst * (64 * 4096) + tt * 4096 + (2 * (k + PD - 33) + 1) * 1024);
    }
    SBAR();
    aB = MFMA(as_bf(fb[k % 3]), as_bf(xf[k % PD]), aB);
    pin16(aB); SBAR();
    if constexpr (k + 2 < 32) fb[(k + 2) % 3] = wfrag(wl + 32768, k + 2);
    if constexpr (MODE == 1 && k >= 23 && k < 31) {
      constexpr int i = k - 23;
      const int kk = (i == 7 && c.w >= 2) ? 6 : i;
      w2r[2 * i] = ld16(c, c.voff, w2s + kk * 1024); w2r[2 * i + 1] = ld16(c, c.voff, w2o + kk * 1024);
    }
    if constexpr (k == 28) {
      ((LAS f32x4*)sc)[c.lane] = (__builtin_bit_cast(f32x4, sp[0]) + __builtin_bit_cast(f32x4, sp[1])) +
                                 (__builtin_bit_cast(f32x4, sp[2]) + __builtin_bit_cast(f32x4, sp[3]));
#pragma unroll
      for (int i = 0; i < 4; ++i) sc4[i] = sc[i * 64 + c.lane];
    }
    if constexpr (k == 30) {
      const float s = (sc4[0] + sc4[1]) + (sc4[2] + sc4[3]);
      auto rr = __builtin_amdgcn_permlane32_swap(__float_as_uint(s), __float_as_uint(s), false, false);
      const float mu = __uint_as_float(rr[0]) * (1.f / D_);
      const float var = fmaxf(__uint_as_float(rr[1]) * (1.f / D_) - mu * mu, 0.f);
      mean = rbf(mu);
      rstd = __builtin_amdgcn_rcpf(rbf(__builtin_amdgcn_sqrtf(rbf(rbf(var) + 1e-5f))));
      ((LAS f32x2*)(c.lds + LD_ST))[c.w * 32 + c.r] = (f32x2){mean, rstd};
    }
    SBAR();
    if (has3) aC = MODE == 0 ? MFMA(as_bf(xf[k % PD]), as_bf(fc[k % 3]), aC) : MFMA(as_bf(fc[k % 3]), as_bf(xf[k % PD]), aC);
    pin16(aC); SBAR();
    if constexpr (k + 2 < 32) fc[(k + 2) % 3] = wfrag(wl2, k + 2);
    SBAR();
  });
  STAMP(PH + 2);
  if (MODE == 0) ln_tokstats<0>(c, ts);
  if (!FIRST) ln_params<MODE, 0>(c, par);
  SBAR();
  {
    LAS f32x4* ex = (LAS f32x4*)(c.lds + LD_EX) + c.w * 384 + c.lane;
#pragma unroll
    for (int g = 0; g < 4; ++g) ex[g * 64] = (f32x4){aB[4 * g], aB[4 * g + 1], aB[4 * g + 2], aB[4 * g + 3]};
    ex[256] = (f32x4){aC[8], aC[9], aC[10], aC[11]}; ex[320] = (f32x4){aC[12], aC[13], aC[14], aC[15]};
  }
  BARRIER_LDS();
  STAMP(PH + 4);
  if (FIRST) ln_params<MODE, 0>(c, par);
  ln_epilogue<MODE, 0>(c, l, aA, LO8(aC), mean, rstd, par, ts);
}
template <int MODE, bool FIRST>
DI void help_ln(Ctx& c, int l) {
  LnPar par; f32x4 ts[4];
  if (!FIRST) ln_params<MODE, 1>(c, par);
  BARRIER_LDS();
  if (FIRST) ln_params<MODE, 1>(c, par);
  const LAS f32x4* ex = (const LAS f32x4*)(c.lds + LD_EX) + (c.w & 3) * 384 + c.lane;
  const f32x2 ms = ((const LAS f32x2*)(c.lds + LD_ST))[(c.w & 3) * 32 + c.r];
  const f32x8 lo = cat8(ex[0], ex[64]), hi = cat8(ex[128], ex[192]), ch = cat8(ex[256], ex[320]);
  if (MODE == 0) ln_tokstats<1>(c, ts);
  SBAR();
  const f32x16 a = __builtin_shufflevector(lo, hi, 0, 1, 2, 3, 4, 5, 6, 7, 8, 9, 10, 11, 12, 13, 14, 15);
  ln_epilogue<MODE, 1>(c, l, a, ch, ms[0], ms[1], par, ts);
}
DI void help_colsum(const Ctx& c, int mode) {
  const int t = (c.w - 4) * 64 + c.lane;
  if (t < 96) {
    float v[32];
#pragma unroll
    for (int i = 0; i < 32; ++i) v[i] = ld4(c, t * 4, WS_CSP + ((mode * 16 + c.nb) * 32 + i) * 384);
    float s = 0.f;
#pragma unroll
    for (int i = 0; i < 32; i += 4) s += (v[i] + v[i + 1]) + (v[i + 2] + v[i + 3]);
    ((LAS float*)(c.lds + LD_PAR))[mode * 96 + t] = s;
  }
}

DI void reduce_res(Ctx& c, const f32x16 (&a)[4], u32x4 (&res)[2], int ctr, int PH) {
  LAS f32x4* red = (LAS f32x4*)(c.lds + LD_EX) + c.lane;
#pragma unroll
  for (int j = 1; j < 4; ++j) {
    LAS f32x4* p = red + ((c.w ^ j) * 3 + j - 1) * 256;
#pragma unroll
    for (int g = 0; g < 4; ++g) p[g * 64] = (f32x4){a[j][4 * g], a[j][4 * g + 1], a[j][4 * g + 2], a[j][4 * g + 3]};
  }
  STAMP(PH + 3);
  if (ctr >= 0) { arrive4(c, ctr); wait4(c, ctr); } else BARRIER_LDS();
  STAMP(PH + 4);
  const LAS f32x4* q = red + c.w * 768;
#pragma unroll
  for (int g = 0; g < 4; ++g) {
    const f32x4 s = ((f32x4){a[0][4 * g], a[0][4 * g + 1], a[0][4 * g + 2], a[0][4 * g + 3]} + q[g * 64]) + (q[256 + g * 64] + q[512 + g * 64]);
    const unsigned u0 = res[g >> 1][2 * (g & 1)], u1 = res[g >> 1][2 * (g & 1) + 1];
    res[g >> 1][2 * (g & 1)] = pk2(bf_lo(u0) + rbf(s[0]), bf_hi(u0) + rbf(s[1]));
    res[g >> 1][2 * (g & 1) + 1] = pk2(bf_lo(u1) + rbf(s[2]), bf_hi(u1) + rbf(s[3]));
  }
}
#define MM4(w0, w1, x0, x1) do { a[0] = MFMA(as_bf(w0), as_bf(x0), a[0]); a[1] = MFMA(as_bf(w1), as_bf(x0), a[1]); \
                                 a[2] = MFMA(as_bf(w0), as_bf(x1), a[2]); a[3] = MFMA(as_bf(w1), as_bf(x1), a[3]); } while (0)

DI void phase_wo(Ctx& c, int l, const u32x4 (&wo)[16], u32x4 (&res)[2]) {
  const unsigned xs = WS_L + l * L_SZ + L_O + ((2 * c.tb64 + (c.w >> 1)) * 32 + 8 * c.w) * 1024;
  const unsigned xo = WS_L + l * L_SZ + L_O + ((2 * c.tb64 + 1 - (c.w >> 1)) * 32 + 8 * c.w) * 1024;
  u32x4 xf[16];
#pragma unroll
  for (int k = 0; k < 8; ++k) { xf[2 * k] = ld16(c, c.voff, xs + k * 1024); xf[2 * k + 1] = ld16(c, c.voff, xo + k * 1024); }
  asm volatile("s_barrier" ::: "memory");
  SBAR();
  f32x16 a[4];
#pragma unroll
  for (int i = 0; i < 64; ++i) a[i >> 4][i & 15] = 0.f;
#pragma unroll
  for (int k = 0; k < 8; ++k) { MM4(wo[2 * k], wo[2 * k + 1], xf[2 * k], xf[2 * k + 1]); PIN(a[0]); PIN(a[1]); PIN(a[2]); PIN(a[3]); SBAR(); }
  STAMP((3 + 5 * l) * 10 + 2);
  reduce_res(c, a, res, -1, (3 + 5 * l) * 10);
}

DI void phase_w2(Ctx& c, int l, const u32x4 (&w2r)[16], u32x4 (&res)[2]) {
  const int k0 = c.w * 21 + (c.w < 2 ? c.w : 2);
  const unsigned xs = WS_L + l * L_SZ + L_H + ((2 * c.tb64 + (c.w >> 1)) * KS_F + k0) * 1024;
  const unsigned xo = WS_L + l * L_SZ + L_H + ((2 * c.tb64 + 1 - (c.w >> 1)) * KS_F + k0) * 1024;
  constexpr int WIN = 10;
  u32x4 xf[2 * WIN];
#pragma unroll
  for (int k = 0; k < WIN; ++k) { xf[2 * k] = ld16(c, c.voff, xs + k * 1024); xf[2 * k + 1] = ld16(c, c.voff, xo + k * 1024); }
  SBAR();
  const LAS u32x4* w0 = (const LAS u32x4*)(c.lds + w2_run(2 * c.w + (c.w & 1))) + c.lane;
  const LAS u32x4* w1 = (const LAS u32x4*)(c.lds + w2_run(2 * c.w + 1 - (c.w & 1))) + c.lane;
  f32x16 a[4];
#pragma unroll
  for (int i = 0; i < 64; ++i) a[i >> 4][i & 15] = 0.f;
  u32x4 f0[2], f1[2];
  f0[0] = w0[0]; f1[0] = w1[0];
  static_for<0, 22>([&](auto kc) __attribute__((always_inline)) {
    constexpr int k = decltype(kc)::v;
    const u32x4 g0 = k < 14 ? f0[k & 1] : w2r[k < 14 ? 0 : 2 * (k - 14)], g1 = k < 14 ? f1[k & 1] : w2r[k < 14 ? 0 : 2 * (k - 14) + 1];
    const u32x4 x0 = xf[2 * (k % WIN)], x1 = xf[2 * (k % WIN) + 1];
    constexpr int kn = k - 1 + WIN;
    const int kk = (kn == 21 && c.w >= 2) ? 20 : kn;
    if constexpr (kn == 22) asm volatile("s_barrier" ::: "memory");
    if (k < 21 || c.w < 2) {
      a[0] = MFMA(as_bf(g0), as_bf(x0), a[0]);
      pin16(a[0]); SBAR();
      if constexpr (k >= 1 && kn < 22) xf[2 * ((k - 1) % WIN)] = ld16(c, c.voff, xs + kk * 1024);
      SBAR();
      a[1] = MFMA(as_bf(g1), as_bf(x0), a[1]);
      pin16(a[1]); SBAR();
      if constexpr (k + 1 < 14) f0[(k + 1) & 1] = w0[(k + 1) * 64];
      SBAR();
      a[2] = MFMA(as_bf(g0), as_bf(x1), a[2]);
      pin16(a[2]); SBAR();
      if constexpr (k >= 1 && kn < 22) xf[2 * ((k - 1) % WIN) + 1] = ld16(c, c.voff, xo + kk * 1024);
      SBAR();
      a[3] = MFMA(as_bf(g1), as_bf(x1), a[3]);
      pin16(a[3]); SBAR();
      if constexpr (k + 1 < 14) f1[(k + 1) & 1] = w1[(k + 1) * 64];
      SBAR();
    }
  });
  STAMP((5 + 5 * l) * 10 + 2);
  if (l == 0) { arrive4(c, 16); wait4(c, 16); } else BARRIER_LDS();
  reduce_res(c, a, res, l == 0 ? 17 : -1, (5 + 5 * l) * 10);
}

#define SMF(a, b8, c) __builtin_amdgcn_smfmac_f32_32x32x32_bf16((a), __builtin_bit_cast(bf16x16, b8), (c), sidx, 0, 0)
#define LDF(p) (*(const LAS bf16x8*)(p))
DI float max16(const f32x16& s) {
  float a = fmaxf(fmaxf(s[0], s[1]), s[2]), b = fmaxf(fmaxf(s[3], s[4]), s[5]);
  a = fmaxf(fmaxf(a, s[6]), s[7]); b = fmaxf(fmaxf(b, s[8]), s[9]);
  a = fmaxf(fmaxf(a, s[10]), s[11]); b = fmaxf(fmaxf(b, s[12]), s[13]);
  a = fmaxf(fmaxf(a, s[14]), s[15]);
  return swap_half_max(fmaxf(a, b));
}
DI void attn_slow(const LAS char* kp, const LAS char* vp, bool isv, bf16x8 q, f32x16& o, float& m) {
  const int sidx = 0xDDDDDDDD;
  f32x16 zero;
#pragma unroll
  for (int i = 0; i < 16; ++i) zero[i] = 0.f;
  o = zero; m = -3.0e38f;
  for (int t = 0; t < 32; ++t) {
    f32x16 s = MFMA(LDF(kp + t * 1024), q, zero);
    const float mn = fmaxf(m, max16(s));
    const float al = __builtin_amdgcn_exp2f(m - mn);
    m = mn;
#pragma unroll
    for (int i = 0; i < 16; ++i) { o[i] *= al; s[i] = __builtin_amdgcn_exp2f(s[i] - mn); }
    const int vo = isv ? t * 1024 : 0;
    o = SMF(LDF(vp + vo), LO8(s), o);
    o = SMF(LDF(vp + vo + 512), HI8(s), o);
  }
}

struct In { const unsigned short *x, *wqkv, *wo, *w1, *b1, *w2; unsigned short* out; };
struct Jobs { unsigned e[4][8], dst[4]; bool ok[4]; };
DI void jobs_load(const Ctx& c, const In& in, Jobs& jb) {
  const int t0 = c.nb < 11 ? 3 * c.nb : 33 + 2 * (c.nb - 11), nt1 = c.nb < 11 ? 3 : 2;
  const unsigned short* W[4]; int N[4];
#pragma unroll
  for (int u = 0; u < 4; ++u) {
    const int j = c.w + 4 * u;
    int nt, ks;
    if (j < 6) { W[u] = in.w1; N[u] = F_; jb.ok[u] = (j >> 1) < nt1; nt = t0 + (jb.ok[u] ? j >> 1 : 0); ks = 2 * c.tb + (j & 1); jb.dst[u] = WS_W1 + (nt * 32 + ks) * 1024; }
    else if (j < 8) { W[u] = in.wo; N[u] = D_; jb.ok[u] = true; nt = 2 * c.nb8 + (j & 1); ks = c.tb64; jb.dst[u] = WS_WO + (nt * 32 + ks) * 1024; }
    else { W[u] = in.w2; N[u] = D_; ks = c.tb64 + 32 * ((j - 8) >> 1); jb.ok[u] = ks < KS_F; if (!jb.ok[u]) ks = 0; nt = 2 * c.nb8 + (j & 1); jb.dst[u] = WS_W2 + (nt * KS_F + ks) * 1024; }
    W[u] += 16 * ks * N[u] + 32 * nt;
    asm volatile("" : "+s"(W[u]));
    asm volatile("" : "+s"(N[u]));
  }
#pragma unroll
  for (int u = 0; u < 4; ++u) {
    const __amdgpu_buffer_rsrc_t rw = __builtin_amdgcn_make_buffer_rsrc((void*)W[u], 0, 32 * N[u], 0x00020000);
    const unsigned o = (4 * c.h * N[u] + c.r) * 2;
#pragma unroll
    for (int j = 0; j < 8; ++j) jb.e[u][j] = __builtin_amdgcn_raw_buffer_load_b16(rw, o, (8 * (j >> 2) + (j & 3)) * 2 * N[u], 0);
  }
}
DI void jobs_store(const Ctx& c, Jobs& jb) {
#pragma unroll
  for (int i = 0; i < 32; ++i) PIN(jb.e[i >> 3][i & 7]);
#pragma unroll
  for (int u = 0; u < 4; ++u) {
    const int j = c.w + 4 * u;
    if (jb.ok[u]) wfrag_store(c, jb.e[u], jb.dst[u], j < 6, WS_CSP + ((16 + c.nb) * 32 + 2 * c.tb + (j & 1)) * 384 + (j >> 1) * 128);
  }
}
DI void jobs_publish(const Ctx& c) {
  asm volatile("s_waitcnt vmcnt(0)" ::: "memory");
  unsigned old = 0;
  if (c.lane == 0) old = __hip_atomic_fetch_add((LAS unsigned*)(c.lds + LD_CTR) + 2, 1u, __ATOMIC_RELAXED, __HIP_MEMORY_SCOPE_WORKGROUP);
  if (__builtin_amdgcn_readfirstlane(old) == 3 && c.lane < 2)
    __hip_atomic_store(c.lane ? flag_ptr(c, FL_W1 + c.nb, c.tb) : flag_ptr(c, FL_W2 + c.nb8, c.tb64), c.token, RLX_AGENT);
}

#define EX1(r) "v_exp_f32 v" #r ", v" #r "\n\t"
#define ASTEP(KOFF, VOFF, kc_, kn_, v0_, v1_, nv0_, pv1_)                                                                  \
  asm volatile("s_waitcnt lgkmcnt(0)\n\t" EX1(64) EX1(65)                                                                  \
               "v_smfmac_f32_32x32x32_bf16 %[o1], %[pv1], v[88:95], %[idx]\n\t"                                            \
               "ds_read_b128 %[nv0], %[vp] offset:%[vo]\n\tds_read_b128 %[pv1], %[vp] offset:%[vo1]\n\t"                   \
               EX1(66) EX1(67) EX1(68)                                                                                     \
               "v_mfma_f32_32x32x16_bf16 v[80:95], %[kc], %[q1], %[c1]\n\t"                                                \
               "ds_read_b128 %[kc], %[kp] offset:%[ko]\n\t"                                                                \
               EX1(69) EX1(70) EX1(71) EX1(72) EX1(73)                                                                     \
               "v_smfmac_f32_32x32x32_bf16 %[o0], %[v0], v[64:71], %[idx]\n\t"                                             \
               EX1(74) EX1(75) EX1(76) EX1(77) EX1(78) EX1(79) EX1(80) EX1(81)                                             \
               "v_smfmac_f32_32x32x32_bf16 %[o0], %[v1], v[72:79], %[idx]\n\t"                                             \
               EX1(82) EX1(83) EX1(84)                                                                                     \
               "v_mfma_f32_32x32x16_bf16 v[64:79], %[kn], %[q0], %[c0]\n\t"                                                \
               EX1(85) EX1(86) EX1(87) EX1(88) EX1(89)                                                                     \
               "v_smfmac_f32_32x32x32_bf16 %[o1], %[v0], v[80:87], %[idx]\n\t"                                             \
               EX1(90) EX1(91) EX1(92) EX1(93) EX1(94) "v_exp_f32 v95, v95"                                                \
               : "+{v[64:79]}"(sA), "+{v[80:95]}"(sB), [o0] "+v"(o0), [o1] "+v"(o1), [kc] "+v"(kc_), [nv0] "=&v"(nv0_),    \
                 [pv1] "+v"(pv1_)                                                                                          \
               : [kn] "v"(kn_), [v0] "v"(v0_), [v1] "v"(v1_), [q0] "v"(q0), [q1] "v"(q1), [c0] "v"(cin0), [c1] "v"(cin1),  \
                 [idx] "v"(sidx), [kp] "v"(kpa), [vp] "v"(vpa), [ko] "n"(KOFF), [vo] "n"(VOFF), [vo1] "n"((VOFF) + 512)     \
               : "memory")

template <int half, int l>
DI void phase_attn(Ctx& c, const In& in, u32x4 (&wo)[16], u32x4 (&hw)[28]) {
  const int qt = c.qb * 8 + (c.w & 3) * 2;
  const unsigned lay = WS_L + l * L_SZ;
  const char* kvg = c.ws + lay + ((c.w & 1) ? L_V : L_K) + c.head * 65536 + ((c.w >> 1) & 1) * 32768 + half * 4096;
  const unsigned kvd = c.lbase + ((c.w & 1) ? LD_V : LD_W) + ((c.w >> 1) & 1) * 32768 + half * 4096;
  u32x4 qv0, qv1;
  asm volatile("s_nop 4\n\tglobal_load_dwordx4 %0, %2, %3 sc1\n\tglobal_load_dwordx4 %1, %2, %3 offset:1024 sc1"
               : "=&v"(qv0), "=&v"(qv1) : "v"(c.voff), "s"(c.ws + lay + L_Q + (c.head * 64 + qt) * 1024) : "memory");
#define KV4(u) glds16x4(kvg + (u) * 8192, c.voff, kvd + (u) * 8192)
  glds16x4(kvg, c.voff, kvd);
  if (half == 0) {
    const unsigned o1 = (c.lane & 11) == 0 ? 0x3f803f80u : 0u;
    ((LAS u32x4*)(c.lds + LD_ONE))[c.w * 64 + c.lane] = (u32x4){o1, o1, o1, o1};
  }
  int sidx = 0xDDDDDDDD;
  const bool isv = c.r < 16;
  const LAS char* kp0 = c.lds + LD_W + half * 32768 + c.lane * 16;
  const LAS char* vp0 = c.lds + (isv ? LD_V + half * 32768 : LD_ONE) + c.h * 256 + (c.r & 15) * 16;
  const int vstep = isv ? 4096 : 0;
  if (half == 0) asm volatile("s_waitcnt vmcnt(0) lgkmcnt(0)\n\ts_barrier" : "+v"(qv0), "+v"(qv1)::"memory");
  else asm volatile("s_waitcnt vmcnt(4)\n\ts_barrier" : "+v"(qv0), "+v"(qv1)::"memory");
  STAMP((2 + 5 * l) * 10 + 1);
  if (half == 0) __builtin_amdgcn_s_setprio(1);
  const bf16x8 q0 = as_bf(qv0), q1 = as_bf(qv1);
  f32x16 zero;
#pragma unroll
  for (int i = 0; i < 16; ++i) zero[i] = 0.f;
  f32x16 cin0, cin1, o0 = zero, o1 = zero, sA, sB = zero;
  float m0, m1;
  bf16x8 ka = LDF(kp0), kb = LDF(kp0 + 1024), va0 = LDF(vp0), va1 = LDF(vp0 + 512), vb0, vb1 = as_bf((u32x4){0u, 0u, 0u, 0u});
  {
    const f32x16 s = MFMA(ka, q0, zero), s1 = MFMA(ka, q1, zero);
    m0 = max16(s); m1 = max16(s1);
#pragma unroll
    for (int i = 0; i < 16; ++i) { cin0[i] = -m0; cin1[i] = -m1; sA[i] = s[i] - m0; }
  }
  unsigned kpa = (unsigned)(size_t)kp0, vpa = (unsigned)(size_t)vp0;
  asm volatile("s_nop 1" : "+v"(cin0), "+v"(cin1), "+v"(sA), "+v"(sB), "+v"(o0), "+v"(o1), "+v"(sidx), "+v"(vb1));
#define QUAD(A, B, MID, C, D)                                                        \
  do {                                                                               \
    ASTEP(2048, 1024, ka, kb, va0, va1, vb0, vb1); A;                                \
    ASTEP(3072, 2048, kb, ka, vb0, vb1, va0, va1); B; MID;                           \
    ASTEP(4096, 3072, ka, kb, va0, va1, vb0, vb1); C;                                \
    vpa += vstep;                                                                    \
    ASTEP(5120, 0, kb, ka, vb0, vb1, va0, va1); D;                                   \
    kpa += 4096; QSTAMP();                                                           \
  } while (0)
#define NOP (void)0
#define QSTAMP()
#define WBAR(n) asm volatile("s_waitcnt vmcnt(" #n ")\n\ts_barrier" ::: "memory")
  constexpr bool JOBS = half == 0 && l == 0;
  Jobs jb;
  KV4(1);
  QUAD(KV4(2), NOP, WBAR(8), KV4(3), if (JOBS) { jobs_load(c, in, jb); SBAR(); });
  QUAD(NOP, NOP, if (JOBS) WBAR(32); else WBAR(0); if (half == 1 && l == 1) (w2_fetch<0, 21>(c, hw)), NOP,
       if (JOBS) { jobs_store(c, jb); SBAR(); });
  QUAD(if (JOBS) jobs_publish(c), NOP, NOP,
       if (half == 1 && l == 0) { poll_any(c, FL_W2 + c.nb8, c.lane & 31); (w2_fetch<0, 21>(c, hw)); }, NOP);
  STAMP((2 + 5 * l) * 10 + 5);
  for (int it = 3; it < 8; ++it) QUAD(NOP, NOP, NOP, NOP, NOP);
#undef WBAR
  asm volatile("s_nop 1\n\tv_smfmac_f32_32x32x32_bf16 %[o1], %[pv1], v[88:95], %[idx]\n\ts_waitcnt lgkmcnt(0)\n\ts_nop 7\n\ts_nop 7"
               : [o1] "+v"(o1), "+v"(o0), "+v"(ka), "+v"(kb), "+v"(va0), "+v"(va1) : [pv1] "v"(vb1), "{v[80:95]}"(sB), [idx] "v"(sidx));
#undef QUAD
#undef KV4
  STAMP((2 + 5 * l) * 10 + 2);
  if (__any(!(o0[8] < 1.0995116e12f))) attn_slow(kp0, vp0, isv, q0, o0, m0);
  if (__any(!(o1[8] < 1.0995116e12f))) attn_slow(kp0, vp0, isv, q1, o1, m1);
  LAS f32x4* cmb = (LAS f32x4*)(c.lds + LD_CMB) + (c.w & 3) * 320 + c.lane;
  const unsigned w1src = WS_W1 + (c.nb < 11 ? 3 * c.nb : 33 + 2 * (c.nb - 11)) * 32768;
  if (half == 0) {
    __builtin_amdgcn_s_setprio(0);
    cmb[0] = (f32x4){o0[0], o0[1], o0[2], o0[3]}; cmb[64] = (f32x4){o0[4], o0[5], o0[6], o0[7]};
    cmb[128] = (f32x4){o1[0], o1[1], o1[2], o1[3]}; cmb[192] = (f32x4){o1[4], o1[5], o1[6], o1[7]};
    cmb[256] = (f32x4){o0[8], m0, o1[8], m1};
    arrive4(c, 12 + l);
    if (l == 0) poll_any(c, c.lane < 32 ? FL_W1 + c.nb : FL_W2 + c.nb8, c.lane < 32 ? (c.lane & 15) : (c.lane & 31));
    const unsigned wos = WS_WO + ((2 * c.nb8 + (c.w & 1)) * 32 + 8 * c.w) * 1024;
    const unsigned woo = WS_WO + ((2 * c.nb8 + 1 - (c.w & 1)) * 32 + 8 * c.w) * 1024;
#pragma unroll
    for (int k = 0; k < 8; ++k) { wo[2 * k] = ld16(c, c.voff, wos + k * 1024); wo[2 * k + 1] = ld16(c, c.voff, woo + k * 1024); }
    SBAR();
    wait4<true>(c, 12 + l);
    help_dma<8>(c, w1src, LD_W);
    if (c.nb < 11) help_dma<8>(c, w1src + 65536, LD_W + 65536);
  }
  STAMP((2 + 5 * l) * 10 + 3);
  BARRIER_LDS();
  STAMP((2 + 5 * l) * 10 + 4);
  if (half == 1) {
    const f32x4 lm = cmb[256];
#pragma unroll
    for (int u = 0; u < 2; ++u) {
      const f32x16& o = u ? o1 : o0; const float m = u ? m1 : m0;
      const f32x8 p2 = cat8(cmb[128 * u], cmb[128 * u + 64]);
      const float m2 = lm[2 * u + 1];
      const float mm = fmaxf(m, m2);
      const float f1 = __builtin_amdgcn_exp2f(m - mm), f2 = __builtin_amdgcn_exp2f(m2 - mm);
      const float inv = __builtin_amdgcn_rcpf(o[8] * f1 + lm[2 * u] * f2);
      const float g1 = f1 * inv, g2 = f2 * inv;
      unsigned p[4];
#pragma unroll
      for (int i = 0; i < 4; ++i) p[i] = pk2(o[2 * i] * g1 + p2[2 * i] * g2, o[2 * i + 1] * g1 + p2[2 * i + 1] * g2);
      st16(c, (u32x4){p[0], p[1], p[2], p[3]}, c.voff, lay + L_O + ((qt + u) * 32 + c.head) * 1024);
    }
    STAMP((2 + 5 * l) * 10 + 6);
    asm volatile("s_waitcnt vmcnt(0)" ::: "memory");
    if (c.lane < 8) __hip_atomic_store(flag_ptr(c, FL_O + l * 256 + (c.qb * 4 + (c.w & 3)) * 8 + c.lane, c.head), c.token, RLX_AGENT);
  }
}

DI u64 uniform64(u64 v) {
  return ((u64)__builtin_amdgcn_readfirstlane((unsigned)(v >> 32)) << 32) | (unsigned)__builtin_amdgcn_readfirstlane((unsigned)v);
}

DI void run_main(Ctx& c, const In& in) {
  const u64 epoch = __hip_atomic_load((gu64*)c.ws, RLX_AGENT);
  u32x4 res[2];
  {
    if (c.w == 0 && c.lane < 32) ((LAS unsigned*)(c.lds + LD_CTR))[c.lane] = 0u;
    const unsigned short* src = in.x + (size_t)((2 * c.tb64 + (c.w >> 1)) * 32 + c.r) * D_ + 32 * (2 * c.nb8 + (c.w & 1)) + 4 * c.h;
    u32x2 xr[4];
#pragma unroll
    for (int i = 0; i < 4; ++i) xr[i] = *(const u32x2*)(src + 8 * i);
    BARRIER_LDS();
    c.token = uniform64(epoch) + 1;
    res[0] = (u32x4){xr[0][0], xr[0][1], xr[1][0], xr[1][1]}; res[1] = (u32x4){xr[2][0], xr[2][1], xr[3][0], xr[3][1]};
    store_x(c, 0, res);
    publish(c, 0, 4, FL_X + c.tb * 16, c.nb);
    STAMP(1);
    poll(c, (c.lane & 16) ? FL_WQ + c.nb * 16 + c.tb : FL_X + c.tb * 16 + c.nb, c.lane & 15);
    STAMP(2);
    BARRIER_LDS();
    STAMP(11);
    u32x4 dummy[16];
    fresh(c);
    phase_ln<0, true>(c, 0, 0, dummy);
  }
  for (int l = 0; l < 2; ++l) {
    u32x4 wo[16], w2r[16];
    if (l == 1) {
      STAMP((1 + 5 * l) * 10 + 1);
      fresh(c);
      u32x4 dummy[16];
      phase_ln<0, false>(c, l, 2 * l, dummy);
    }
    fresh(c);
    STAMP((1 + 5 * l) * 10 + 9);
    poll(c, FL_Q + l * 256 + c.head * 8 + c.qb, c.lane & 15);
    BARRIER_LDS();
    STAMP((2 + 5 * l) * 10);
    { u32x4 unused[28]; if (l == 0) phase_attn<0, 0>(c, in, wo, unused); else phase_attn<0, 1>(c, in, wo, unused); }
    fresh(c);
    STAMP((2 + 5 * l) * 10 + 9);
    poll(c, FL_O + l * 256 + c.tb64 * 8 + c.nb8, c.lane & 31);
    BARRIER_LDS();
    STAMP((3 + 5 * l) * 10 + 1);
    phase_wo(c, l, wo, res);
    store_x(c, 2 * l + 1, res);
    STAMP((3 + 5 * l) * 10 + 6);
    publish(c, 8 + l, 4, FL_X + (2 * l + 1) * 256 + c.tb * 16, c.nb);
    fresh(c);
    STAMP((3 + 5 * l) * 10 + 9);
    poll(c, FL_X + (2 * l + 1) * 256 + c.tb * 16 + c.nb, c.lane & 15);
    BARRIER_ALL();
    STAMP((4 + 5 * l) * 10 + 1);
    phase_ln<1, false>(c, l, 2 * l + 1, w2r);
    fresh(c);
    STAMP((4 + 5 * l) * 10 + 9);
    poll(c, FL_H + l * 256 + c.tb * 16 + c.nb, c.lane & 15);
    STAMP((4 + 5 * l) * 10 + 8);
    BARRIER_LDS();
    STAMP((5 + 5 * l) * 10 + 1);
    phase_w2(c, l, w2r, res);
    fresh(c);
    if (l == 0) {
      store_x(c, 2, res);
      STAMP((5 + 5 * l) * 10 + 6);
      publish(c, 10, 4, FL_X + 2 * 256 + c.tb * 16, c.nb);
      STAMP((5 + 5 * l) * 10 + 9);
      poll(c, FL_X + 2 * 256 + c.tb * 16 + c.nb, c.lane & 15);
      STAMP((5 + 5 * l) * 10 + 8);
      BARRIER_LDS();
    }
  }
  {
    auto s0 = __builtin_amdgcn_permlane32_swap(res[0][0], res[0][2], false, false), s1 = __builtin_amdgcn_permlane32_swap(res[0][1], res[0][3], false, false);
    auto s2 = __builtin_amdgcn_permlane32_swap(res[1][0], res[1][2], false, false), s3 = __builtin_amdgcn_permlane32_swap(res[1][1], res[1][3], false, false);
    unsigned short* op = in.out + (size_t)((2 * c.tb64 + (c.w >> 1)) * 32 + c.r) * D_ + 32 * (2 * c.nb8 + (c.w & 1)) + 8 * c.h;
    __builtin_nontemporal_store((u32x4){s0[0], s1[0], s0[1], s1[1]}, (u32x4*)op);
    __builtin_nontemporal_store((u32x4){s2[0], s3[0], s2[1], s3[1]}, (u32x4*)(op + 16));
  }
  STAMP(109);
  if (c.bid == 0 && c.w == 0 && c.lane == 0) __hip_atomic_store((gu64*)c.ws, c.token, RLX_AGENT);
}

DI void run_help(Ctx& c, const In& in) {
  const u64 epoch = __hip_atomic_load((gu64*)c.ws, RLX_AGENT);
  {
    unsigned e0[8], e1[8];
    const int qk = 2 * c.tb + (c.w & 1), qa = (c.w >> 1) & 1;
    wfrag_load(in.wqkv, 3 * D_, qa * 16 + c.nb, qk, c.r, c.h, e0);
    wfrag_load(in.wqkv, 3 * D_, 32 + c.nb, qk, c.r, c.h, e1);
    const int t = (c.w & 3) * 64 + c.lane;
    const int bcol = 32 * (c.nb < 11 ? 3 * c.nb : 33 + 2 * (c.nb - 11)) + t;
    const unsigned bv = in.b1[bcol < F_ ? bcol : 0];
    BARRIER_LDS();
    c.token = uniform64(epoch) + 1;
    wfrag_store(c, e0, WS_WQKV + ((qa * 16 + c.nb) * 32 + qk) * 1024, true, WS_CSP + (c.nb * 32 + qk) * 384 + qa * 128);
    if (qa == 0) wfrag_store(c, e1, WS_WQKV + ((32 + c.nb) * 32 + qk) * 1024, true, WS_CSP + (c.nb * 32 + qk) * 384 + 256);
    publish(c, 1, 4, FL_WQ + c.nb * 16, c.tb);
    STAMP(1);
    if (t < 96) ((LAS float*)(c.lds + LD_PAR))[192 + t] = __uint_as_float(bv << 16);
    BARRIER_LDS();
    STAMP(11);
    fresh(c);
#define WQC(q) _Pragma("unroll") for (int t_ = 0; t_ < 3; ++t_) \
    glds16x2(c.ws + WS_WQKV + ((16 * t_ + c.nb) * 32 + 8 * (q) + 2 * (c.w & 3)) * 1024, c.voff, c.lbase + LD_W + t_ * 32768 + (8 * (q) + 2 * (c.w & 3)) * 1024)
    WQC(0); WQC(1);
    asm volatile("s_waitcnt vmcnt(6)\n\ts_barrier" ::: "memory");
    WQC(2);
    asm volatile("s_waitcnt vmcnt(6)\n\ts_barrier" ::: "memory");
    WQC(3);
    asm volatile("s_waitcnt vmcnt(6)\n\ts_barrier" ::: "memory");
    asm volatile("s_waitcnt vmcnt(0)\n\ts_barrier" ::: "memory");
#undef WQC
    help_colsum(c, 0);
    STAMP(12);
    help_ln<0, true>(c, 0);
  }
  for (int l = 0; l < 2; ++l) {
    u32x4 wo[16];
    fresh(c);
    if (l == 1) help_ln<0, false>(c, l);
    STAMP((1 + 5 * l) * 10 + 9);
    BARRIER_LDS();
    STAMP((2 + 5 * l) * 10);
    fresh(c);
    u32x4 hw[28];
    if (l == 0) phase_attn<1, 0>(c, in, wo, hw); else phase_attn<1, 1>(c, in, wo, hw);
    fresh(c);
    STAMP((2 + 5 * l) * 10 + 9);
    BARRIER_LDS();
    asm volatile("s_barrier" ::: "memory");
    help_dma<8>(c, WS_W1 + (c.nb < 11 ? 3 * c.nb + 1 : 34 + 2 * (c.nb - 11)) * 32768, LD_W + 32768);
    w2_fetch<21, 28>(c, hw);
    asm volatile("s_barrier" ::: "memory");
    if (l == 0) help_colsum(c, 1);
    BARRIER_ALL();
    STAMP((4 + 5 * l) * 10 + 1);
    w2_put<21, 28>(c, hw);
    STAMP((4 + 5 * l) * 10 + 2);
    help_ln<1, false>(c, l);
    STAMP((4 + 5 * l) * 10 + 6);
    w2_put<0, 21>(c, hw);
    STAMP((4 + 5 * l) * 10 + 9);
    BARRIER_LDS();
    STAMP((5 + 5 * l) * 10 + 1);
    asm volatile("s_barrier" ::: "memory");
    if (l == 0) {
      fresh(c);
#pragma unroll
      for (int u = 0; u < 24; ++u) hw[u] = ld16(c, c.voff, WS_WQKV + (((u >> 3) * 16 + c.nb) * 32 + (u & 7) * 4 + (c.w & 3)) * 1024);
      SBAR();
      STAMP((5 + 5 * l) * 10 + 2);
    }
    if (l == 0) {
      wait4<true>(c, 16);
#pragma unroll
      for (int u = 0; u < 24; ++u) *(LAS u32x4*)(c.lds + LD_W + (u * 4 + (c.w & 3)) * 1024 + c.voff) = hw[u];
      STAMP((5 + 5 * l) * 10 + 9);
      BARRIER_LDS();
    } else {
      asm volatile("s_barrier\n\ts_barrier" ::: "memory");
    }
  }
}

__global__ __launch_bounds__(512, 2) void mega(const unsigned short* __restrict__ x, const unsigned short* __restrict__ wqkv,
                                               const unsigned short* __restrict__ wo_g, const unsigned short* __restrict__ w1,
                                               const unsigned short* __restrict__ b1, const unsigned short* __restrict__ w2,
                                               char* __restrict__ ws, unsigned short* __restrict__ out) {
  extern __shared__ __attribute__((aligned(16))) char lds_[];
  Ctx c;
  c.ws = ws; c.lds = (LAS char*)lds_; c.rs = __builtin_amdgcn_make_buffer_rsrc(ws, 0, WS_END, 0x00020000);
  c.lbase = (unsigned)(size_t)c.lds;
  c.bid = blockIdx.x; c.lane = threadIdx.x & 63; c.w = __builtin_amdgcn_readfirstlane(threadIdx.x >> 6); c.dead = false;
  fresh(c);
  STAMP(0);
  const In in = {x, wqkv, wo_g, w1, b1, w2, out};
  if (c.w < 4) run_main(c, in); else run_help(c, in);
}

extern "C" void kernel_launch(void* const* d_in, const int* in_sizes, int n_in, void* d_out, int out_size, void* d_ws,
                              size_t ws_size, hipStream_t stream) {
  (void)hipFuncSetAttribute((const void*)mega, hipFuncAttributeMaxDynamicSharedMemorySize, LDS_BYTES);
  mega<<<256, 512, LDS_BYTES, stream>>>((const unsigned short*)d_in[0], (const unsigned short*)d_in[1], (const unsigned short*)d_in[2],
                                        (const unsigned short*)d_in[4], (const unsigned short*)d_in[5], (const unsigned short*)d_in[6],
                                        (char*)d_ws, (unsigned short*)d_out);
}
```

```cpp
#include <hip/hip_runtime.h>
#include <hip/hip_bf16.h>

typedef __attribute__((ext_vector_type(2))) __bf16 bf16x2;
typedef __attribute__((ext_vector_type(8))) __bf16 bf16x8;
typedef __attribute__((ext_vector_type(16))) __bf16 bf16x16;
typedef __attribute__((ext_vector_type(16))) float f32x16;
typedef __attribute__((ext_vector_type(8))) float f32x8;
typedef __attribute__((ext_vector_type(4))) float f32x4;
typedef __attribute__((ext_vector_type(2))) float f32x2;
typedef __attribute__((ext_vector_type(4))) unsigned u32x4;
typedef __attribute__((ext_vector_type(2))) unsigned u32x2;
typedef unsigned long long u64;
#define LAS __attribute__((address_space(3)))
#define GAS __attribute__((address_space(1)))
typedef GAS u64 gu64;
#define DI __device__ __forceinline__
#define RLX_AGENT __ATOMIC_RELAXED, __HIP_MEMORY_SCOPE_AGENT

constexpr int D_ = 512, F_ = 1376;
constexpr int KS_F = F_ / 16;

constexpr unsigned MB = 1u << 20;
constexpr unsigned WS_FLAG = 0;
constexpr unsigned WS_WQKV = 1024 * 1024;
constexpr unsigned WS_WO = WS_WQKV + 48 * 32 * 1024;
constexpr unsigned WS_W1 = WS_WO + 16 * 32 * 1024;
constexpr unsigned WS_W2 = WS_W1 + 43 * 32 * 1024;
constexpr unsigned WS_CSP = WS_W2 + 16 * 86 * 1024;
constexpr unsigned WS_ST = WS_CSP + 2 * 16 * 32 * 384;
constexpr unsigned WS_X = WS_ST + 4 * 64 * 4096;
constexpr unsigned WS_L = WS_X + 8 * MB;
constexpr unsigned L_Q = 0, L_K = 2 * MB, L_V = 4 * MB, L_O = 6 * MB, L_H = 8 * MB, L_SZ = 14 * MB;
constexpr unsigned WS_END = WS_L + 2 * L_SZ;
constexpr int FL_X = 1;
constexpr int FL_H = FL_X + 1024;
constexpr int FL_Q = FL_H + 512;
constexpr int FL_O = FL_Q + 512;
constexpr int FL_WQ = FL_O + 512;
constexpr int FL_W1 = FL_WQ + 256;
constexpr int FL_W2 = FL_W1 + 16;
static_assert((FL_W2 + 8) * 256 <= WS_WQKV, "flag region");

constexpr int LD_W = 0;
constexpr int LD_V = 65536;
constexpr int LD_EX = 98304;
constexpr int LD_ONE = 131072;
constexpr int LD_CMB = 135168;
constexpr int LD_W2E = 122880;
constexpr int LD_SC = 151552;
constexpr int LD_ST = 155648;
constexpr int LD_PAR = 157696;
constexpr int LD_CTR = LD_PAR + 1280;
constexpr int LDS_BYTES = 163840;

#define MFMA(a, b, c) __builtin_amdgcn_mfma_f32_32x32x16_bf16((a), (b), (c), 0, 0, 0)
#define SBAR() __builtin_amdgcn_sched_barrier(0)
#define PIN(x) asm volatile("" : "+v"(x))
#define BARRIER_LDS() asm volatile("s_waitcnt lgkmcnt(0)\n\ts_barrier" ::: "memory")
#define BARRIER_ALL() asm volatile("s_waitcnt vmcnt(0) lgkmcnt(0)\n\ts_barrier" ::: "memory")

DI unsigned pk2(float lo, float hi) {
  bf16x2 v; v[0] = (__bf16)lo; v[1] = (__bf16)hi;
  return __builtin_bit_cast(unsigned, v);
}
DI float bf_lo(unsigned u) { return __uint_as_float(u << 16); }
DI float bf_hi(unsigned u) { return __uint_as_float(u & 0xffff0000u); }
DI float rbf(float x) { return (float)(__bf16)x; }
DI bf16x8 as_bf(u32x4 v) { return __builtin_bit_cast(bf16x8, v); }
DI float swap_half_sum(float x) {
  auto r = __builtin_amdgcn_permlane32_swap(__float_as_uint(x), __float_as_uint(x), false, false);
  return __uint_as_float(r[0]) + __uint_as_float(r[1]);
}
DI float swap_half_max(float x) {
  auto r = __builtin_amdgcn_permlane32_swap(__float_as_uint(x), __float_as_uint(x), false, false);
  return fmaxf(__uint_as_float(r[0]), __uint_as_float(r[1]));
}
DI void glds16x2(const char* sbase, unsigned voff, unsigned lds_base) {
  unsigned sv;
  asm volatile("s_mov_b32 %0, m0\n\ts_mov_b32 m0, %3\n\ts_nop 2\n\tglobal_load_lds_dwordx4 %1, %2 sc1\n\t"
               "global_load_lds_dwordx4 %1, %2 offset:1024 sc1\n\ts_mov_b32 m0, %0"
               : "=&s"(sv) : "v"(voff), "s"(sbase), "s"(lds_base) : "memory");
}
DI void glds16x4(const char* sbase, unsigned voff, unsigned lds_base) {
  unsigned sv;
  asm volatile("s_mov_b32 %0, m0\n\ts_mov_b32 m0, %3\n\ts_nop 2\n\tglobal_load_lds_dwordx4 %1, %2 sc1\n\t"
               "global_load_lds_dwordx4 %1, %2 offset:1024 sc1\n\tglobal_load_lds_dwordx4 %1, %2 offset:2048 sc1\n\t"
               "global_load_lds_dwordx4 %1, %2 offset:3072 sc1\n\ts_mov_b32 m0, %0"
               : "=&s"(sv) : "v"(voff), "s"(sbase), "s"(lds_base) : "memory");
}
template <int I> struct IC { static constexpr int v = I; };
template <int I, int N, class F> DI void static_for(F&& f) {
  if constexpr (I < N) { f(IC<I>{}); static_for<I + 1, N>(f); }
}

struct Ctx {
  char* ws; LAS char* lds; __amdgpu_buffer_rsrc_t rs;
  u64 token; unsigned lbase, voff;
  int bid, lane, w, r, h, tb, nb, tb64, nb8, head, qb;
  bool dead;
};
DI void fresh(Ctx& c) {
  asm volatile("" : "+s"(c.bid));
  asm volatile("" : "+s"(c.w));
  asm volatile("" : "+v"(c.lane));
  const int g = c.bid & 7, idx = c.bid >> 3;
  c.r = c.lane & 31; c.h = c.lane >> 5; c.voff = c.lane * 16;
  c.tb = 2 * g + (idx >> 4); c.nb = idx & 15; c.tb64 = 4 * g + (idx >> 3); c.nb8 = idx & 7;
  c.head = 4 * g + (idx & 3); c.qb = idx >> 2;
}
DI u32x4 ld16(const Ctx& c, unsigned voff, unsigned soff) { return __builtin_amdgcn_raw_buffer_load_b128(c.rs, voff, soff, 16); }
DI float ld4(const Ctx& c, unsigned voff, unsigned soff) { return __uint_as_float(__builtin_amdgcn_raw_buffer_load_b32(c.rs, voff, soff, 16)); }
DI void st16(const Ctx& c, u32x4 v, unsigned voff, unsigned soff) { __builtin_amdgcn_raw_buffer_store_b128(v, c.rs, voff + soff, 0, 16); }
DI void st4(const Ctx& c, float v, unsigned voff, unsigned soff) { __builtin_amdgcn_raw_buffer_store_b32(__float_as_uint(v), c.rs, voff, soff, 16); }
DI gu64* flag_ptr(const Ctx& c, int line, int slot) { return (gu64*)(c.ws + WS_FLAG) + line * 32 + slot; }

DI void publish(const Ctx& c, int ctr, unsigned n, int line0, int slot) {
  asm volatile("s_waitcnt vmcnt(0)" ::: "memory");
  unsigned old = 0;
  if (c.lane == 0) old = __hip_atomic_fetch_add((LAS unsigned*)(c.lds + LD_CTR) + ctr, 1u, __ATOMIC_RELAXED, __HIP_MEMORY_SCOPE_WORKGROUP);
  if (__builtin_amdgcn_readfirstlane(old) == n - 1 && c.lane < 16) __hip_atomic_store(flag_ptr(c, line0 + c.lane, slot), c.token, RLX_AGENT);
}
DI void poll_any(Ctx& c, int line, int slot) {
  if (!c.dead) {
    gu64* p = flag_ptr(c, line, slot);
    unsigned spins = 0;
    while (!__all(__hip_atomic_load(p, RLX_AGENT) == c.token)) {
      if (++spins > (1u << 18)) { c.dead = true; break; }
    }
    __builtin_amdgcn_fence(__ATOMIC_ACQUIRE, "wavefront");
  }
}
template <bool NAP = false> DI void wait4(const Ctx& c, int ctr) {
  while (__hip_atomic_load((LAS unsigned*)(c.lds + LD_CTR) + ctr, __ATOMIC_RELAXED, __HIP_MEMORY_SCOPE_WORKGROUP) < 4u)
    if (NAP) __builtin_amdgcn_s_sleep(2);
  asm volatile("" ::: "memory");
}
DI void arrive4(const Ctx& c, int ctr) {
  asm volatile("s_waitcnt lgkmcnt(0)" ::: "memory");
  if (c.lane == 0) __hip_atomic_fetch_add((LAS unsigned*)(c.lds + LD_CTR) + ctr, 1u, __ATOMIC_RELAXED, __HIP_MEMORY_SCOPE_WORKGROUP);
}
DI void poll(Ctx& c, int line, int slot) { if (c.w == 0) poll_any(c, line, slot); }
template <int NQ> DI void help_dma(const Ctx& c, unsigned src, int dst) {
#pragma unroll
  for (int u = 0; u < NQ / 4; ++u) {
    const int p = (c.w & 3) + 4 * u;
    glds16x4(c.ws + src + p * 4096, c.voff, c.lbase + dst + p * 4096);
  }
}
DI int w2_run(int run) { return run < 6 ? LD_W + run * 14336 : LD_W2E + (run - 6) * 14336; }
template <int U0, int U1> DI void w2_fetch(const Ctx& c, u32x4 (&h)[28]) {
#pragma unroll
  for (int u = U0; u < U1; ++u) {
    const int f = (c.w & 3) + 4 * u, run = f / 14, kq = run >> 1;
    h[u] = ld16(c, c.voff, WS_W2 + ((2 * c.nb8 + (run & 1)) * KS_F + kq * 21 + (kq < 2 ? kq : 2) + f % 14) * 1024);
  }
  SBAR();
}
template <int U0, int U1> DI void w2_put(const Ctx& c, const u32x4 (&h)[28]) {
#pragma unroll
  for (int u = U0; u < U1; ++u) {
    const int f = (c.w & 3) + 4 * u;
    *(LAS u32x4*)(c.lds + (u < 21 ? LD_W : LD_W2E - 84 * 1024) + f * 1024 + c.voff) = h[u];
  }
}

DI void store_x(const Ctx& c, int inst, u32x4 (&res)[2]) {
  const int tt = 2 * c.tb64 + (c.w >> 1), nt = 2 * c.nb8 + (c.w & 1);
  st16(c, res[0], c.voff, WS_X + inst * (2 * MB) + (tt * 32 + 2 * nt) * 1024);
  st16(c, res[1], c.voff, WS_X + inst * (2 * MB) + (tt * 32 + 2 * nt + 1) * 1024);
  float s1 = 0.f, s2 = 0.f;
#pragma unroll
  for (int i = 0; i < 8; ++i) {
    const unsigned u = res[i >> 2][i & 3];
    const float a = bf_lo(u), b = bf_hi(u);
    s1 += a + b; s2 = fmaf(a, a, fmaf(b, b, s2));
  }
  auto r1 = __builtin_amdgcn_permlane32_swap(__float_as_uint(s1), __float_as_uint(s2), false, false);
  st4(c, __uint_as_float(r1[0]) + __uint_as_float(r1[1]), c.lane * 4, WS_ST + inst * (64 * 4096) + tt * 4096 + nt * 256);
  PIN(res[0]); PIN(res[1]);
}

DI void wfrag_load(const unsigned short* W, int N, int nt, int ks, int r, int h, unsigned (&e)[8]) {
#pragma unroll
  for (int j = 0; j < 8; ++j) e[j] = W[(size_t)(16 * ks + 8 * (j >> 2) + 4 * h + (j & 3)) * N + 32 * nt + r];
}
DI void wfrag_store(const Ctx& c, const unsigned (&e)[8], unsigned dst, bool docs, unsigned csdst) {
  u32x4 v;
#pragma unroll
  for (int j = 0; j < 4; ++j) v[j] = e[2 * j] | (e[2 * j + 1] << 16);
  st16(c, v, c.voff, dst);
  if (docs) {
    float s = 0.f;
#pragma unroll
    for (int j = 0; j < 8; ++j) s += __uint_as_float(e[j] << 16);
    s = swap_half_sum(s);
    if (c.h == 0) st4(c, s, c.r * 4, csdst);
  }
}

DI float mish_f(float hh) {
  const float u = __builtin_amdgcn_exp2f(hh * 1.4426950408889634f);
  const float r = __builtin_amdgcn_rcpf(fmaf(u, u + 2.f, 2.f));
  return hh * fmaf(-2.f, r, 1.f);
}
template <int MODE>
DI u32x2 epi4(float a0, float a1, float a2, float a3, f32x4 cs, f32x4 bb, float mr, float rstd) {
  const float a[4] = {a0, a1, a2, a3};
  float v[4];
#pragma unroll
  for (int j = 0; j < 4; ++j) {
    if (MODE == 0) v[j] = fmaf(a[j], rstd, mr * cs[j]);
    else v[j] = mish_f(fmaf(a[j], rstd, fmaf(mr, cs[j], bb[j])));
  }
  return (u32x2){pk2(v[0], v[1]), pk2(v[2], v[3])};
}
template <int MODE>
DI u32x4 epi8(const f32x8& a, const f32x4* cs, const f32x4* bb, float mr, float rstd) {
  const u32x2 lo = epi4<MODE>(a[0], a[1], a[2], a[3], cs[0], bb[0], mr, rstd);
  const u32x2 hi = epi4<MODE>(a[4], a[5], a[6], a[7], cs[1], bb[1], mr, rstd);
  return (u32x4){lo[0], lo[1], hi[0], hi[1]};
}
#define LO8(s) __builtin_shufflevector(s, s, 0, 1, 2, 3, 4, 5, 6, 7)
#define HI8(s) __builtin_shufflevector(s, s, 8, 9, 10, 11, 12, 13, 14, 15)
DI f32x4 q4(const f32x8& v, int g) { return g ? (f32x4){v[4], v[5], v[6], v[7]} : (f32x4){v[0], v[1], v[2], v[3]}; }
DI f32x8 cat8(f32x4 a, f32x4 b) { return __builtin_shufflevector(a, b, 0, 1, 2, 3, 4, 5, 6, 7); }

struct LnPar { f32x4 cs[6], bb[6]; float csv; };
template <int MODE, int T> DI void ln_params(const Ctx& c, LnPar& p) {
  const LAS float* par = (const LAS float*)(c.lds + LD_PAR) + MODE * 96 + 4 * c.h;
#pragma unroll
  for (int g = 0; g < 6; ++g) {
    const int o = g < 4 ? 32 * T + 8 * g : 64 + 16 * T + 8 * (g - 4);
    if (MODE == 1 || g < 4) p.cs[g] = *(const LAS f32x4*)(par + o);
    if (MODE == 1) p.bb[g] = *(const LAS f32x4*)(par + 96 + o);
  }
  if (MODE == 0) p.csv = ((const LAS float*)(c.lds + LD_PAR))[64 + c.r];
}
template <int T> DI void ln_tokstats(const Ctx& c, f32x4 (&ts)[4]) {
  const LAS f32x2* st = (const LAS f32x2*)(c.lds + LD_ST) + (c.w & 3) * 32 + 16 * T + 4 * c.h;
#pragma unroll
  for (int g = 0; g < 4; ++g) ts[g] = *(const LAS f32x4*)(st + 8 * (g >> 1) + 2 * (g & 1));
}
template <int MODE, int T>
DI void ln_epilogue(Ctx& c, int l, const f32x16& a, const f32x8& ch, float mean, float rstd, const LnPar& p, const f32x4 (&ts)[4]) {
  const int tt = 4 * c.tb + (c.w & 3);
  const unsigned lay = WS_L + l * L_SZ;
  if (MODE == 0) {
    const float rs = T ? rstd : rstd * (0.25f * 1.4426950408889634f), mr = -mean * rs;
    const unsigned vo = T ? c.h * 512 + ((c.r & 19) | ((c.r & 4) << 1) | ((c.r & 8) >> 1)) * 16 : c.voff;
    const unsigned so = lay + (T ? L_K : L_Q) + (2 * c.nb * 64 + tt) * 1024;
    st16(c, epi8<0>(LO8(a), p.cs, p.cs, mr, rs), vo, so);
    st16(c, epi8<0>(HI8(a), p.cs + 2, p.cs, mr, rs), vo, so + 65536);
    unsigned q[4];
#pragma unroll
    for (int g = 0; g < 4; ++g) q[g] = pk2((ch[2 * g] - ts[g][0] * p.csv) * ts[g][1], (ch[2 * g + 1] - ts[g][2] * p.csv) * ts[g][3]);
    st16(c, (u32x4){q[0], q[1], q[2], q[3]}, (c.r >> 4) * 65536 + c.h * 256 + (c.r & 15) * 16, lay + L_V + (2 * c.nb * 128 + 2 * tt + T) * 512);
    publish(c, 4 + l, 8, FL_Q + l * 256 + c.nb * 16, c.tb);
  } else {
    const float mr = -mean * rstd;
    const unsigned so = lay + L_H + (tt * KS_F + 2 * (c.nb < 11 ? 3 * c.nb : 33 + 2 * (c.nb - 11))) * 1024;
    st16(c, epi8<1>(LO8(a), p.cs, p.bb, mr, rstd), c.voff, so + T * 2048);
    st16(c, epi8<1>(HI8(a), p.cs + 2, p.bb + 2, mr, rstd), c.voff, so + T * 2048 + 1024);
    if (c.nb < 11) st16(c, epi8<1>(ch, p.cs + 4, p.bb + 4, mr, rstd), c.voff, so + 4096 + T * 1024);
    publish(c, 6 + l, 8, FL_H + l * 256 + c.tb * 16, c.nb);
  }
}

DI void pin16(f32x16& v) { PIN(v); }
DI u32x4 wfrag(const LAS char* base, int ks) { return *(const LAS u32x4*)(base + ks * 1024); }

template <int MODE, bool FIRST>
DI void phase_ln(Ctx& c, int l, int inst, u32x4 (&w2r)[16]) {
  const int tt = 4 * c.tb + c.w;
  const bool has3 = MODE == 0 || c.nb < 11;
  const unsigned xs = WS_X + inst * (2 * MB) + tt * 32768;
  constexpr int PD = 12;
  u32x4 xf[PD], sp[4];
#pragma unroll
  for (int k = 0; k < PD; ++k) xf[k] = ld16(c, c.voff, xs + k * 1024);
  SBAR();
  const LAS char* wl = c.lds + LD_W + c.lane * 16;
  const LAS char* wl2 = wl + 65536;
  asm volatile("" : "+v"(wl2));
  f32x16 aA, aB, aC;
#pragma unroll
  for (int i = 0; i < 16; ++i) { aA[i] = 0.f; aB[i] = 0.f; aC[i] = 0.f; }
  u32x4 fa[3], fb[3], fc[3];
  if (FIRST) BARRIER_LDS();
#pragma unroll
  for (int k = 0; k < 2; ++k) { fa[k] = wfrag(wl, k); fb[k] = wfrag(wl + 32768, k); fc[k] = wfrag(wl2, k); }
  float mean = 0.f, rstd = 0.f, sc4[4];
  LnPar par; f32x4 ts[4];
  LAS float* sc = (LAS float*)(c.lds + LD_SC) + c.w * 256;
  const int k0q = c.w * 21 + (c.w < 2 ? c.w : 2);
  const unsigned w2s = WS_W2 + ((2 * c.nb8 + (c.w & 1)) * KS_F + k0q + 14) * 1024;
  const unsigned w2o = WS_W2 + ((2 * c.nb8 + 1 - (c.w & 1)) * KS_F + k0q + 14) * 1024;
  static_for<0, 32>([&](auto kc) __attribute__((always_inline)) {
    constexpr int k = decltype(kc)::v;
    if constexpr (FIRST && (k & 7) == 6 && k < 24) asm volatile("s_barrier" ::: "memory");
    aA = MFMA(as_bf(fa[k % 3]), as_bf(xf[k % PD]), aA);
    pin16(aA); SBAR();
    if constexpr (k + 2 < 32) fa[(k + 2) % 3] = wfrag(wl, k + 2);
    if constexpr (k >= 1 && k - 1 + PD < 32) xf[(k - 1) % PD] = ld16(c, c.voff, xs + (k - 1 + PD) * 1024);
    if constexpr (k >= 33 - PD && k < 35 - PD) {
      sp[2 * (k + PD - 33)] = ld16(c, c.voff, WS_ST + inst * (64 * 4096) + tt * 4096 + 2 * (k + PD - 33) * 1024);
      sp[2 * (k + PD - 33) + 1] = ld16(c, c.voff, WS_ST + inst * (64 * 4096) + tt * 4096 + (2 * (k + PD - 33) + 1) * 1024);
    }
    SBAR();
    aB = MFMA(as_bf(fb[k % 3]), as_bf(xf[k % PD]), aB);
    pin16(aB); SBAR();
    if constexpr (k + 2 < 32) fb[(k + 2) % 3] = wfrag(wl + 32768, k + 2);
    if constexpr (MODE == 1 && k >= 23 && k < 31) {
      constexpr int i = k - 23;
      const int kk = (i == 7 && c.w >= 2) ? 6 : i;
      w2r[2 * i] = ld16(c, c.voff, w2s + kk * 1024); w2r[2 * i + 1] = ld16(c, c.voff, w2o + kk * 1024);
    }
    if constexpr (k == 28) {
      ((LAS f32x4*)sc)[c.lane] = (__builtin_bit_cast(f32x4, sp[0]) + __builtin_bit_cast(f32x4, sp[1])) +
                                 (__builtin_bit_cast(f32x4, sp[2]) + __builtin_bit_cast(f32x4, sp[3]));
#pragma unroll
      for (int i = 0; i < 4; ++i) sc4[i] = sc[i * 64 + c.lane];
    }
    if constexpr (k == 30) {
      const float s = (sc4[0] + sc4[1]) + (sc4[2] + sc4[3]);
      auto rr = __builtin_amdgcn_permlane32_swap(__float_as_uint(s), __float_as_uint(s), false, false);
      const float mu = __uint_as_float(rr[0]) * (1.f / D_);
      const float var = fmaxf(__uint_as_float(rr[1]) * (1.f / D_) - mu * mu, 0.f);
      mean = rbf(mu);
      rstd = __builtin_amdgcn_rcpf(rbf(__builtin_amdgcn_sqrtf(rbf(rbf(var) + 1e-5f))));
      ((LAS f32x2*)(c.lds + LD_ST))[c.w * 32 + c.r] = (f32x2){mean, rstd};
    }
    SBAR();
    if (has3) aC = MODE == 0 ? MFMA(as_bf(xf[k % PD]), as_bf(fc[k % 3]), aC) : MFMA(as_bf(fc[k % 3]), as_bf(xf[k % PD]), aC);
    pin16(aC); SBAR();
    if constexpr (k + 2 < 32) fc[(k + 2) % 3] = wfrag(wl2, k + 2);
    SBAR();
  });
  if (MODE == 0) ln_tokstats<0>(c, ts);
  if (!FIRST) ln_params<MODE, 0>(c, par);
  SBAR();
  {
    LAS f32x4* ex = (LAS f32x4*)(c.lds + LD_EX) + c.w * 384 + c.lane;
#pragma unroll
    for (int g = 0; g < 4; ++g) ex[g * 64] = (f32x4){aB[4 * g], aB[4 * g + 1], aB[4 * g + 2], aB[4 * g + 3]};
    ex[256] = (f32x4){aC[8], aC[9], aC[10], aC[11]}; ex[320] = (f32x4){aC[12], aC[13], aC[14], aC[15]};
  }
  BARRIER_LDS();
  if (FIRST) ln_params<MODE, 0>(c, par);
  ln_epilogue<MODE, 0>(c, l, aA, LO8(aC), mean, rstd, par, ts);
}
template <int MODE, bool FIRST>
DI void help_ln(Ctx& c, int l) {
  LnPar par; f32x4 ts[4];
  if (!FIRST) ln_params<MODE, 1>(c, par);
  BARRIER_LDS();
  if (FIRST) ln_params<MODE, 1>(c, par);
  const LAS f32x4* ex = (const LAS f32x4*)(c.lds + LD_EX) + (c.w & 3) * 384 + c.lane;
  const f32x2 ms = ((const LAS f32x2*)(c.lds + LD_ST))[(c.w & 3) * 32 + c.r];
  const f32x8 lo = cat8(ex[0], ex[64]), hi = cat8(ex[128], ex[192]), ch = cat8(ex[256], ex[320]);
  if (MODE == 0) ln_tokstats<1>(c, ts);
  SBAR();
  const f32x16 a = __builtin_shufflevector(lo, hi, 0, 1, 2, 3, 4, 5, 6, 7, 8, 9, 10, 11, 12, 13, 14, 15);
  ln_epilogue<MODE, 1>(c, l, a, ch, ms[0], ms[1], par, ts);
}
DI void help_colsum(const Ctx& c, int mode) {
  const int t = (c.w - 4) * 64 + c.lane;
  if (t < 96) {
    float v[32];
#pragma unroll
    for (int i = 0; i < 32; ++i) v[i] = ld4(c, t * 4, WS_CSP + ((mode * 16 + c.nb) * 32 + i) * 384);
    float s = 0.f;
#pragma unroll
    for (int i = 0; i < 32; i += 4) s += (v[i] + v[i + 1]) + (v[i + 2] + v[i + 3]);
    ((LAS float*)(c.lds + LD_PAR))[mode * 96 + t] = s;
  }
}

DI void reduce_res(Ctx& c, const f32x16 (&a)[4], u32x4 (&res)[2], int ctr) {
  LAS f32x4* red = (LAS f32x4*)(c.lds + LD_EX) + c.lane;
#pragma unroll
  for (int j = 1; j < 4; ++j) {
    LAS f32x4* p = red + ((c.w ^ j) * 3 + j - 1) * 256;
#pragma unroll
    for (int g = 0; g < 4; ++g) p[g * 64] = (f32x4){a[j][4 * g], a[j][4 * g + 1], a[j][4 * g + 2], a[j][4 * g + 3]};
  }
  if (ctr >= 0) { arrive4(c, ctr); wait4(c, ctr); } else BARRIER_LDS();
  const LAS f32x4* q = red + c.w * 768;
#pragma unroll
  for (int g = 0; g < 4; ++g) {
    const f32x4 s = ((f32x4){a[0][4 * g], a[0][4 * g + 1], a[0][4 * g + 2], a[0][4 * g + 3]} + q[g * 64]) + (q[256 + g * 64] + q[512 + g * 64]);
    const unsigned u0 = res[g >> 1][2 * (g & 1)], u1 = res[g >> 1][2 * (g & 1) + 1];
    res[g >> 1][2 * (g & 1)] = pk2(bf_lo(u0) + rbf(s[0]), bf_hi(u0) + rbf(s[1]));
    res[g >> 1][2 * (g & 1) + 1] = pk2(bf_lo(u1) + rbf(s[2]), bf_hi(u1) + rbf(s[3]));
  }
}
#define MM4(w0, w1, x0, x1) do { a[0] = MFMA(as_bf(w0), as_bf(x0), a[0]); a[1] = MFMA(as_bf(w1), as_bf(x0), a[1]); \
                                 a[2] = MFMA(as_bf(w0), as_bf(x1), a[2]); a[3] = MFMA(as_bf(w1), as_bf(x1), a[3]); } while (0)

DI void phase_wo(Ctx& c, int l, const u32x4 (&wo)[16], u32x4 (&res)[2]) {
  const unsigned xs = WS_L + l * L_SZ + L_O + ((2 * c.tb64 + (c.w >> 1)) * 32 + 8 * c.w) * 1024;
  const unsigned xo = WS_L + l * L_SZ + L_O + ((2 * c.tb64 + 1 - (c.w >> 1)) * 32 + 8 * c.w) * 1024;
  u32x4 xf[16];
#pragma unroll
  for (int k = 0; k < 8; ++k) { xf[2 * k] = ld16(c, c.voff, xs + k * 1024); xf[2 * k + 1] = ld16(c, c.voff, xo + k * 1024); }
  asm volatile("s_barrier" ::: "memory");
  SBAR();
  f32x16 a[4];
#pragma unroll
  for (int i = 0; i < 64; ++i) a[i >> 4][i & 15] = 0.f;
#pragma unroll
  for (int k = 0; k < 8; ++k) { MM4(wo[2 * k], wo[2 * k + 1], xf[2 * k], xf[2 * k + 1]); PIN(a[0]); PIN(a[1]); PIN(a[2]); PIN(a[3]); SBAR(); }
  reduce_res(c, a, res, -1);
}

DI void phase_w2(Ctx& c, int l, const u32x4 (&w2r)[16], u32x4 (&res)[2]) {
  const int k0 = c.w * 21 + (c.w < 2 ? c.w : 2);
  const unsigned xs = WS_L + l * L_SZ + L_H + ((2 * c.tb64 + (c.w >> 1)) * KS_F + k0) * 1024;
  const unsigned xo = WS_L + l * L_SZ + L_H + ((2 * c.tb64 + 1 - (c.w >> 1)) * KS_F + k0) * 1024;
  constexpr int WIN = 10;
  u32x4 xf[2 * WIN];
#pragma unroll
  for (int k = 0; k < WIN; ++k) { xf[2 * k] = ld16(c, c.voff, xs + k * 1024); xf[2 * k + 1] = ld16(c, c.voff, xo + k * 1024); }
  SBAR();
  const LAS u32x4* w0 = (const LAS u32x4*)(c.lds + w2_run(2 * c.w + (c.w & 1))) + c.lane;
  const LAS u32x4* w1 = (const LAS u32x4*)(c.lds + w2_run(2 * c.w + 1 - (c.w & 1))) + c.lane;
  f32x16 a[4];
#pragma unroll
  for (int i = 0; i < 64; ++i) a[i >> 4][i & 15] = 0.f;
  u32x4 f0[2], f1[2];
  f0[0] = w0[0]; f1[0] = w1[0];
  static_for<0, 22>([&](auto kc) __attribute__((always_inline)) {
    constexpr int k = decltype(kc)::v;
    const u32x4 g0 = k < 14 ? f0[k & 1] : w2r[k < 14 ? 0 : 2 * (k - 14)], g1 = k < 14 ? f1[k & 1] : w2r[k < 14 ? 0 : 2 * (k - 14) + 1];
    const u32x4 x0 = xf[2 * (k % WIN)], x1 = xf[2 * (k % WIN) + 1];
    constexpr int kn = k - 1 + WIN;
    const int kk = (kn == 21 && c.w >= 2) ? 20 : kn;
    if constexpr (kn == 22) asm volatile("s_barrier" ::: "memory");
    if (k < 21 || c.w < 2) {
      a[0] = MFMA(as_bf(g0), as_bf(x0), a[0]);
      pin16(a[0]); SBAR();
      if constexpr (k >= 1 && kn < 22) xf[2 * ((k - 1) % WIN)] = ld16(c, c.voff, xs + kk * 1024);
      SBAR();
      a[1] = MFMA(as_bf(g1), as_bf(x0), a[1]);
      pin16(a[1]); SBAR();
      if constexpr (k + 1 < 14) f0[(k + 1) & 1] = w0[(k + 1) * 64];
      SBAR();
      a[2] = MFMA(as_bf(g0), as_bf(x1), a[2]);
      pin16(a[2]); SBAR();
      if constexpr (k >= 1 && kn < 22) xf[2 * ((k - 1) % WIN) + 1] = ld16(c, c.voff, xo + kk * 1024);
      SBAR();
      a[3] = MFMA(as_bf(g1), as_bf(x1), a[3]);
      pin16(a[3]); SBAR();
      if constexpr (k + 1 < 14) f1[(k + 1) & 1] = w1[(k + 1) * 64];
      SBAR();
    }
  });
  if (l == 0) { arrive4(c, 16); wait4(c, 16); } else BARRIER_LDS();
  reduce_res(c, a, res, l == 0 ? 17 : -1);
}

#define SMF(a, b8, c) __builtin_amdgcn_smfmac_f32_32x32x32_bf16((a), __builtin_bit_cast(bf16x16, b8), (c), sidx, 0, 0)
#define LDF(p) (*(const LAS bf16x8*)(p))
DI float max16(const f32x16& s) {
  float a = fmaxf(fmaxf(s[0], s[1]), s[2]), b = fmaxf(fmaxf(s[3], s[4]), s[5]);
  a = fmaxf(fmaxf(a, s[6]), s[7]); b = fmaxf(fmaxf(b, s[8]), s[9]);
  a = fmaxf(fmaxf(a, s[10]), s[11]); b = fmaxf(fmaxf(b, s[12]), s[13]);
  a = fmaxf(fmaxf(a, s[14]), s[15]);
  return swap_half_max(fmaxf(a, b));
}
DI void attn_slow(const LAS char* kp, const LAS char* vp, bool isv, bf16x8 q, f32x16& o, float& m) {
  const int sidx = 0xDDDDDDDD;
  f32x16 zero;
#pragma unroll
  for (int i = 0; i < 16; ++i) zero[i] = 0.f;
  o = zero; m = -3.0e38f;
  for (int t = 0; t < 32; ++t) {
    f32x16 s = MFMA(LDF(kp + t * 1024), q, zero);
    const float mn = fmaxf(m, max16(s));
    const float al = __builtin_amdgcn_exp2f(m - mn);
    m = mn;
#pragma unroll
    for (int i = 0; i < 16; ++i) { o[i] *= al; s[i] = __builtin_amdgcn_exp2f(s[i] - mn); }
    const int vo = isv ? t * 1024 : 0;
    o = SMF(LDF(vp + vo), LO8(s), o);
    o = SMF(LDF(vp + vo + 512), HI8(s), o);
  }
}

struct In { const unsigned short *x, *wqkv, *wo, *w1, *b1, *w2; unsigned short* out; };
struct Jobs { unsigned e[4][8], dst[4]; bool ok[4]; };
DI void jobs_load(const Ctx& c, const In& in, Jobs& jb) {
  const int t0 = c.nb < 11 ? 3 * c.nb : 33 + 2 * (c.nb - 11), nt1 = c.nb < 11 ? 3 : 2;
  const unsigned short* W[4]; int N[4];
#pragma unroll
  for (int u = 0; u < 4; ++u) {
    const int j = c.w + 4 * u;
    int nt, ks;
    if (j < 6) { W[u] = in.w1; N[u] = F_; jb.ok[u] = (j >> 1) < nt1; nt = t0 + (jb.ok[u] ? j >> 1 : 0); ks = 2 * c.tb + (j & 1); jb.dst[u] = WS_W1 + (nt * 32 + ks) * 1024; }
    else if (j < 8) { W[u] = in.wo; N[u] = D_; jb.ok[u] = true; nt = 2 * c.nb8 + (j & 1); ks = c.tb64; jb.dst[u] = WS_WO + (nt * 32 + ks) * 1024; }
    else { W[u] = in.w2; N[u] = D_; ks = c.tb64 + 32 * ((j - 8) >> 1); jb.ok[u] = ks < KS_F; if (!jb.ok[u]) ks = 0; nt = 2 * c.nb8 + (j & 1); jb.dst[u] = WS_W2 + (nt * KS_F + ks) * 1024; }
    W[u] += 16 * ks * N[u] + 32 * nt;
    asm volatile("" : "+s"(W[u]));
    asm volatile("" : "+s"(N[u]));
  }
#pragma unroll
  for (int u = 0; u < 4; ++u) {
    const __amdgpu_buffer_rsrc_t rw = __builtin_amdgcn_make_buffer_rsrc((void*)W[u], 0, 32 * N[u], 0x00020000);
    const unsigned o = (4 * c.h * N[u] + c.r) * 2;
#pragma unroll
    for (int j = 0; j < 8; ++j) jb.e[u][j] = __builtin_amdgcn_raw_buffer_load_b16(rw, o, (8 * (j >> 2) + (j & 3)) * 2 * N[u], 0);
  }
}
DI void jobs_store(const Ctx& c, Jobs& jb) {
#pragma unroll
  for (int i = 0; i < 32; ++i) PIN(jb.e[i >> 3][i & 7]);
#pragma unroll
  for (int u = 0; u < 4; ++u) {
    const int j = c.w + 4 * u;
    if (jb.ok[u]) wfrag_store(c, jb.e[u], jb.dst[u], j < 6, WS_CSP + ((16 + c.nb) * 32 + 2 * c.tb + (j & 1)) * 384 + (j >> 1) * 128);
  }
}
DI void jobs_publish(const Ctx& c) {
  asm volatile("s_waitcnt vmcnt(0)" ::: "memory");
  unsigned old = 0;
  if (c.lane == 0) old = __hip_atomic_fetch_add((LAS unsigned*)(c.lds + LD_CTR) + 2, 1u, __ATOMIC_RELAXED, __HIP_MEMORY_SCOPE_WORKGROUP);
  if (__builtin_amdgcn_readfirstlane(old) == 3 && c.lane < 2)
    __hip_atomic_store(c.lane ? flag_ptr(c, FL_W1 + c.nb, c.tb) : flag_ptr(c, FL_W2 + c.nb8, c.tb64), c.token, RLX_AGENT);
}

#define EX1(r) "v_exp_f32 v" #r ", v" #r "\n\t"
#define ASTEP(KOFF, VOFF, kc_, kn_, v0_, v1_, nv0_, pv1_)                                                                  \
  asm volatile("s_waitcnt lgkmcnt(0)\n\t" EX1(64) EX1(65)                                                                  \
               "v_smfmac_f32_32x32x32_bf16 %[o1], %[pv1], v[88:95], %[idx]\n\t"                                            \
               "ds_read_b128 %[nv0], %[vp] offset:%[vo]\n\tds_read_b128 %[pv1], %[vp] offset:%[vo1]\n\t"                   \
               EX1(66) EX1(67) EX1(68)                                                                                     \
               "v_mfma_f32_32x32x16_bf16 v[80:95], %[kc], %[q1], %[c1]\n\t"                                                \
               "ds_read_b128 %[kc], %[kp] offset:%[ko]\n\t"                                                                \
               EX1(69) EX1(70) EX1(71) EX1(72) EX1(73)                                                                     \
               "v_smfmac_f32_32x32x32_bf16 %[o0], %[v0], v[64:71], %[idx]\n\t"                                             \
               EX1(74) EX1(75) EX1(76) EX1(77) EX1(78) EX1(79) EX1(80) EX1(81)                                             \
               "v_smfmac_f32_32x32x32_bf16 %[o0], %[v1], v[72:79], %[idx]\n\t"                                             \
               EX1(82) EX1(83) EX1(84)                                                                                     \
               "v_mfma_f32_32x32x16_bf16 v[64:79], %[kn], %[q0], %[c0]\n\t"                                                \
               EX1(85) EX1(86) EX1(87) EX1(88) EX1(89)                                                                     \
               "v_smfmac_f32_32x32x32_bf16 %[o1], %[v0], v[80:87], %[idx]\n\t"                                             \
               EX1(90) EX1(91) EX1(92) EX1(93) EX1(94) "v_exp_f32 v95, v95"                                                \
               : "+{v[64:79]}"(sA), "+{v[80:95]}"(sB), [o0] "+v"(o0), [o1] "+v"(o1), [kc] "+v"(kc_), [nv0] "=&v"(nv0_),    \
                 [pv1] "+v"(pv1_)                                                                                          \
               : [kn] "v"(kn_), [v0] "v"(v0_), [v1] "v"(v1_), [q0] "v"(q0), [q1] "v"(q1), [c0] "v"(cin0), [c1] "v"(cin1),  \
                 [idx] "v"(sidx), [kp] "v"(kpa), [vp] "v"(vpa), [ko] "n"(KOFF), [vo] "n"(VOFF), [vo1] "n"((VOFF) + 512)     \
               : "memory")

template <int half, int l>
DI void phase_attn(Ctx& c, const In& in, u32x4 (&wo)[16], u32x4 (&hw)[28]) {
  const int qt = c.qb * 8 + (c.w & 3) * 2;
  const unsigned lay = WS_L + l * L_SZ;
  const char* kvg = c.ws + lay + ((c.w & 1) ? L_V : L_K) + c.head * 65536 + ((c.w >> 1) & 1) * 32768 + half * 4096;
  const unsigned kvd = c.lbase + ((c.w & 1) ? LD_V : LD_W) + ((c.w >> 1) & 1) * 32768 + half * 4096;
  u32x4 qv0, qv1;
  asm volatile("s_nop 4\n\tglobal_load_dwordx4 %0, %2, %3 sc1\n\tglobal_load_dwordx4 %1, %2, %3 offset:1024 sc1"
               : "=&v"(qv0), "=&v"(qv1) : "v"(c.voff), "s"(c.ws + lay + L_Q + (c.head * 64 + qt) * 1024) : "memory");
#define KV4(u) glds16x4(kvg + (u) * 8192, c.voff, kvd + (u) * 8192)
  glds16x4(kvg, c.voff, kvd);
  if (half == 0) {
    const unsigned o1 = (c.lane & 11) == 0 ? 0x3f803f80u : 0u;
    ((LAS u32x4*)(c.lds + LD_ONE))[c.w * 64 + c.lane] = (u32x4){o1, o1, o1, o1};
  }
  int sidx = 0xDDDDDDDD;
  const bool isv = c.r < 16;
  const LAS char* kp0 = c.lds + LD_W + half * 32768 + c.lane * 16;
  const LAS char* vp0 = c.lds + (isv ? LD_V + half * 32768 : LD_ONE) + c.h * 256 + (c.r & 15) * 16;
  const int vstep = isv ? 4096 : 0;
  if (half == 0) asm volatile("s_waitcnt vmcnt(0) lgkmcnt(0)\n\ts_barrier" : "+v"(qv0), "+v"(qv1)::"memory");
  else asm volatile("s_waitcnt vmcnt(4)\n\ts_barrier" : "+v"(qv0), "+v"(qv1)::"memory");
  if (half == 0) __builtin_amdgcn_s_setprio(1);
  const bf16x8 q0 = as_bf(qv0), q1 = as_bf(qv1);
  f32x16 zero;
#pragma unroll
  for (int i = 0; i < 16; ++i) zero[i] = 0.f;
  f32x16 cin0, cin1, o0 = zero, o1 = zero, sA, sB = zero;
  float m0, m1;
  bf16x8 ka = LDF(kp0), kb = LDF(kp0 + 1024), va0 = LDF(vp0), va1 = LDF(vp0 + 512), vb0, vb1 = as_bf((u32x4){0u, 0u, 0u, 0u});
  {
    const f32x16 s = MFMA(ka, q0, zero), s1 = MFMA(ka, q1, zero);
    m0 = max16(s); m1 = max16(s1);
#pragma unroll
    for (int i = 0; i < 16; ++i) { cin0[i] = -m0; cin1[i] = -m1; sA[i] = s[i] - m0; }
  }
  unsigned kpa = (unsigned)(size_t)kp0, vpa = (unsigned)(size_t)vp0;
  asm volatile("s_nop 1" : "+v"(cin0), "+v"(cin1), "+v"(sA), "+v"(sB), "+v"(o0), "+v"(o1), "+v"(sidx), "+v"(vb1));
#define QUAD(A, B, MID, C, D)                                                        \
  do {                                                                               \
    ASTEP(2048, 1024, ka, kb, va0, va1, vb0, vb1); A;                                \
    ASTEP(3072, 2048, kb, ka, vb0, vb1, va0, va1); B; MID;                           \
    ASTEP(4096, 3072, ka, kb, va0, va1, vb0, vb1); C;                                \
    vpa += vstep;                                                                    \
    ASTEP(5120, 0, kb, ka, vb0, vb1, va0, va1); D;                                   \
    kpa += 4096;                                                                      \
  } while (0)
#define NOP (void)0
#define WBAR(n) asm volatile("s_waitcnt vmcnt(" #n ")\n\ts_barrier" ::: "memory")
  constexpr bool JOBS = half == 0 && l == 0;
  Jobs jb;
  KV4(1);
  QUAD(KV4(2), NOP, WBAR(8), KV4(3), if (JOBS) { jobs_load(c, in, jb); SBAR(); });
  QUAD(NOP, NOP, if (JOBS) WBAR(32); else WBAR(0); if (half == 1 && l == 1) (w2_fetch<0, 21>(c, hw)), NOP,
       if (JOBS) { jobs_store(c, jb); SBAR(); });
  QUAD(if (JOBS) jobs_publish(c), NOP, NOP,
       if (half == 1 && l == 0) { poll_any(c, FL_W2 + c.nb8, c.lane & 31); (w2_fetch<0, 21>(c, hw)); }, NOP);
  for (int it = 3; it < 8; ++it) QUAD(NOP, NOP, NOP, NOP, NOP);
#undef WBAR
  asm volatile("s_nop 1\n\tv_smfmac_f32_32x32x32_bf16 %[o1], %[pv1], v[88:95], %[idx]\n\ts_waitcnt lgkmcnt(0)\n\ts_nop 7\n\ts_nop 7"
               : [o1] "+v"(o1), "+v"(o0), "+v"(ka), "+v"(kb), "+v"(va0), "+v"(va1) : [pv1] "v"(vb1), "{v[80:95]}"(sB), [idx] "v"(sidx));
#undef QUAD
#undef KV4
  if (__any(!(o0[8] < 1.0995116e12f))) attn_slow(kp0, vp0, isv, q0, o0, m0);
  if (__any(!(o1[8] < 1.0995116e12f))) attn_slow(kp0, vp0, isv, q1, o1, m1);
  LAS f32x4* cmb = (LAS f32x4*)(c.lds + LD_CMB) + (c.w & 3) * 320 + c.lane;
  const unsigned w1src = WS_W1 + (c.nb < 11 ? 3 * c.nb : 33 + 2 * (c.nb - 11)) * 32768;
  if (half == 0) {
    __builtin_amdgcn_s_setprio(0);
    cmb[0] = (f32x4){o0[0], o0[1], o0[2], o0[3]}; cmb[64] = (f32x4){o0[4], o0[5], o0[6], o0[7]};
    cmb[128] = (f32x4){o1[0], o1[1], o1[2], o1[3]}; cmb[192] = (f32x4){o1[4], o1[5], o1[6], o1[7]};
    cmb[256] = (f32x4){o0[8], m0, o1[8], m1};
    arrive4(c, 12 + l);
    if (l == 0) poll_any(c, c.lane < 32 ? FL_W1 + c.nb : FL_W2 + c.nb8, c.lane < 32 ? (c.lane & 15) : (c.lane & 31));
    const unsigned wos = WS_WO + ((2 * c.nb8 + (c.w & 1)) * 32 + 8 * c.w) * 1024;
    const unsigned woo = WS_WO + ((2 * c.nb8 + 1 - (c.w & 1)) * 32 + 8 * c.w) * 1024;
#pragma unroll
    for (int k = 0; k < 8; ++k) { wo[2 * k] = ld16(c, c.voff, wos + k * 1024); wo[2 * k + 1] = ld16(c, c.voff, woo + k * 1024); }
    SBAR();
    wait4<true>(c, 12 + l);
    help_dma<8>(c, w1src, LD_W);
    if (c.nb < 11) help_dma<8>(c, w1src + 65536, LD_W + 65536);
  }
  BARRIER_LDS();
  if (half == 1) {
    const f32x4 lm = cmb[256];
#pragma unroll
    for (int u = 0; u < 2; ++u) {
      const f32x16& o = u ? o1 : o0; const float m = u ? m1 : m0;
      const f32x8 p2 = cat8(cmb[128 * u], cmb[128 * u + 64]);
      const float m2 = lm[2 * u + 1];
      const float mm = fmaxf(m, m2);
      const float f1 = __builtin_amdgcn_exp2f(m - mm), f2 = __builtin_amdgcn_exp2f(m2 - mm);
      const float inv = __builtin_amdgcn_rcpf(o[8] * f1 + lm[2 * u] * f2);
      const float g1 = f1 * inv, g2 = f2 * inv;
      unsigned p[4];
#pragma unroll
      for (int i = 0; i < 4; ++i) p[i] = pk2(o[2 * i] * g1 + p2[2 * i] * g2, o[2 * i + 1] * g1 + p2[2 * i + 1] * g2);
      st16(c, (u32x4){p[0], p[1], p[2], p[3]}, c.voff, lay + L_O + ((qt + u) * 32 + c.head) * 1024);
    }
    asm volatile("s_waitcnt vmcnt(0)" ::: "memory");
    if (c.lane < 8) __hip_atomic_store(flag_ptr(c, FL_O + l * 256 + (c.qb * 4 + (c.w & 3)) * 8 + c.lane, c.head), c.token, RLX_AGENT);
  }
}

DI u64 uniform64(u64 v) {
  return ((u64)__builtin_amdgcn_readfirstlane((unsigned)(v >> 32)) << 32) | (unsigned)__builtin_amdgcn_readfirstlane((unsigned)v);
}

DI void run_main(Ctx& c, const In& in) {
  const u64 epoch = __hip_atomic_load((gu64*)c.ws, RLX_AGENT);
  u32x4 res[2];
  {
    if (c.w == 0 && c.lane < 32) ((LAS unsigned*)(c.lds + LD_CTR))[c.lane] = 0u;
    const unsigned short* src = in.x + (size_t)((2 * c.tb64 + (c.w >> 1)) * 32 + c.r) * D_ + 32 * (2 * c.nb8 + (c.w & 1)) + 4 * c.h;
    u32x2 xr[4];
#pragma unroll
    for (int i = 0; i < 4; ++i) xr[i] = *(const u32x2*)(src + 8 * i);
    BARRIER_LDS();
    c.token = uniform64(epoch) + 1;
    res[0] = (u32x4){xr[0][0], xr[0][1], xr[1][0], xr[1][1]}; res[1] = (u32x4){xr[2][0], xr[2][1], xr[3][0], xr[3][1]};
    store_x(c, 0, res);
    publish(c, 0, 4, FL_X + c.tb * 16, c.nb);
    poll(c, (c.lane & 16) ? FL_WQ + c.nb * 16 + c.tb : FL_X + c.tb * 16 + c.nb, c.lane & 15);
    BARRIER_LDS();
    u32x4 dummy[16];
    fresh(c);
    phase_ln<0, true>(c, 0, 0, dummy);
  }
  for (int l = 0; l < 2; ++l) {
    u32x4 wo[16], w2r[16];
    if (l == 1) {
      fresh(c);
      u32x4 dummy[16];
      phase_ln<0, false>(c, l, 2 * l, dummy);
    }
    fresh(c);
    poll(c, FL_Q + l * 256 + c.head * 8 + c.qb, c.lane & 15);
    BARRIER_LDS();
    { u32x4 unused[28]; if (l == 0) phase_attn<0, 0>(c, in, wo, unused); else phase_attn<0, 1>(c, in, wo, unused); }
    fresh(c);
    poll(c, FL_O + l * 256 + c.tb64 * 8 + c.nb8, c.lane & 31);
    BARRIER_LDS();
    phase_wo(c, l, wo, res);
    store_x(c, 2 * l + 1, res);
    publish(c, 8 + l, 4, FL_X + (2 * l + 1) * 256 + c.tb * 16, c.nb);
    fresh(c);
    poll(c, FL_X + (2 * l + 1) * 256 + c.tb * 16 + c.nb, c.lane & 15);
    BARRIER_ALL();
    phase_ln<1, false>(c, l, 2 * l + 1, w2r);
    fresh(c);
    poll(c, FL_H + l * 256 + c.tb * 16 + c.nb, c.lane & 15);
    BARRIER_LDS();
    phase_w2(c, l, w2r, res);
    fresh(c);
    if (l == 0) {
      store_x(c, 2, res);
      publish(c, 10, 4, FL_X + 2 * 256 + c.tb * 16, c.nb);
      poll(c, FL_X + 2 * 256 + c.tb * 16 + c.nb, c.lane & 15);
      BARRIER_LDS();
    }
  }
  {
    auto s0 = __builtin_amdgcn_permlane32_swap(res[0][0], res[0][2], false, false), s1 = __builtin_amdgcn_permlane32_swap(res[0][1], res[0][3], false, false);
    auto s2 = __builtin_amdgcn_permlane32_swap(res[1][0], res[1][2], false, false), s3 = __builtin_amdgcn_permlane32_swap(res[1][1], res[1][3], false, false);
    unsigned short* op = in.out + (size_t)((2 * c.tb64 + (c.w >> 1)) * 32 + c.r) * D_ + 32 * (2 * c.nb8 + (c.w & 1)) + 8 * c.h;
    __builtin_nontemporal_store((u32x4){s0[0], s1[0], s0[1], s1[1]}, (u32x4*)op);
    __builtin_nontemporal_store((u32x4){s2[0], s3[0], s2[1], s3[1]}, (u32x4*)(op + 16));
  }
  if (c.bid == 0 && c.w == 0 && c.lane == 0) __hip_atomic_store((gu64*)c.ws, c.token, RLX_AGENT);
}

DI void run_help(Ctx& c, const In& in) {
  const u64 epoch = __hip_atomic_load((gu64*)c.ws, RLX_AGENT);
  {
    unsigned e0[8], e1[8];
    const int qk = 2 * c.tb + (c.w & 1), qa = (c.w >> 1) & 1;
    wfrag_load(in.wqkv, 3 * D_, qa * 16 + c.nb, qk, c.r, c.h, e0);
    wfrag_load(in.wqkv, 3 * D_, 32 + c.nb, qk, c.r, c.h, e1);
    const int t = (c.w & 3) * 64 + c.lane;
    const int bcol = 32 * (c.nb < 11 ? 3 * c.nb : 33 + 2 * (c.nb - 11)) + t;
    const unsigned bv = in.b1[bcol < F_ ? bcol : 0];
    BARRIER_LDS();
    c.token = uniform64(epoch) + 1;
    wfrag_store(c, e0, WS_WQKV + ((qa * 16 + c.nb) * 32 + qk) * 1024, true, WS_CSP + (c.nb * 32 + qk) * 384 + qa * 128);
    if (qa == 0) wfrag_store(c, e1, WS_WQKV + ((32 + c.nb) * 32 + qk) * 1024, true, WS_CSP + (c.nb * 32 + qk) * 384 + 256);
    publish(c, 1, 4, FL_WQ + c.nb * 16, c.tb);
    if (t < 96) ((LAS float*)(c.lds + LD_PAR))[192 + t] = __uint_as_float(bv << 16);
    BARRIER_LDS();
    fresh(c);
#define WQC(q) _Pragma("unroll") for (int t_ = 0; t_ < 3; ++t_) \
    glds16x2(c.ws + WS_WQKV + ((16 * t_ + c.nb) * 32 + 8 * (q) + 2 * (c.w & 3)) * 1024, c.voff, c.lbase + LD_W + t_ * 32768 + (8 * (q) + 2 * (c.w & 3)) * 1024)
    WQC(0); WQC(1);
    asm volatile("s_waitcnt vmcnt(6)\n\ts_barrier" ::: "memory");
    WQC(2);
    asm volatile("s_waitcnt vmcnt(6)\n\ts_barrier" ::: "memory");
    WQC(3);
    asm volatile("s_waitcnt vmcnt(6)\n\ts_barrier" ::: "memory");
    asm volatile("s_waitcnt vmcnt(0)\n\ts_barrier" ::: "memory");
#undef WQC
    help_colsum(c, 0);
    help_ln<0, true>(c, 0);
  }
  for (int l = 0; l < 2; ++l) {
    u32x4 wo[16];
    fresh(c);
    if (l == 1) help_ln<0, false>(c, l);
    BARRIER_LDS();
    fresh(c);
    u32x4 hw[28];
    if (l == 0) phase_attn<1, 0>(c, in, wo, hw); else phase_attn<1, 1>(c, in, wo, hw);
    fresh(c);
    BARRIER_LDS();
    asm volatile("s_barrier" ::: "memory");
    help_dma<8>(c, WS_W1 + (c.nb < 11 ? 3 * c.nb + 1 : 34 + 2 * (c.nb - 11)) * 32768, LD_W + 32768);
    w2_fetch<21, 28>(c, hw);
    asm volatile("s_barrier" ::: "memory");
    if (l == 0) help_colsum(c, 1);
    BARRIER_ALL();
    w2_put<21, 28>(c, hw);
    help_ln<1, false>(c, l);
    w2_put<0, 21>(c, hw);
    BARRIER_LDS();
    asm volatile("s_barrier" ::: "memory");
    if (l == 0) {
      fresh(c);
#pragma unroll
      for (int u = 0; u < 24; ++u) hw[u] = ld16(c, c.voff, WS_WQKV + (((u >> 3) * 16 + c.nb) * 32 + (u & 7) * 4 + (c.w & 3)) * 1024);
      SBAR();
      wait4<true>(c, 16);
#pragma unroll
      for (int u = 0; u < 24; ++u) *(LAS u32x4*)(c.lds + LD_W + (u * 4 + (c.w & 3)) * 1024 + c.voff) = hw[u];
      BARRIER_LDS();
    } else {
      asm volatile("s_barrier\n\ts_barrier" ::: "memory");
    }
  }
}

__global__ __launch_bounds__(512, 2) void mega(const unsigned short* __restrict__ x, const unsigned short* __restrict__ wqkv,
                                               const unsigned short* __restrict__ wo_g, const unsigned short* __restrict__ w1,
                                               const unsigned short* __restrict__ b1, const unsigned short* __restrict__ w2,
                                               char* __restrict__ ws, unsigned short* __restrict__ out) {
  extern __shared__ __attribute__((aligned(16))) char lds_[];
  Ctx c;
  c.ws = ws; c.lds = (LAS char*)lds_; c.rs = __builtin_amdgcn_make_buffer_rsrc(ws, 0, WS_END, 0x00020000);
  c.lbase = (unsigned)(size_t)c.lds;
  c.bid = blockIdx.x; c.lane = threadIdx.x & 63; c.w = __builtin_amdgcn_readfirstlane(threadIdx.x >> 6); c.dead = false;
  fresh(c);
  const In in = {x, wqkv, wo_g, w1, b1, w2, out};
  if (c.w < 4) run_main(c, in); else run_help(c, in);
}

extern "C" void kernel_launch(void* const* d_in, const int* in_sizes, int n_in, void* d_out, int out_size, void* d_ws,
                              size_t ws_size, hipStream_t stream) {
  (void)hipFuncSetAttribute((const void*)mega, hipFuncAttributeMaxDynamicSharedMemorySize, LDS_BYTES);
  mega<<<256, 512, LDS_BYTES, stream>>>((const unsigned short*)d_in[0], (const unsigned short*)d_in[1], (const unsigned short*)d_in[2],
                                        (const unsigned short*)d_in[4], (const unsigned short*)d_in[5], (const unsigned short*)d_in[6],
                                        (char*)d_ws, (unsigned short*)d_out);
}
```

```cpp
#include <hip/hip_runtime.h>
#include <hip/hip_bf16.h>

typedef __attribute__((ext_vector_type(2))) __bf16 bf16x2;
typedef __attribute__((ext_vector_type(8))) __bf16 bf16x8;
typedef __attribute__((ext_vector_type(16))) __bf16 bf16x16;
typedef __attribute__((ext_vector_type(16))) float f32x16;
typedef __attribute__((ext_vector_type(8))) float f32x8;
typedef __attribute__((ext_vector_type(4))) float f32x4;
typedef __attribute__((ext_vector_type(2))) float f32x2;
typedef __attribute__((ext_vector_type(4))) unsigned u32x4;
typedef __attribute__((ext_vector_type(2))) unsigned u32x2;
typedef unsigned long long u64;
#define LAS __attribute__((address_space(3)))
#define GAS __attribute__((address_space(1)))
typedef GAS u64 gu64;
#define DI __device__ __forceinline__
#define RLX_AGENT __ATOMIC_RELAXED, __HIP_MEMORY_SCOPE_AGENT

constexpr int D_ = 512, F_ = 1376;
constexpr int KS_F = F_ / 16;

constexpr unsigned MB = 1u << 20;
constexpr unsigned WS_FLAG = 0;
constexpr unsigned WS_WQKV = 1024 * 1024;
constexpr unsigned WS_WO = WS_WQKV + 48 * 32 * 1024;
constexpr unsigned WS_W1 = WS_WO + 16 * 32 * 1024;
constexpr unsigned WS_W2 = WS_W1 + 43 * 32 * 1024;
constexpr unsigned WS_CSP = WS_W2 + 16 * 86 * 1024;
constexpr unsigned WS_ST = WS_CSP + 2 * 16 * 32 * 384;
constexpr unsigned WS_X = WS_ST + 4 * 64 * 4096;
constexpr unsigned WS_L = WS_X + 8 * MB;
constexpr unsigned L_Q = 0, L_K = 2 * MB, L_V = 4 * MB, L_O = 6 * MB, L_H = 8 * MB, L_SZ = 14 * MB;
constexpr unsigned WS_END = WS_L + 2 * L_SZ;
constexpr int FL_X = 1;
constexpr int FL_H = FL_X + 1024;
constexpr int FL_Q = FL_H + 512;
constexpr int FL_O = FL_Q + 512;
constexpr int FL_WQ = FL_O + 512;
constexpr int FL_W1 = FL_WQ + 256;
constexpr int FL_W2 = FL_W1 + 16;
static_assert((FL_W2 + 8) * 256 <= WS_WQKV, "flag region");

constexpr int LD_W = 0;
constexpr int LD_V = 65536;
constexpr int LD_EX = 98304;
constexpr int LD_ONE = 131072;
constexpr int LD_CMB = 135168;
constexpr int LD_W2E = 122880;
constexpr int LD_SC = 151552;
constexpr int LD_ST = 155648;
constexpr int LD_PAR = 157696;
constexpr int LD_CTR = LD_PAR + 1536;
constexpr int LDS_BYTES = 163840;

#define MFMA(a, b, c) __builtin_amdgcn_mfma_f32_32x32x16_bf16((a), (b), (c), 0, 0, 0)
#define SBAR() __builtin_amdgcn_sched_barrier(0)
#define PIN(x) asm volatile("" : "+v"(x))
#define BARRIER_LDS() asm volatile("s_waitcnt lgkmcnt(0)\n\ts_barrier" ::: "memory")
#define BARRIER_ALL() asm volatile("s_waitcnt vmcnt(0) lgkmcnt(0)\n\ts_barrier" ::: "memory")

DI unsigned pk2(float lo, float hi) {
  bf16x2 v; v[0] = (__bf16)lo; v[1] = (__bf16)hi;
  return __builtin_bit_cast(unsigned, v);
}
DI float bf_lo(unsigned u) { return __uint_as_float(u << 16); }
DI float bf_hi(unsigned u) { return __uint_as_float(u & 0xffff0000u); }
DI float rbf(float x) { return (float)(__bf16)x; }
DI bf16x8 as_bf(u32x4 v) { return __builtin_bit_cast(bf16x8, v); }
DI float swap_half_sum(float x) {
  auto r = __builtin_amdgcn_permlane32_swap(__float_as_uint(x), __float_as_uint(x), false, false);
  return __uint_as_float(r[0]) + __uint_as_float(r[1]);
}
DI float swap_half_max(float x) {
  auto r = __builtin_amdgcn_permlane32_swap(__float_as_uint(x), __float_as_uint(x), false, false);
  return fmaxf(__uint_as_float(r[0]), __uint_as_float(r[1]));
}
DI void glds16x2(const char* sbase, unsigned voff, unsigned lds_base) {
  unsigned sv;
  asm volatile("s_mov_b32 %0, m0\n\ts_mov_b32 m0, %3\n\ts_nop 2\n\tglobal_load_lds_dwordx4 %1, %2 sc1\n\t"
               "global_load_lds_dwordx4 %1, %2 offset:1024 sc1\n\ts_mov_b32 m0, %0"
               : "=&s"(sv) : "v"(voff), "s"(sbase), "s"(lds_base) : "memory");
}
DI void glds16x4(const char* sbase, unsigned voff, unsigned lds_base) {
  unsigned sv;
  asm volatile("s_mov_b32 %0, m0\n\ts_mov_b32 m0, %3\n\ts_nop 2\n\tglobal_load_lds_dwordx4 %1, %2 sc1\n\t"
               "global_load_lds_dwordx4 %1, %2 offset:1024 sc1\n\tglobal_load_lds_dwordx4 %1, %2 offset:2048 sc1\n\t"
               "global_load_lds_dwordx4 %1, %2 offset:3072 sc1\n\ts_mov_b32 m0, %0"
               : "=&s"(sv) : "v"(voff), "s"(sbase), "s"(lds_base) : "memory");
}
template <int I> struct IC { static constexpr int v = I; };
template <int I, int N, class F> DI void static_for(F&& f) {
  if constexpr (I < N) { f(IC<I>{}); static_for<I + 1, N>(f); }
}

struct Ctx {
  char* ws; LAS char* lds; __amdgpu_buffer_rsrc_t rs;
  u64 token; unsigned lbase, voff;
  int bid, lane, w, r, h, tb, nb, tb64, nb8, head, qb;
  bool dead;
};
DI void fresh(Ctx& c) {
  asm volatile("" : "+s"(c.bid));
  asm volatile("" : "+s"(c.w));
  asm volatile("" : "+v"(c.lane));
  const int g = c.bid & 7, idx = c.bid >> 3;
  c.r = c.lane & 31; c.h = c.lane >> 5; c.voff = c.lane * 16;
  c.tb = 2 * g + (idx >> 4); c.nb = idx & 15; c.tb64 = 4 * g + (idx >> 3); c.nb8 = idx & 7;
  c.head = 4 * g + (idx & 3); c.qb = idx >> 2;
}
DI u32x4 ld16(const Ctx& c, unsigned voff, unsigned soff) { return __builtin_amdgcn_raw_buffer_load_b128(c.rs, voff, soff, 16); }
DI float ld4(const Ctx& c, unsigned voff, unsigned soff) { return __uint_as_float(__builtin_amdgcn_raw_buffer_load_b32(c.rs, voff, soff, 16)); }
DI void st16(const Ctx& c, u32x4 v, unsigned voff, unsigned soff) { __builtin_amdgcn_raw_buffer_store_b128(v, c.rs, voff + soff, 0, 16); }
DI void st4(const Ctx& c, float v, unsigned voff, unsigned soff) { __builtin_amdgcn_raw_buffer_store_b32(__float_as_uint(v), c.rs, voff, soff, 16); }
DI gu64* flag_ptr(const Ctx& c, int line, int slot) { return (gu64*)(c.ws + WS_FLAG) + line * 32 + slot; }

DI void publish(const Ctx& c, int ctr, unsigned n, int line0, int slot) {
  asm volatile("s_waitcnt vmcnt(0)" ::: "memory");
  unsigned old = 0;
  if (c.lane == 0) old = __hip_atomic_fetch_add((LAS unsigned*)(c.lds + LD_CTR) + ctr, 1u, __ATOMIC_RELAXED, __HIP_MEMORY_SCOPE_WORKGROUP);
  if (__builtin_amdgcn_readfirstlane(old) == n - 1 && c.lane < 16) __hip_atomic_store(flag_ptr(c, line0 + c.lane, slot), c.token, RLX_AGENT);
}
DI void poll_any(Ctx& c, int line, int slot) {
  if (!c.dead) {
    gu64* p = flag_ptr(c, line, slot);
    unsigned spins = 0;
    while (!__all(__hip_atomic_load(p, RLX_AGENT) == c.token)) {
      if (++spins > (1u << 18)) { c.dead = true; break; }
    }
    __builtin_amdgcn_fence(__ATOMIC_ACQUIRE, "wavefront");
  }
}
template <bool NAP = false> DI void wait4(const Ctx& c, int ctr) {
  while (__hip_atomic_load((LAS unsigned*)(c.lds + LD_CTR) + ctr, __ATOMIC_RELAXED, __HIP_MEMORY_SCOPE_WORKGROUP) < 4u)
    if (NAP) __builtin_amdgcn_s_sleep(2);
  asm volatile("" ::: "memory");
}
DI void arrive4(const Ctx& c, int ctr) {
  asm volatile("s_waitcnt lgkmcnt(0)" ::: "memory");
  if (c.lane == 0) __hip_atomic_fetch_add((LAS unsigned*)(c.lds + LD_CTR) + ctr, 1u, __ATOMIC_RELAXED, __HIP_MEMORY_SCOPE_WORKGROUP);
}
DI void poll(Ctx& c, int line, int slot) { if (c.w == 0) poll_any(c, line, slot); }
template <int NQ> DI void help_dma(const Ctx& c, unsigned src, int dst) {
#pragma unroll
  for (int u = 0; u < NQ / 4; ++u) {
    const int p = (c.w & 3) + 4 * u;
    glds16x4(c.ws + src + p * 4096, c.voff, c.lbase + dst + p * 4096);
  }
}
DI int w2_run(int run) { return run < 6 ? LD_W + run * 14336 : LD_W2E + (run - 6) * 14336; }
template <int U0, int U1> DI void w2_fetch(const Ctx& c, u32x4 (&h)[28]) {
#pragma unroll
  for (int u = U0; u < U1; ++u) {
    const int f = (c.w & 3) + 4 * u, run = f / 14, kq = run >> 1;
    h[u] = ld16(c, c.voff, WS_W2 + ((2 * c.nb8 + (run & 1)) * KS_F + kq * 21 + (kq < 2 ? kq : 2) + f % 14) * 1024);
  }
  SBAR();
}
template <int U0, int U1> DI void w2_put(const Ctx& c, const u32x4 (&h)[28]) {
#pragma unroll
  for (int u = U0; u < U1; ++u) {
    const int f = (c.w & 3) + 4 * u;
    *(LAS u32x4*)(c.lds + (u < 21 ? LD_W : LD_W2E - 84 * 1024) + f * 1024 + c.voff) = h[u];
  }
}

DI void store_x(const Ctx& c, int inst, u32x4 (&res)[2]) {
  const int tt = 2 * c.tb64 + (c.w >> 1), nt = 2 * c.nb8 + (c.w & 1);
  st16(c, res[0], c.voff, WS_X + inst * (2 * MB) + (tt * 32 + 2 * nt) * 1024);
  st16(c, res[1], c.voff, WS_X + inst * (2 * MB) + (tt * 32 + 2 * nt + 1) * 1024);
  float s1 = 0.f, s2 = 0.f;
#pragma unroll
  for (int i = 0; i < 8; ++i) {
    const unsigned u = res[i >> 2][i & 3];
    const float a = bf_lo(u), b = bf_hi(u);
    s1 += a + b; s2 = fmaf(a, a, fmaf(b, b, s2));
  }
  auto r1 = __builtin_amdgcn_permlane32_swap(__float_as_uint(s1), __float_as_uint(s2), false, false);
  st4(c, __uint_as_float(r1[0]) + __uint_as_float(r1[1]), c.lane * 4, WS_ST + inst * (64 * 4096) + tt * 4096 + nt * 256);
  PIN(res[0]); PIN(res[1]);
}

DI void wfrag_load(const unsigned short* W, int N, int nt, int ks, int r, int h, unsigned (&e)[8]) {
#pragma unroll
  for (int j = 0; j < 8; ++j) e[j] = W[(size_t)(16 * ks + 8 * (j >> 2) + 4 * h + (j & 3)) * N + 32 * nt + r];
}
DI void wfrag_store(const Ctx& c, const unsigned (&e)[8], unsigned dst, bool docs, unsigned csdst) {
  u32x4 v;
#pragma unroll
  for (int j = 0; j < 4; ++j) v[j] = e[2 * j] | (e[2 * j + 1] << 16);
  st16(c, v, c.voff, dst);
  if (docs) {
    float s = 0.f;
#pragma unroll
    for (int j = 0; j < 8; ++j) s += __uint_as_float(e[j] << 16);
    s = swap_half_sum(s);
    if (c.h == 0) st4(c, s, c.r * 4, csdst);
  }
}

template <int MODE>
DI u32x4 epi8(const f32x8& a, float rs) {
  float v[8];
#pragma unroll
  for (int j = 0; j < 8; ++j) {
    if (MODE == 0) v[j] = a[j] * rs;
    else {
      const float u = __builtin_amdgcn_exp2f(a[j] * (rs * 1.4426950408889634f));
      v[j] = a[j] * fmaf(-2.f * rs, __builtin_amdgcn_rcpf(fmaf(u, u + 2.f, 2.f)), rs);
    }
  }
  return (u32x4){pk2(v[0], v[1]), pk2(v[2], v[3]), pk2(v[4], v[5]), pk2(v[6], v[7])};
}
#define LO8(s) __builtin_shufflevector(s, s, 0, 1, 2, 3, 4, 5, 6, 7)
#define HI8(s) __builtin_shufflevector(s, s, 8, 9, 10, 11, 12, 13, 14, 15)
DI f32x8 cat8(f32x4 a, f32x4 b) { return __builtin_shufflevector(a, b, 0, 1, 2, 3, 4, 5, 6, 7); }

template <int T> DI void ln_tokstats(const Ctx& c, f32x4 (&ts)[2]) {
  const LAS float* st = (const LAS float*)(c.lds + LD_ST) + (c.w & 3) * 32 + 16 * T + 4 * c.h;
  ts[0] = *(const LAS f32x4*)st; ts[1] = *(const LAS f32x4*)(st + 8);
}
template <int MODE, int T>
DI void ln_epilogue(Ctx& c, int l, const f32x16& a, const f32x8& ch, float rstd, const f32x4 (&ts)[2]) {
  const int tt = 4 * c.tb + (c.w & 3);
  const unsigned lay = WS_L + l * L_SZ;
  if (MODE == 0) {
    const float rs = T ? rstd : rstd * (0.25f * 1.4426950408889634f);
    const unsigned vo = T ? c.h * 512 + ((c.r & 19) | ((c.r & 4) << 1) | ((c.r & 8) >> 1)) * 16 : c.voff;
    const unsigned so = lay + (T ? L_K : L_Q) + (2 * c.nb * 64 + tt) * 1024;
    st16(c, epi8<0>(LO8(a), rs), vo, so);
    st16(c, epi8<0>(HI8(a), rs), vo, so + 65536);
    unsigned q[4];
#pragma unroll
    for (int g = 0; g < 4; ++g) q[g] = pk2(ch[2 * g] * ts[g >> 1][2 * (g & 1)], ch[2 * g + 1] * ts[g >> 1][2 * (g & 1) + 1]);
    st16(c, (u32x4){q[0], q[1], q[2], q[3]}, (c.r >> 4) * 65536 + c.h * 256 + (c.r & 15) * 16, lay + L_V + (2 * c.nb * 128 + 2 * tt + T) * 512);
    publish(c, 4 + l, 8, FL_Q + l * 256 + c.nb * 16, c.tb);
  } else {
    const unsigned so = lay + L_H + (tt * KS_F + 2 * (c.nb < 11 ? 3 * c.nb : 33 + 2 * (c.nb - 11))) * 1024;
    st16(c, epi8<1>(LO8(a), rstd), c.voff, so + T * 2048);
    st16(c, epi8<1>(HI8(a), rstd), c.voff, so + T * 2048 + 1024);
    if (c.nb < 11) st16(c, epi8<1>(ch, rstd), c.voff, so + 4096 + T * 1024);
    publish(c, 6 + l, 8, FL_H + l * 256 + c.tb * 16, c.nb);
  }
}

DI void pin16(f32x16& v) { PIN(v); }
DI u32x4 wfrag(const LAS char* base, int ks) { return *(const LAS u32x4*)(base + ks * 1024); }

template <int MODE, bool FIRST>
DI void phase_ln(Ctx& c, int l, int inst, u32x4 (&w2r)[16]) {
  const int tt = 4 * c.tb + c.w;
  const bool has3 = MODE == 0 || c.nb < 11;
  const unsigned xs = WS_X + inst * (2 * MB) + tt * 32768;
  constexpr int PD = 12;
  u32x4 xf[PD], sp[4];
#pragma unroll
  for (int k = 0; k < PD; ++k) xf[k] = ld16(c, c.voff, xs + k * 1024);
  SBAR();
  const LAS char* wl = c.lds + LD_W + c.lane * 16;
  const LAS char* wl2 = wl + 65536;
  asm volatile("" : "+v"(wl2));
  f32x16 aA, aB, aC;
#pragma unroll
  for (int i = 0; i < 16; ++i) { aA[i] = 0.f; aB[i] = 0.f; aC[i] = 0.f; }
  u32x4 fa[3], fb[3], fc[3];
  if (FIRST) BARRIER_LDS();
#pragma unroll
  for (int k = 0; k < 2; ++k) { fa[k] = wfrag(wl, k); fb[k] = wfrag(wl + 32768, k); fc[k] = wfrag(wl2, k); }
  float rstd = 0.f, sc4[4];
  f32x4 ts[2]; u32x2 we[3]; u32x4 xe;
  LAS float* sc = (LAS float*)(c.lds + LD_SC) + c.w * 256;
  const int k0q = c.w * 21 + (c.w < 2 ? c.w : 2);
  const unsigned w2s = WS_W2 + ((2 * c.nb8 + (c.w & 1)) * KS_F + k0q + 14) * 1024;
  const unsigned w2o = WS_W2 + ((2 * c.nb8 + 1 - (c.w & 1)) * KS_F + k0q + 14) * 1024;
  static_for<0, 32>([&](auto kc) __attribute__((always_inline)) {
    constexpr int k = decltype(kc)::v;
    if constexpr (FIRST && (((k & 7) == 6 && k < 24) || k == 31)) asm volatile("s_barrier" ::: "memory");
    aA = MFMA(as_bf(fa[k % 3]), as_bf(xf[k % PD]), aA);
    pin16(aA); SBAR();
    if constexpr (k + 2 < 32) fa[(k + 2) % 3] = wfrag(wl, k + 2);
    if constexpr (k >= 1 && k - 1 + PD < 32) xf[(k - 1) % PD] = ld16(c, c.voff, xs + (k - 1 + PD) * 1024);
    if constexpr (k >= 33 - PD && k < 35 - PD) {
      sp[2 * (k + PD - 33)] = ld16(c, c.voff, WS_ST + inst * (64 * 4096) + tt * 4096 + 2 * (k + PD - 33) * 1024);
      sp[2 * (k + PD - 33) + 1] = ld16(c, c.voff, WS_ST + inst * (64 * 4096) + tt * 4096 + (2 * (k + PD - 33) + 1) * 1024);
    }
    SBAR();
    aB = MFMA(as_bf(fb[k % 3]), as_bf(xf[k % PD]), aB);
    pin16(aB); SBAR();
    if constexpr (k + 2 < 32) fb[(k + 2) % 3] = wfrag(wl + 32768, k + 2);
    if constexpr (MODE == 1 && k >= 23 && k < 31) {
      constexpr int i = k - 23;
      const int kk = (i == 7 && c.w >= 2) ? 6 : i;
      w2r[2 * i] = ld16(c, c.voff, w2s + kk * 1024); w2r[2 * i + 1] = ld16(c, c.voff, w2o + kk * 1024);
    }
    if constexpr (k == 28) {
      ((LAS f32x4*)sc)[c.lane] = (__builtin_bit_cast(f32x4, sp[0]) + __builtin_bit_cast(f32x4, sp[1])) +
                                 (__builtin_bit_cast(f32x4, sp[2]) + __builtin_bit_cast(f32x4, sp[3]));
#pragma unroll
      for (int i = 0; i < 4; ++i) sc4[i] = sc[i * 64 + c.lane];
    }
    if constexpr (k == (FIRST ? 31 : 29)) {
#pragma unroll
      for (int i = 0; i < 3; ++i) we[i] = ((const LAS u32x2*)(c.lds + LD_PAR))[MODE * 96 + 32 * i + c.r];
    }
    if constexpr (k == 30) {
      const float s = (sc4[0] + sc4[1]) + (sc4[2] + sc4[3]);
      auto rr = __builtin_amdgcn_permlane32_swap(__float_as_uint(s), __float_as_uint(s), false, false);
      const float mu = __uint_as_float(rr[0]) * (1.f / D_);
      const float var = fmaxf(__uint_as_float(rr[1]) * (1.f / D_) - mu * mu, 0.f);
      const float sd = rbf(__builtin_amdgcn_sqrtf(rbf(rbf(var) + 1e-5f)));
      rstd = __builtin_amdgcn_rcpf(sd);
      ((LAS float*)(c.lds + LD_ST))[c.w * 32 + c.r] = rstd;
      xe = (u32x4){c.h ? 0u : pk2(-mu, -mu), c.h ? 0u : pk2(-mu, sd), 0u, 0u};
    }
    SBAR();
    if (has3) aC = MODE == 0 ? MFMA(as_bf(xf[k % PD]), as_bf(fc[k % 3]), aC) : MFMA(as_bf(fc[k % 3]), as_bf(xf[k % PD]), aC);
    pin16(aC); SBAR();
    if constexpr (k + 2 < 32) fc[(k + 2) % 3] = wfrag(wl2, k + 2);
    SBAR();
  });
  aB = MFMA(as_bf((u32x4){we[1][0], we[1][1], 0u, 0u}), as_bf(xe), aB);
  if (has3) aC = MODE == 0 ? MFMA(as_bf(xe), as_bf((u32x4){we[2][0], we[2][1], 0u, 0u}), aC) : MFMA(as_bf((u32x4){we[2][0], we[2][1], 0u, 0u}), as_bf(xe), aC);
  aA = MFMA(as_bf((u32x4){we[0][0], we[0][1], 0u, 0u}), as_bf(xe), aA);
  if (MODE == 0) ln_tokstats<0>(c, ts);
  SBAR();
  {
    LAS f32x4* ex = (LAS f32x4*)(c.lds + LD_EX) + c.w * 384 + c.lane;
#pragma unroll
    for (int g = 0; g < 4; ++g) ex[g * 64] = (f32x4){aB[4 * g], aB[4 * g + 1], aB[4 * g + 2], aB[4 * g + 3]};
    ex[256] = (f32x4){aC[8], aC[9], aC[10], aC[11]}; ex[320] = (f32x4){aC[12], aC[13], aC[14], aC[15]};
  }
  BARRIER_LDS();
  ln_epilogue<MODE, 0>(c, l, aA, LO8(aC), rstd, ts);
}
template <int MODE>
DI void help_ln(Ctx& c, int l) {
  f32x4 ts[2];
  BARRIER_LDS();
  const LAS f32x4* ex = (const LAS f32x4*)(c.lds + LD_EX) + (c.w & 3) * 384 + c.lane;
  const float rstd = ((const LAS float*)(c.lds + LD_ST))[(c.w & 3) * 32 + c.r];
  const f32x8 lo = cat8(ex[0], ex[64]), hi = cat8(ex[128], ex[192]), ch = cat8(ex[256], ex[320]);
  if (MODE == 0) ln_tokstats<1>(c, ts);
  SBAR();
  const f32x16 a = __builtin_shufflevector(lo, hi, 0, 1, 2, 3, 4, 5, 6, 7, 8, 9, 10, 11, 12, 13, 14, 15);
  ln_epilogue<MODE, 1>(c, l, a, ch, rstd, ts);
}
DI void colsum_load(const Ctx& c, int mode, float (&v)[32]) {
  const int t = min((c.w - 4) * 64 + c.lane, 95);
#pragma unroll
  for (int i = 0; i < 32; ++i) v[i] = ld4(c, t * 4, WS_CSP + ((mode * 16 + c.nb) * 32 + i) * 384);
  SBAR();
}
DI void colsum_put(const Ctx& c, int mode, const float (&v)[32], unsigned bias) {
  const int t = (c.w - 4) * 64 + c.lane;
  float s = 0.f;
#pragma unroll
  for (int i = 0; i < 32; i += 4) s += (v[i] + v[i + 1]) + (v[i + 2] + v[i + 3]);
  const float s0 = rbf(s), s1 = rbf(s - s0);
  if (t < 96) ((LAS u32x2*)(c.lds + LD_PAR))[mode * 96 + t] = (u32x2){pk2(s0, s1), (pk2(s - s0 - s1, 0.f) & 0xffffu) | (bias << 16)};
}

DI void reduce_res(Ctx& c, const f32x16 (&a)[4], u32x4 (&res)[2], int ctr) {
  LAS f32x4* red = (LAS f32x4*)(c.lds + LD_EX) + c.lane;
#pragma unroll
  for (int j = 1; j < 4; ++j) {
    LAS f32x4* p = red + ((c.w ^ j) * 3 + j - 1) * 256;
#pragma unroll
    for (int g = 0; g < 4; ++g) p[g * 64] = (f32x4){a[j][4 * g], a[j][4 * g + 1], a[j][4 * g + 2], a[j][4 * g + 3]};
  }
  if (ctr >= 0) { arrive4(c, ctr); wait4(c, ctr); } else BARRIER_LDS();
  const LAS f32x4* q = red + c.w * 768;
#pragma unroll
  for (int g = 0; g < 4; ++g) {
    const f32x4 s = ((f32x4){a[0][4 * g], a[0][4 * g + 1], a[0][4 * g + 2], a[0][4 * g + 3]} + q[g * 64]) + (q[256 + g * 64] + q[512 + g * 64]);
    const unsigned u0 = res[g >> 1][2 * (g & 1)], u1 = res[g >> 1][2 * (g & 1) + 1];
    res[g >> 1][2 * (g & 1)] = pk2(bf_lo(u0) + rbf(s[0]), bf_hi(u0) + rbf(s[1]));
    res[g >> 1][2 * (g & 1) + 1] = pk2(bf_lo(u1) + rbf(s[2]), bf_hi(u1) + rbf(s[3]));
  }
}
#define MM4(w0, w1, x0, x1) do { a[0] = MFMA(as_bf(w0), as_bf(x0), a[0]); a[1] = MFMA(as_bf(w1), as_bf(x0), a[1]); \
                                 a[2] = MFMA(as_bf(w0), as_bf(x1), a[2]); a[3] = MFMA(as_bf(w1), as_bf(x1), a[3]); } while (0)

DI void phase_wo(Ctx& c, int l, const u32x4 (&wo)[16], u32x4 (&res)[2]) {
  const unsigned xs = WS_L + l * L_SZ + L_O + ((2 * c.tb64 + (c.w >> 1)) * 32 + 8 * c.w) * 1024;
  const unsigned xo = WS_L + l * L_SZ + L_O + ((2 * c.tb64 + 1 - (c.w >> 1)) * 32 + 8 * c.w) * 1024;
  u32x4 xf[16];
#pragma unroll
  for (int k = 0; k < 8; ++k) { xf[2 * k] = ld16(c, c.voff, xs + k * 1024); xf[2 * k + 1] = ld16(c, c.voff, xo + k * 1024); }
  asm volatile("s_barrier" ::: "memory");
  SBAR();
  f32x16 a[4];
#pragma unroll
  for (int i = 0; i < 64; ++i) a[i >> 4][i & 15] = 0.f;
#pragma unroll
  for (int k = 0; k < 8; ++k) { MM4(wo[2 * k], wo[2 * k + 1], xf[2 * k], xf[2 * k + 1]); PIN(a[0]); PIN(a[1]); PIN(a[2]); PIN(a[3]); SBAR(); }
  reduce_res(c, a, res, -1);
}

DI void phase_w2(Ctx& c, int l, const u32x4 (&w2r)[16], u32x4 (&res)[2]) {
  const int k0 = c.w * 21 + (c.w < 2 ? c.w : 2);
  const unsigned xs = WS_L + l * L_SZ + L_H + ((2 * c.tb64 + (c.w >> 1)) * KS_F + k0) * 1024;
  const unsigned xo = WS_L + l * L_SZ + L_H + ((2 * c.tb64 + 1 - (c.w >> 1)) * KS_F + k0) * 1024;
  constexpr int WIN = 10;
  u32x4 xf[2 * WIN];
#pragma unroll
  for (int k = 0; k < WIN; ++k) { xf[2 * k] = ld16(c, c.voff, xs + k * 1024); xf[2 * k + 1] = ld16(c, c.voff, xo + k * 1024); }
  SBAR();
  const LAS u32x4* w0 = (const LAS u32x4*)(c.lds + w2_run(2 * c.w + (c.w & 1))) + c.lane;
  const LAS u32x4* w1 = (const LAS u32x4*)(c.lds + w2_run(2 * c.w + 1 - (c.w & 1))) + c.lane;
  f32x16 a[4];
#pragma unroll
  for (int i = 0; i < 64; ++i) a[i >> 4][i & 15] = 0.f;
  u32x4 f0[2], f1[2];
  f0[0] = w0[0]; f1[0] = w1[0];
  static_for<0, 22>([&](auto kc) __attribute__((always_inline)) {
    constexpr int k = decltype(kc)::v;
    const u32x4 g0 = k < 14 ? f0[k & 1] : w2r[k < 14 ? 0 : 2 * (k - 14)], g1 = k < 14 ? f1[k & 1] : w2r[k < 14 ? 0 : 2 * (k - 14) + 1];
    const u32x4 x0 = xf[2 * (k % WIN)], x1 = xf[2 * (k % WIN) + 1];
    constexpr int kn = k - 1 + WIN;
    const int kk = (kn == 21 && c.w >= 2) ? 20 : kn;
    if constexpr (kn == 22) asm volatile("s_barrier" ::: "memory");
    if (k < 21 || c.w < 2) {
      a[0] = MFMA(as_bf(g0), as_bf(x0), a[0]);
      pin16(a[0]); SBAR();
      if constexpr (k >= 1 && kn < 22) xf[2 * ((k - 1) % WIN)] = ld16(c, c.voff, xs + kk * 1024);
      SBAR();
      a[1] = MFMA(as_bf(g1), as_bf(x0), a[1]);
      pin16(a[1]); SBAR();
      if constexpr (k + 1 < 14) f0[(k + 1) & 1] = w0[(k + 1) * 64];
      SBAR();
      a[2] = MFMA(as_bf(g0), as_bf(x1), a[2]);
      pin16(a[2]); SBAR();
      if constexpr (k >= 1 && kn < 22) xf[2 * ((k - 1) % WIN) + 1] = ld16(c, c.voff, xo + kk * 1024);
      SBAR();
      a[3] = MFMA(as_bf(g1), as_bf(x1), a[3]);
      pin16(a[3]); SBAR();
      if constexpr (k + 1 < 14) f1[(k + 1) & 1] = w1[(k + 1) * 64];
      SBAR();
    }
  });
  if (l == 0) { arrive4(c, 16); wait4(c, 16); } else BARRIER_LDS();
  reduce_res(c, a, res, l == 0 ? 17 : -1);
}

#define SMF(a, b8, c) __builtin_amdgcn_smfmac_f32_32x32x32_bf16((a), __builtin_bit_cast(bf16x16, b8), (c), sidx, 0, 0)
#define LDF(p) (*(const LAS bf16x8*)(p))
DI float max16(const f32x16& s) {
  float a = fmaxf(fmaxf(s[0], s[1]), s[2]), b = fmaxf(fmaxf(s[3], s[4]), s[5]);
  a = fmaxf(fmaxf(a, s[6]), s[7]); b = fmaxf(fmaxf(b, s[8]), s[9]);
  a = fmaxf(fmaxf(a, s[10]), s[11]); b = fmaxf(fmaxf(b, s[12]), s[13]);
  a = fmaxf(fmaxf(a, s[14]), s[15]);
  return swap_half_max(fmaxf(a, b));
}
DI void attn_slow(const LAS char* kp, const LAS char* vp, bool isv, bf16x8 q, f32x16& o, float& m) {
  const int sidx = 0xDDDDDDDD;
  f32x16 zero;
#pragma unroll
  for (int i = 0; i < 16; ++i) zero[i] = 0.f;
  o = zero; m = -3.0e38f;
  for (int t = 0; t < 32; ++t) {
    f32x16 s = MFMA(LDF(kp + t * 1024), q, zero);
    const float mn = fmaxf(m, max16(s));
    const float al = __builtin_amdgcn_exp2f(m - mn);
    m = mn;
#pragma unroll
    for (int i = 0; i < 16; ++i) { o[i] *= al; s[i] = __builtin_amdgcn_exp2f(s[i] - mn); }
    const int vo = isv ? t * 1024 : 0;
    o = SMF(LDF(vp + vo), LO8(s), o);
    o = SMF(LDF(vp + vo + 512), HI8(s), o);
  }
}

struct In { const unsigned short *x, *wqkv, *wo, *w1, *b1, *w2; unsigned short* out; };
struct Jobs { unsigned e[4][8], dst[4]; bool ok[4]; };
DI void jobs_load(const Ctx& c, const In& in, Jobs& jb) {
  const int t0 = c.nb < 11 ? 3 * c.nb : 33 + 2 * (c.nb - 11), nt1 = c.nb < 11 ? 3 : 2;
  const unsigned short* W[4]; int N[4];
#pragma unroll
  for (int u = 0; u < 4; ++u) {
    const int j = c.w + 4 * u;
    int nt, ks;
    if (j < 6) { W[u] = in.w1; N[u] = F_; jb.ok[u] = (j >> 1) < nt1; nt = t0 + (jb.ok[u] ? j >> 1 : 0); ks = 2 * c.tb + (j & 1); jb.dst[u] = WS_W1 + (nt * 32 + ks) * 1024; }
    else if (j < 8) { W[u] = in.wo; N[u] = D_; jb.ok[u] = true; nt = 2 * c.nb8 + (j & 1); ks = c.tb64; jb.dst[u] = WS_WO + (nt * 32 + ks) * 1024; }
    else { W[u] = in.w2; N[u] = D_; ks = c.tb64 + 32 * ((j - 8) >> 1); jb.ok[u] = ks < KS_F; if (!jb.ok[u]) ks = 0; nt = 2 * c.nb8 + (j & 1); jb.dst[u] = WS_W2 + (nt * KS_F + ks) * 1024; }
    W[u] += 16 * ks * N[u] + 32 * nt;
    asm volatile("" : "+s"(W[u]));
    asm volatile("" : "+s"(N[u]));
  }
#pragma unroll
  for (int u = 0; u < 4; ++u) {
    const __amdgpu_buffer_rsrc_t rw = __builtin_amdgcn_make_buffer_rsrc((void*)W[u], 0, 32 * N[u], 0x00020000);
    const unsigned o = (4 * c.h * N[u] + c.r) * 2;
#pragma unroll
    for (int j = 0; j < 8; ++j) jb.e[u][j] = __builtin_amdgcn_raw_buffer_load_b16(rw, o, (8 * (j >> 2) + (j & 3)) * 2 * N[u], 0);
  }
}
DI void jobs_store(const Ctx& c, Jobs& jb) {
#pragma unroll
  for (int i = 0; i < 32; ++i) PIN(jb.e[i >> 3][i & 7]);
#pragma unroll
  for (int u = 0; u < 4; ++u) {
    const int j = c.w + 4 * u;
    if (jb.ok[u]) wfrag_store(c, jb.e[u], jb.dst[u], j < 6, WS_CSP + ((16 + c.nb) * 32 + 2 * c.tb + (j & 1)) * 384 + (j >> 1) * 128);
  }
}
DI void jobs_publish(const Ctx& c) {
  asm volatile("s_waitcnt vmcnt(0)" ::: "memory");
  unsigned old = 0;
  if (c.lane == 0) old = __hip_atomic_fetch_add((LAS unsigned*)(c.lds + LD_CTR) + 2, 1u, __ATOMIC_RELAXED, __HIP_MEMORY_SCOPE_WORKGROUP);
  if (__builtin_amdgcn_readfirstlane(old) == 3 && c.lane < 2)
    __hip_atomic_store(c.lane ? flag_ptr(c, FL_W1 + c.nb, c.tb) : flag_ptr(c, FL_W2 + c.nb8, c.tb64), c.token, RLX_AGENT);
}

#define EX1(r) "v_exp_f32 v" #r ", v" #r "\n\t"
#define ASTEP(KOFF, VOFF, kc_, kn_, v0_, v1_, nv0_, pv1_)                                                                  \
  asm volatile("s_waitcnt lgkmcnt(0)\n\t" EX1(64) EX1(65)                                                                  \
               "v_smfmac_f32_32x32x32_bf16 %[o1], %[pv1], v[88:95], %[idx]\n\t"                                            \
               "ds_read_b128 %[nv0], %[vp] offset:%[vo]\n\tds_read_b128 %[pv1], %[vp] offset:%[vo1]\n\t"                   \
               EX1(66) EX1(67) EX1(68)                                                                                     \
               "v_mfma_f32_32x32x16_bf16 v[80:95], %[kc], %[q1], %[c1]\n\t"                                                \
               "ds_read_b128 %[kc], %[kp] offset:%[ko]\n\t"                                                                \
               EX1(69) EX1(70) EX1(71) EX1(72) EX1(73)                                                                     \
               "v_smfmac_f32_32x32x32_bf16 %[o0], %[v0], v[64:71], %[idx]\n\t"                                             \
               EX1(74) EX1(75) EX1(76) EX1(77) EX1(78) EX1(79) EX1(80) EX1(81)                                             \
               "v_smfmac_f32_32x32x32_bf16 %[o0], %[v1], v[72:79], %[idx]\n\t"                                             \
               EX1(82) EX1(83) EX1(84)                                                                                     \
               "v_mfma_f32_32x32x16_bf16 v[64:79], %[kn], %[q0], %[c0]\n\t"                                                \
               EX1(85) EX1(86) EX1(87) EX1(88) EX1(89)                                                                     \
               "v_smfmac_f32_32x32x32_bf16 %[o1], %[v0], v[80:87], %[idx]\n\t"                                             \
               EX1(90) EX1(91) EX1(92) EX1(93) EX1(94) "v_exp_f32 v95, v95"                                                \
               : "+{v[64:79]}"(sA), "+{v[80:95]}"(sB), [o0] "+v"(o0), [o1] "+v"(o1), [kc] "+v"(kc_), [nv0] "=&v"(nv0_),    \
                 [pv1] "+v"(pv1_)                                                                                          \
               : [kn] "v"(kn_), [v0] "v"(v0_), [v1] "v"(v1_), [q0] "v"(q0), [q1] "v"(q1), [c0] "v"(cin0), [c1] "v"(cin1),  \
                 [idx] "v"(sidx), [kp] "v"(kpa), [vp] "v"(vpa), [ko] "n"(KOFF), [vo] "n"(VOFF), [vo1] "n"((VOFF) + 512)     \
               : "memory")

template <int half, int l>
DI void phase_attn(Ctx& c, const In& in, u32x4 (&wo)[16], u32x4 (&hw)[28]) {
  const int qt = c.qb * 8 + (c.w & 3) * 2;
  const unsigned lay = WS_L + l * L_SZ;
  const char* kvg = c.ws + lay + ((c.w & 1) ? L_V : L_K) + c.head * 65536 + ((c.w >> 1) & 1) * 32768 + half * 4096;
  const unsigned kvd = c.lbase + ((c.w & 1) ? LD_V : LD_W) + ((c.w >> 1) & 1) * 32768 + half * 4096;
  u32x4 qv0, qv1;
  asm volatile("s_nop 4\n\tglobal_load_dwordx4 %0, %2, %3 sc1\n\tglobal_load_dwordx4 %1, %2, %3 offset:1024 sc1"
               : "=&v"(qv0), "=&v"(qv1) : "v"(c.voff), "s"(c.ws + lay + L_Q + (c.head * 64 + qt) * 1024) : "memory");
#define KV4(u) glds16x4(kvg + (u) * 8192, c.voff, kvd + (u) * 8192)
  glds16x4(kvg, c.voff, kvd);
  if (half == 0) {
    const unsigned o1 = (c.lane & 11) == 0 ? 0x3f803f80u : 0u;
    ((LAS u32x4*)(c.lds + LD_ONE))[c.w * 64 + c.lane] = (u32x4){o1, o1, o1, o1};
  }
  int sidx = 0xDDDDDDDD;
  const bool isv = c.r < 16;
  const LAS char* kp0 = c.lds + LD_W + half * 32768 + c.lane * 16;
  const LAS char* vp0 = c.lds + (isv ? LD_V + half * 32768 : LD_ONE) + c.h * 256 + (c.r & 15) * 16;
  const int vstep = isv ? 4096 : 0;
  if (half == 0) asm volatile("s_waitcnt vmcnt(0) lgkmcnt(0)\n\ts_barrier" : "+v"(qv0), "+v"(qv1)::"memory");
  else asm volatile("s_waitcnt vmcnt(4)\n\ts_barrier" : "+v"(qv0), "+v"(qv1)::"memory");
  if (half == 0) __builtin_amdgcn_s_setprio(1);
  const bf16x8 q0 = as_bf(qv0), q1 = as_bf(qv1);
  f32x16 zero;
#pragma unroll
  for (int i = 0; i < 16; ++i) zero[i] = 0.f;
  f32x16 cin0, cin1, o0 = zero, o1 = zero, sA, sB = zero;
  float m0, m1;
  bf16x8 ka = LDF(kp0), kb = LDF(kp0 + 1024), va0 = LDF(vp0), va1 = LDF(vp0 + 512), vb0, vb1 = as_bf((u32x4){0u, 0u, 0u, 0u});
  {
    const f32x16 s = MFMA(ka, q0, zero), s1 = MFMA(ka, q1, zero);
    m0 = max16(s); m1 = max16(s1);
#pragma unroll
    for (int i = 0; i < 16; ++i) { cin0[i] = -m0; cin1[i] = -m1; sA[i] = s[i] - m0; }
  }
  unsigned kpa = (unsigned)(size_t)kp0, vpa = (unsigned)(size_t)vp0;
  asm volatile("s_nop 1" : "+v"(cin0), "+v"(cin1), "+v"(sA), "+v"(sB), "+v"(o0), "+v"(o1), "+v"(sidx), "+v"(vb1));
#define QUAD(A, B, MID, C, D)                                                        \
  do {                                                                               \
    ASTEP(2048, 1024, ka, kb, va0, va1, vb0, vb1); A;                                \
    ASTEP(3072, 2048, kb, ka, vb0, vb1, va0, va1); B; MID;                           \
    ASTEP(4096, 3072, ka, kb, va0, va1, vb0, vb1); C;                                \
    vpa += vstep;                                                                    \
    ASTEP(5120, 0, kb, ka, vb0, vb1, va0, va1); D;                                   \
    kpa += 4096;                                                                      \
  } while (0)
#define NOP (void)0
#define WBAR(n) asm volatile("s_waitcnt vmcnt(" #n ")\n\ts_barrier" ::: "memory")
  constexpr bool JOBS = half == 0 && l == 0;
  Jobs jb;
  KV4(1);
  QUAD(KV4(2), NOP, WBAR(8), KV4(3), if (JOBS) { jobs_load(c, in, jb); SBAR(); });
  QUAD(NOP, NOP, if (JOBS) WBAR(32); else WBAR(0); if (half == 1 && l == 1) (w2_fetch<0, 21>(c, hw)), NOP,
       if (JOBS) { jobs_store(c, jb); SBAR(); });
  QUAD(if (JOBS) jobs_publish(c), NOP, NOP,
       if (half == 1 && l == 0) { poll_any(c, FL_W2 + c.nb8, c.lane & 31); (w2_fetch<0, 21>(c, hw)); }, NOP);
  for (int it = 3; it < 8; ++it) QUAD(NOP, NOP, NOP, NOP, NOP);
#undef WBAR
  asm volatile("s_nop 1\n\tv_smfmac_f32_32x32x32_bf16 %[o1], %[pv1], v[88:95], %[idx]\n\ts_waitcnt lgkmcnt(0)\n\ts_nop 7\n\ts_nop 7"
               : [o1] "+v"(o1), "+v"(o0), "+v"(ka), "+v"(kb), "+v"(va0), "+v"(va1) : [pv1] "v"(vb1), "{v[80:95]}"(sB), [idx] "v"(sidx));
#undef QUAD
#undef KV4
  if (__any(!(o0[8] < 1.0995116e12f))) attn_slow(kp0, vp0, isv, q0, o0, m0);
  if (__any(!(o1[8] < 1.0995116e12f))) attn_slow(kp0, vp0, isv, q1, o1, m1);
  LAS f32x4* cmb = (LAS f32x4*)(c.lds + LD_CMB) + (c.w & 3) * 320 + c.lane;
  const unsigned w1src = WS_W1 + (c.nb < 11 ? 3 * c.nb : 33 + 2 * (c.nb - 11)) * 32768;
  if (half == 0) {
    __builtin_amdgcn_s_setprio(0);
    cmb[0] = (f32x4){o0[0], o0[1], o0[2], o0[3]}; cmb[64] = (f32x4){o0[4], o0[5], o0[6], o0[7]};
    cmb[128] = (f32x4){o1[0], o1[1], o1[2], o1[3]}; cmb[192] = (f32x4){o1[4], o1[5], o1[6], o1[7]};
    cmb[256] = (f32x4){o0[8], m0, o1[8], m1};
    arrive4(c, 12 + l);
    if (l == 0) poll_any(c, c.lane < 32 ? FL_W1 + c.nb : FL_W2 + c.nb8, c.lane < 32 ? (c.lane & 15) : (c.lane & 31));
    const unsigned wos = WS_WO + ((2 * c.nb8 + (c.w & 1)) * 32 + 8 * c.w) * 1024;
    const unsigned woo = WS_WO + ((2 * c.nb8 + 1 - (c.w & 1)) * 32 + 8 * c.w) * 1024;
#pragma unroll
    for (int k = 0; k < 8; ++k) { wo[2 * k] = ld16(c, c.voff, wos + k * 1024); wo[2 * k + 1] = ld16(c, c.voff, woo + k * 1024); }
    SBAR();
    wait4<true>(c, 12 + l);
    help_dma<8>(c, w1src, LD_W);
    if (c.nb < 11) help_dma<8>(c, w1src + 65536, LD_W + 65536);
  }
  BARRIER_LDS();
  if (half == 1) {
    const f32x4 lm = cmb[256];
#pragma unroll
    for (int u = 0; u < 2; ++u) {
      const f32x16& o = u ? o1 : o0; const float m = u ? m1 : m0;
      const f32x8 p2 = cat8(cmb[128 * u], cmb[128 * u + 64]);
      const float m2 = lm[2 * u + 1];
      const float mm = fmaxf(m, m2);
      const float f1 = __builtin_amdgcn_exp2f(m - mm), f2 = __builtin_amdgcn_exp2f(m2 - mm);
      const float inv = __builtin_amdgcn_rcpf(o[8] * f1 + lm[2 * u] * f2);
      const float g1 = f1 * inv, g2 = f2 * inv;
      unsigned p[4];
#pragma unroll
      for (int i = 0; i < 4; ++i) p[i] = pk2(o[2 * i] * g1 + p2[2 * i] * g2, o[2 * i + 1] * g1 + p2[2 * i + 1] * g2);
      st16(c, (u32x4){p[0], p[1], p[2], p[3]}, c.voff, lay + L_O + ((qt + u) * 32 + c.head) * 1024);
    }
    asm volatile("s_waitcnt vmcnt(0)" ::: "memory");
    if (c.lane < 8) __hip_atomic_store(flag_ptr(c, FL_O + l * 256 + (c.qb * 4 + (c.w & 3)) * 8 + c.lane, c.head), c.token, RLX_AGENT);
  }
}

DI u64 uniform64(u64 v) {
  return ((u64)__builtin_amdgcn_readfirstlane((unsigned)(v >> 32)) << 32) | (unsigned)__builtin_amdgcn_readfirstlane((unsigned)v);
}

DI void run_main(Ctx& c, const In& in) {
  const u64 epoch = __hip_atomic_load((gu64*)c.ws, RLX_AGENT);
  u32x4 res[2];
  {
    if (c.w == 0 && c.lane < 32) ((LAS unsigned*)(c.lds + LD_CTR))[c.lane] = 0u;
    const unsigned short* src = in.x + (size_t)((2 * c.tb64 + (c.w >> 1)) * 32 + c.r) * D_ + 32 * (2 * c.nb8 + (c.w & 1)) + 4 * c.h;
    u32x2 xr[4];
#pragma unroll
    for (int i = 0; i < 4; ++i) xr[i] = *(const u32x2*)(src + 8 * i);
    BARRIER_LDS();
    c.token = uniform64(epoch) + 1;
    res[0] = (u32x4){xr[0][0], xr[0][1], xr[1][0], xr[1][1]}; res[1] = (u32x4){xr[2][0], xr[2][1], xr[3][0], xr[3][1]};
    store_x(c, 0, res);
    publish(c, 0, 4, FL_X + c.tb * 16, c.nb);
    poll(c, (c.lane & 16) ? FL_WQ + c.nb * 16 + c.tb : FL_X + c.tb * 16 + c.nb, c.lane & 15);
    BARRIER_LDS();
    u32x4 dummy[16];
    fresh(c);
    phase_ln<0, true>(c, 0, 0, dummy);
  }
  for (int l = 0; l < 2; ++l) {
    u32x4 wo[16], w2r[16];
    if (l == 1) {
      fresh(c);
      u32x4 dummy[16];
      phase_ln<0, false>(c, l, 2 * l, dummy);
    }
    fresh(c);
    poll(c, FL_Q + l * 256 + c.head * 8 + c.qb, c.lane & 15);
    BARRIER_LDS();
    { u32x4 unused[28]; if (l == 0) phase_attn<0, 0>(c, in, wo, unused); else phase_attn<0, 1>(c, in, wo, unused); }
    fresh(c);
    poll(c, FL_O + l * 256 + c.tb64 * 8 + c.nb8, c.lane & 31);
    BARRIER_LDS();
    phase_wo(c, l, wo, res);
    store_x(c, 2 * l + 1, res);
    publish(c, 8 + l, 4, FL_X + (2 * l + 1) * 256 + c.tb * 16, c.nb);
    fresh(c);
    poll(c, FL_X + (2 * l + 1) * 256 + c.tb * 16 + c.nb, c.lane & 15);
    BARRIER_ALL();
    phase_ln<1, false>(c, l, 2 * l + 1, w2r);
    fresh(c);
    poll(c, FL_H + l * 256 + c.tb * 16 + c.nb, c.lane & 15);
    BARRIER_LDS();
    phase_w2(c, l, w2r, res);
    fresh(c);
    if (l == 0) {
      store_x(c, 2, res);
      publish(c, 10, 4, FL_X + 2 * 256 + c.tb * 16, c.nb);
      poll(c, FL_X + 2 * 256 + c.tb * 16 + c.nb, c.lane & 15);
      BARRIER_LDS();
    }
  }
  {
    auto s0 = __builtin_amdgcn_permlane32_swap(res[0][0], res[0][2], false, false), s1 = __builtin_amdgcn_permlane32_swap(res[0][1], res[0][3], false, false);
    auto s2 = __builtin_amdgcn_permlane32_swap(res[1][0], res[1][2], false, false), s3 = __builtin_amdgcn_permlane32_swap(res[1][1], res[1][3], false, false);
    unsigned short* op = in.out + (size_t)((2 * c.tb64 + (c.w >> 1)) * 32 + c.r) * D_ + 32 * (2 * c.nb8 + (c.w & 1)) + 8 * c.h;
    __builtin_nontemporal_store((u32x4){s0[0], s1[0], s0[1], s1[1]}, (u32x4*)op);
    __builtin_nontemporal_store((u32x4){s2[0], s3[0], s2[1], s3[1]}, (u32x4*)(op + 16));
  }
  if (c.bid == 0 && c.w == 0 && c.lane == 0) __hip_atomic_store((gu64*)c.ws, c.token, RLX_AGENT);
}

DI void run_help(Ctx& c, const In& in) {
  const u64 epoch = __hip_atomic_load((gu64*)c.ws, RLX_AGENT);
  {
    unsigned e0[8], e1[8];
    const int qk = 2 * c.tb + (c.w & 1), qa = (c.w >> 1) & 1;
    wfrag_load(in.wqkv, 3 * D_, qa * 16 + c.nb, qk, c.r, c.h, e0);
    wfrag_load(in.wqkv, 3 * D_, 32 + c.nb, qk, c.r, c.h, e1);
    BARRIER_LDS();
    c.token = uniform64(epoch) + 1;
    wfrag_store(c, e0, WS_WQKV + ((qa * 16 + c.nb) * 32 + qk) * 1024, true, WS_CSP + (c.nb * 32 + qk) * 384 + qa * 128);
    if (qa == 0) wfrag_store(c, e1, WS_WQKV + ((32 + c.nb) * 32 + qk) * 1024, true, WS_CSP + (c.nb * 32 + qk) * 384 + 256);
    publish(c, 1, 4, FL_WQ + c.nb * 16, c.tb);
    BARRIER_LDS();
    fresh(c);
#define WQC(q) _Pragma("unroll") for (int t_ = 0; t_ < 3; ++t_) \
    glds16x2(c.ws + WS_WQKV + ((16 * t_ + c.nb) * 32 + 8 * (q) + 2 * (c.w & 3)) * 1024, c.voff, c.lbase + LD_W + t_ * 32768 + (8 * (q) + 2 * (c.w & 3)) * 1024)
    WQC(0); WQC(1);
    asm volatile("s_waitcnt vmcnt(6)\n\ts_barrier" ::: "memory");
    WQC(2);
    asm volatile("s_waitcnt vmcnt(6)\n\ts_barrier" ::: "memory");
    WQC(3);
    asm volatile("s_waitcnt vmcnt(6)\n\ts_barrier" ::: "memory");
    if (c.w < 6) {
      float cv[32];
      colsum_load(c, 0, cv);
      asm volatile("s_waitcnt vmcnt(32)\n\ts_barrier" ::: "memory");
      colsum_put(c, 0, cv, 0u);
    } else {
      asm volatile("s_waitcnt vmcnt(0)\n\ts_barrier" ::: "memory");
    }
    BARRIER_LDS();
#undef WQC
    help_ln<0>(c, 0);
  }
  for (int l = 0; l < 2; ++l) {
    u32x4 wo[16];
    fresh(c);
    if (l == 1) help_ln<0>(c, l);
    BARRIER_LDS();
    fresh(c);
    u32x4 hw[28];
    if (l == 0) phase_attn<1, 0>(c, in, wo, hw); else phase_attn<1, 1>(c, in, wo, hw);
    fresh(c);
    BARRIER_LDS();
    asm volatile("s_barrier" ::: "memory");
    help_dma<8>(c, WS_W1 + (c.nb < 11 ? 3 * c.nb + 1 : 34 + 2 * (c.nb - 11)) * 32768, LD_W + 32768);
    w2_fetch<21, 28>(c, hw);
    asm volatile("s_barrier" ::: "memory");
    if (l == 0 && c.w < 6) {
      const int bcol = 32 * (c.nb < 11 ? 3 * c.nb : 33 + 2 * (c.nb - 11)) + (c.w - 4) * 64 + c.lane;
      const unsigned bv = in.b1[bcol < F_ ? bcol : 0];
      float cv[32];
      colsum_load(c, 1, cv);
      colsum_put(c, 1, cv, bv);
    }
    BARRIER_ALL();
    w2_put<21, 28>(c, hw);
    help_ln<1>(c, l);
    w2_put<0, 21>(c, hw);
    BARRIER_LDS();
    asm volatile("s_barrier" ::: "memory");
    if (l == 0) {
      fresh(c);
#pragma unroll
      for (int u = 0; u < 24; ++u) hw[u] = ld16(c, c.voff, WS_WQKV + (((u >> 3) * 16 + c.nb) * 32 + (u & 7) * 4 + (c.w & 3)) * 1024);
      SBAR();
      wait4<true>(c, 16);
#pragma unroll
      for (int u = 0; u < 24; ++u) *(LAS u32x4*)(c.lds + LD_W + (u * 4 + (c.w & 3)) * 1024 + c.voff) = hw[u];
      BARRIER_LDS();
    } else {
      asm volatile("s_barrier\n\ts_barrier" ::: "memory");
    }
  }
}

__global__ __launch_bounds__(512, 2) void mega(const unsigned short* __restrict__ x, const unsigned short* __restrict__ wqkv,
                                               const unsigned short* __restrict__ wo_g, const unsigned short* __restrict__ w1,
                                               const unsigned short* __restrict__ b1, const unsigned short* __restrict__ w2,
                                               char* __restrict__ ws, unsigned short* __restrict__ out) {
  extern __shared__ __attribute__((aligned(16))) char lds_[];
  Ctx c;
  c.ws = ws; c.lds = (LAS char*)lds_; c.rs = __builtin_amdgcn_make_buffer_rsrc(ws, 0, WS_END, 0x00020000);
  c.lbase = (unsigned)(size_t)c.lds;
  c.bid = blockIdx.x; c.lane = threadIdx.x & 63; c.w = __builtin_amdgcn_readfirstlane(threadIdx.x >> 6); c.dead = false;
  fresh(c);
  const In in = {x, wqkv, wo_g, w1, b1, w2, out};
  if (c.w < 4) run_main(c, in); else run_help(c, in);
}

extern "C" void kernel_launch(void* const* d_in, const int* in_sizes, int n_in, void* d_out, int out_size, void* d_ws,
                              size_t ws_size, hipStream_t stream) {
  (void)hipFuncSetAttribute((const void*)mega, hipFuncAttributeMaxDynamicSharedMemorySize, LDS_BYTES);
  mega<<<256, 512, LDS_BYTES, stream>>>((const unsigned short*)d_in[0], (const unsigned short*)d_in[1], (const unsigned short*)d_in[2],
                                        (const unsigned short*)d_in[4], (const unsigned short*)d_in[5], (const unsigned short*)d_in[6],
                                        (char*)d_ws, (unsigned short*)d_out);
}
```

```cpp
#include <hip/hip_runtime.h>
#include <hip/hip_bf16.h>

typedef __attribute__((ext_vector_type(2))) __bf16 bf16x2;
typedef __attribute__((ext_vector_type(8))) __bf16 bf16x8;
typedef __attribute__((ext_vector_type(16))) __bf16 bf16x16;
typedef __attribute__((ext_vector_type(16))) float f32x16;
typedef __attribute__((ext_vector_type(8))) float f32x8;
typedef __attribute__((ext_vector_type(4))) float f32x4;
typedef __attribute__((ext_vector_type(2))) float f32x2;
typedef __attribute__((ext_vector_type(4))) unsigned u32x4;
typedef __attribute__((ext_vector_type(2))) unsigned u32x2;
typedef unsigned long long u64;
#define LAS __attribute__((address_space(3)))
#define GAS __attribute__((address_space(1)))
typedef GAS u64 gu64;
#define DI __device__ __forceinline__
#define RLX_AGENT __ATOMIC_RELAXED, __HIP_MEMORY_SCOPE_AGENT

constexpr int D_ = 512, F_ = 1376;
constexpr int KS_F = F_ / 16;

constexpr unsigned MB = 1u << 20;
constexpr unsigned WS_FLAG = 0;
constexpr unsigned WS_WQKV = 1024 * 1024;
constexpr unsigned WS_WO = WS_WQKV + 48 * 32 * 1024;
constexpr unsigned WS_W1 = WS_WO + 16 * 32 * 1024;
constexpr unsigned WS_W2 = WS_W1 + 43 * 32 * 1024;
constexpr unsigned WS_CSP = WS_W2 + 16 * 86 * 1024;
constexpr unsigned WS_ST = WS_CSP + 2 * 16 * 32 * 384;
constexpr unsigned WS_X = WS_ST + 4 * 64 * 4096;
constexpr unsigned WS_L = WS_X + 8 * MB;
constexpr unsigned L_Q = 0, L_K = 2 * MB, L_V = 4 * MB, L_O = 6 * MB, L_H = 8 * MB, L_SZ = 14 * MB;
constexpr unsigned WS_END = WS_L + 2 * L_SZ;
constexpr int FL_X = 1;
constexpr int FL_H = FL_X + 1024;
constexpr int FL_Q = FL_H + 512;
constexpr int FL_O = FL_Q + 512;
constexpr int FL_WQ = FL_O + 512;
constexpr int FL_W1 = FL_WQ + 256;
constexpr int FL_W2 = FL_W1 + 16;
static_assert((FL_W2 + 8) * 256 <= WS_WQKV, "flag region");

constexpr int LD_W = 0;
constexpr int LD_V = 65536;
constexpr int LD_EX = 98304;
constexpr int LD_ONE = 131072;
constexpr int LD_CMB = 135168;
constexpr int LD_W2E = 122880;
constexpr int LD_SC = 151552;
constexpr int LD_ST = 155648;
constexpr int LD_PAR = 157696;
constexpr int LD_CTR = LD_PAR + 1536;
constexpr int LDS_BYTES = 163840;

#define MFMA(a, b, c) __builtin_amdgcn_mfma_f32_32x32x16_bf16((a), (b), (c), 0, 0, 0)
#define SBAR() __builtin_amdgcn_sched_barrier(0)
#define PIN(x) asm volatile("" : "+v"(x))
#define BARRIER_LDS() asm volatile("s_waitcnt lgkmcnt(0)\n\ts_barrier" ::: "memory")
#define BARRIER_ALL() asm volatile("s_waitcnt vmcnt(0) lgkmcnt(0)\n\ts_barrier" ::: "memory")

DI unsigned pk2(float lo, float hi) {
  bf16x2 v; v[0] = (__bf16)lo; v[1] = (__bf16)hi;
  return __builtin_bit_cast(unsigned, v);
}
DI float bf_lo(unsigned u) { return __uint_as_float(u << 16); }
DI float bf_hi(unsigned u) { return __uint_as_float(u & 0xffff0000u); }
DI float rbf(float x) { return (float)(__bf16)x; }
DI bf16x8 as_bf(u32x4 v) { return __builtin_bit_cast(bf16x8, v); }
DI float swap_half_sum(float x) {
  auto r = __builtin_amdgcn_permlane32_swap(__float_as_uint(x), __float_as_uint(x), false, false);
  return __uint_as_float(r[0]) + __uint_as_float(r[1]);
}
DI float swap_half_max(float x) {
  auto r = __builtin_amdgcn_permlane32_swap(__float_as_uint(x), __float_as_uint(x), false, false);
  return fmaxf(__uint_as_float(r[0]), __uint_as_float(r[1]));
}
DI void glds16x2(const char* sbase, unsigned voff, unsigned lds_base) {
  unsigned sv;
  asm volatile("s_mov_b32 %0, m0\n\ts_mov_b32 m0, %3\n\ts_nop 2\n\tglobal_load_lds_dwordx4 %1, %2 sc1\n\t"
               "global_load_lds_dwordx4 %1, %2 offset:1024 sc1\n\ts_mov_b32 m0, %0"
               : "=&s"(sv) : "v"(voff), "s"(sbase), "s"(lds_base) : "memory");
}
DI void glds16x4(const char* sbase, unsigned voff, unsigned lds_base) {
  unsigned sv;
  asm volatile("s_mov_b32 %0, m0\n\ts_mov_b32 m0, %3\n\ts_nop 2\n\tglobal_load_lds_dwordx4 %1, %2 sc1\n\t"
               "global_load_lds_dwordx4 %1, %2 offset:1024 sc1\n\tglobal_load_lds_dwordx4 %1, %2 offset:2048 sc1\n\t"
               "global_load_lds_dwordx4 %1, %2 offset:3072 sc1\n\ts_mov_b32 m0, %0"
               : "=&s"(sv) : "v"(voff), "s"(sbase), "s"(lds_base) : "memory");
}
template <int I> struct IC { static constexpr int v = I; };
template <int I, int N, class F> DI void static_for(F&& f) {
  if constexpr (I < N) { f(IC<I>{}); static_for<I + 1, N>(f); }
}

struct Ctx {
  char* ws; LAS char* lds; __amdgpu_buffer_rsrc_t rs;
  u64 token; unsigned lbase, voff;
  int bid, lane, w, r, h, tb, nb, tb64, nb8, head, qb;
  bool dead;
};
DI void fresh(Ctx& c) {
  asm volatile("" : "+s"(c.bid));
  asm volatile("" : "+s"(c.w));
  asm volatile("" : "+v"(c.lane));
  const int g = c.bid & 7, idx = c.bid >> 3;
  c.r = c.lane & 31; c.h = c.lane >> 5; c.voff = c.lane * 16;
  c.tb = 2 * g + (idx >> 4); c.nb = idx & 15; c.tb64 = 4 * g + (idx >> 3); c.nb8 = idx & 7;
  c.head = 4 * g + (idx & 3); c.qb = idx >> 2;
}
DI u32x4 ld16(const Ctx& c, unsigned voff, unsigned soff) { return __builtin_amdgcn_raw_buffer_load_b128(c.rs, voff, soff, 16); }
DI float ld4(const Ctx& c, unsigned voff, unsigned soff) { return __uint_as_float(__builtin_amdgcn_raw_buffer_load_b32(c.rs, voff, soff, 16)); }
DI void st16(const Ctx& c, u32x4 v, unsigned voff, unsigned soff) { __builtin_amdgcn_raw_buffer_store_b128(v, c.rs, voff + soff, 0, 16); }
DI void st4(const Ctx& c, float v, unsigned voff, unsigned soff) { __builtin_amdgcn_raw_buffer_store_b32(__float_as_uint(v), c.rs, voff, soff, 16); }
DI gu64* flag_ptr(const Ctx& c, int line, int slot) { return (gu64*)(c.ws + WS_FLAG) + line * 32 + slot; }

DI void publish(const Ctx& c, int ctr, unsigned n, int line0, int slot) {
  asm volatile("s_waitcnt vmcnt(0)" ::: "memory");
  unsigned old = 0;
  if (c.lane == 0) old = __hip_atomic_fetch_add((LAS unsigned*)(c.lds + LD_CTR) + ctr, 1u, __ATOMIC_RELAXED, __HIP_MEMORY_SCOPE_WORKGROUP);
  if (__builtin_amdgcn_readfirstlane(old) == n - 1 && c.lane < 16) __hip_atomic_store(flag_ptr(c, line0 + c.lane, slot), c.token, RLX_AGENT);
}
DI void poll_any(Ctx& c, int line, int slot) {
  if (!c.dead) {
    gu64* p = flag_ptr(c, line, slot);
    unsigned spins = 0;
    while (!__all(__hip_atomic_load(p, RLX_AGENT) == c.token)) {
      if (++spins > (1u << 18)) { c.dead = true; break; }
    }
    __builtin_amdgcn_fence(__ATOMIC_ACQUIRE, "wavefront");
  }
}
template <bool NAP = false> DI void wait4(const Ctx& c, int ctr) {
  while (__hip_atomic_load((LAS unsigned*)(c.lds + LD_CTR) + ctr, __ATOMIC_RELAXED, __HIP_MEMORY_SCOPE_WORKGROUP) < 4u)
    if (NAP) __builtin_amdgcn_s_sleep(2);
  asm volatile("" ::: "memory");
}
DI void arrive4(const Ctx& c, int ctr) {
  asm volatile("s_waitcnt lgkmcnt(0)" ::: "memory");
  if (c.lane == 0) __hip_atomic_fetch_add((LAS unsigned*)(c.lds + LD_CTR) + ctr, 1u, __ATOMIC_RELAXED, __HIP_MEMORY_SCOPE_WORKGROUP);
}
DI void poll(Ctx& c, int line, int slot) { if (c.w == 0) poll_any(c, line, slot); }
template <int NQ> DI void help_dma(const Ctx& c, unsigned src, int dst) {
#pragma unroll
  for (int u = 0; u < NQ / 4; ++u) {
    const int p = (c.w & 3) + 4 * u;
    glds16x4(c.ws + src + p * 4096, c.voff, c.lbase + dst + p * 4096);
  }
}
DI int w2_run(int run) { return run < 6 ? LD_W + run * 14336 : LD_W2E + (run - 6) * 14336; }
template <int U0, int U1> DI void w2_fetch(const Ctx& c, u32x4 (&h)[28]) {
#pragma unroll
  for (int u = U0; u < U1; ++u) {
    const int f = (c.w & 3) + 4 * u, run = f / 14, kq = run >> 1;
    h[u] = ld16(c, c.voff, WS_W2 + ((2 * c.nb8 + (run & 1)) * KS_F + kq * 21 + (kq < 2 ? kq : 2) + f % 14) * 1024);
  }
  SBAR();
}
template <int U0, int U1> DI void w2_put(const Ctx& c, const u32x4 (&h)[28]) {
#pragma unroll
  for (int u = U0; u < U1; ++u) {
    const int f = (c.w & 3) + 4 * u;
    *(LAS u32x4*)(c.lds + (u < 21 ? LD_W : LD_W2E - 84 * 1024) + f * 1024 + c.voff) = h[u];
  }
}

DI f32x16 unpack16(const u32x4 (&res)[2]) {
  f32x16 v;
#pragma unroll
  for (int i = 0; i < 8; ++i) { const unsigned u = res[i >> 2][i & 3]; v[2 * i] = bf_lo(u); v[2 * i + 1] = bf_hi(u); }
  return v;
}
DI void pack16(const f32x16& v, u32x4 (&res)[2]) {
#pragma unroll
  for (int i = 0; i < 8; ++i) res[i >> 2][i & 3] = pk2(v[2 * i], v[2 * i + 1]);
}
DI void store_x(const Ctx& c, int inst, u32x4 (&res)[2], const f32x16& v) {
  const int tt = 2 * c.tb64 + (c.w >> 1), nt = 2 * c.nb8 + (c.w & 1);
  st16(c, res[0], c.voff, WS_X + inst * (2 * MB) + (tt * 32 + 2 * nt) * 1024);
  st16(c, res[1], c.voff, WS_X + inst * (2 * MB) + (tt * 32 + 2 * nt + 1) * 1024);
  SBAR();
  f32x2 a1 = {v[0], v[1]}, a2 = a1 * a1;
#pragma unroll
  for (int i = 1; i < 8; ++i) { const f32x2 t = {v[2 * i], v[2 * i + 1]}; a1 += t; a2 += t * t; }
  auto r1 = __builtin_amdgcn_permlane32_swap(__float_as_uint(a1[0] + a1[1]), __float_as_uint(a2[0] + a2[1]), false, false);
  st4(c, __uint_as_float(r1[0]) + __uint_as_float(r1[1]), c.lane * 4, WS_ST + inst * (64 * 4096) + tt * 4096 + nt * 256);
  PIN(res[0]); PIN(res[1]);
}

DI void wfrag_load(const unsigned short* W, int N, int nt, int ks, int r, int h, unsigned (&e)[8]) {
#pragma unroll
  for (int j = 0; j < 8; ++j) e[j] = W[(size_t)(16 * ks + 8 * (j >> 2) + 4 * h + (j & 3)) * N + 32 * nt + r];
}
DI void wfrag_store(const Ctx& c, const unsigned (&e)[8], unsigned dst, bool docs, unsigned csdst) {
  u32x4 v;
#pragma unroll
  for (int j = 0; j < 4; ++j) v[j] = e[2 * j] | (e[2 * j + 1] << 16);
  st16(c, v, c.voff, dst);
  if (docs) {
    float s = 0.f;
#pragma unroll
    for (int j = 0; j < 8; ++j) s += __uint_as_float(e[j] << 16);
    s = swap_half_sum(s);
    if (c.h == 0) st4(c, s, c.r * 4, csdst);
  }
}

DI u32x4 mish8(const f32x8& a, float rs) {
  float v[8];
#pragma unroll
  for (int j = 0; j < 8; ++j) {
    const float u = __builtin_amdgcn_exp2f(a[j] * (rs * 1.4426950408889634f));
    v[j] = a[j] * fmaf(-2.f * rs, __builtin_amdgcn_rcpf(fmaf(u, u + 2.f, 2.f)), rs);
  }
  return (u32x4){pk2(v[0], v[1]), pk2(v[2], v[3]), pk2(v[4], v[5]), pk2(v[6], v[7])};
}
#define LO8(s) __builtin_shufflevector(s, s, 0, 1, 2, 3, 4, 5, 6, 7)
#define HI8(s) __builtin_shufflevector(s, s, 8, 9, 10, 11, 12, 13, 14, 15)
DI f32x8 cat8(f32x4 a, f32x4 b) { return __builtin_shufflevector(a, b, 0, 1, 2, 3, 4, 5, 6, 7); }

template <int T, int PARTS = 3>
DI void ln_tile(const Ctx& c, int l, const f32x8& lo, const f32x8& hi, float rstd) {
  const int tt = 4 * c.tb + (c.w & 3);
  const unsigned so = WS_L + l * L_SZ + L_H + (tt * KS_F + 2 * (c.nb < 11 ? 3 * c.nb : 33 + 2 * (c.nb - 11)) + 2 * T) * 1024;
  if (PARTS & 1) st16(c, mish8(lo, rstd), c.voff, so);
  if (PARTS & 2) st16(c, mish8(hi, rstd), c.voff, so + 1024);
}
template <int MODE> DI void ln_publish(const Ctx& c, int l) {
  if (MODE == 0) publish(c, 4 + l, 4, FL_Q + l * 256 + c.nb * 16, c.tb);
  else publish(c, 6 + l, 8, FL_H + l * 256 + c.tb * 16, c.nb);
}

DI void pin16(f32x16& v) { PIN(v); }
DI void pinu(unsigned v) { asm volatile("" ::"v"(v)); }
DI u32x4 wfrag(const LAS char* base, int ks) { return *(const LAS u32x4*)(base + ks * 1024); }
DI void put16(LAS f32x4* ex, const f32x16& a) {
#pragma unroll
  for (int g = 0; g < 4; ++g) ex[g * 64] = (f32x4){a[4 * g], a[4 * g + 1], a[4 * g + 2], a[4 * g + 3]};
}

template <int MODE, bool FIRST>
DI void phase_ln(Ctx& c, int l, int inst, u32x4 (&w2r)[16]) {
  const int tt = 4 * c.tb + c.w;
  const unsigned xs = WS_X + inst * (2 * MB) + tt * 32768;
  constexpr int PD = 14;
  u32x4 xf[32], sp[4];
#pragma unroll
  for (int k = 0; k < PD; ++k) xf[k] = ld16(c, c.voff, xs + k * 1024);
  SBAR();
  const LAS char* wl = c.lds + LD_W + c.lane * 16;
  const LAS char* wl2 = wl + 65536;
  asm volatile("" : "+v"(wl2));
  f32x16 aA, aB;
#pragma unroll
  for (int i = 0; i < 16; ++i) { aA[i] = 0.f; aB[i] = 0.f; }
  u32x4 fa[3], fb[3];
  if (FIRST) BARRIER_LDS();
#pragma unroll
  for (int k = 0; k < 2; ++k) { fa[k] = wfrag(wl, k); fb[k] = wfrag(wl + 32768, k); }
  float rstd = 0.f, sc4[4];
  u32x2 we[3]; u32x4 xe;
  LAS float* sc = (LAS float*)(c.lds + LD_SC) + c.w * 256;
  const int k0q = c.w * 21 + (c.w < 2 ? c.w : 2);
  const unsigned w2s = WS_W2 + ((2 * c.nb8 + (c.w & 1)) * KS_F + k0q + 14) * 1024;
  const unsigned w2o = WS_W2 + ((2 * c.nb8 + 1 - (c.w & 1)) * KS_F + k0q + 14) * 1024;
  auto w2load = [&](int i) __attribute__((always_inline)) {
    w2r[i] = ld16(c, c.voff, ((i & 1) ? w2o : w2s) + (((i >> 1) == 7 && c.w >= 2) ? 6 : (i >> 1)) * 1024);
  };
  static_for<0, 32>([&](auto kc) __attribute__((always_inline)) {
    constexpr int k = decltype(kc)::v;
    if constexpr (FIRST && (((k & 7) == 6 && k < 24) || k == 31)) asm volatile("s_barrier" ::: "memory");
    aA = MFMA(as_bf(fa[k % 3]), as_bf(xf[k]), aA);
    pin16(aA); SBAR();
    if constexpr (k + 2 < 32) fa[(k + 2) % 3] = wfrag(wl, k + 2);
    if constexpr (k + PD < 32) xf[k + PD] = ld16(c, c.voff, xs + (k + PD) * 1024);
    if constexpr (k + PD >= 32 && k + PD < 36) sp[k + PD - 32] = ld16(c, c.voff, WS_ST + inst * (64 * 4096) + tt * 4096 + (k + PD - 32) * 1024);
    if constexpr (MODE == 1 && k >= 24) w2load(k - (24));
    SBAR();
    aB = MFMA(as_bf(fb[k % 3]), as_bf(xf[k]), aB);
    pin16(aB); SBAR();
    if constexpr (k + 2 < 32) fb[(k + 2) % 3] = wfrag(wl + 32768, k + 2);
    if constexpr (k == 29) {
      ((LAS f32x4*)sc)[c.lane] = (__builtin_bit_cast(f32x4, sp[0]) + __builtin_bit_cast(f32x4, sp[1])) +
                                 (__builtin_bit_cast(f32x4, sp[2]) + __builtin_bit_cast(f32x4, sp[3]));
#pragma unroll
      for (int i = 0; i < 4; ++i) sc4[i] = sc[i * 64 + c.lane];
    }
    if constexpr (k == (FIRST ? 31 : 30)) {
#pragma unroll
      for (int i = 0; i < 3; ++i) we[i] = ((const LAS u32x2*)(c.lds + LD_PAR))[MODE * 96 + 32 * i + c.r];
    }
    if constexpr (k == 31) {
      const float s = (sc4[0] + sc4[1]) + (sc4[2] + sc4[3]);
      auto rr = __builtin_amdgcn_permlane32_swap(__float_as_uint(s), __float_as_uint(s), false, false);
      const float mu = __uint_as_float(rr[0]) * (1.f / D_);
      const float var = fmaxf(__uint_as_float(rr[1]) * (1.f / D_) - mu * mu, 0.f);
      const float sd = rbf(__builtin_amdgcn_sqrtf(rbf(rbf(var) + 1e-5f)));
      rstd = __builtin_amdgcn_rcpf(sd);
      ((LAS float*)(c.lds + LD_ST))[c.w * 32 + c.r] = rstd;
      xe = (u32x4){c.h ? 0u : pk2(-mu, -mu), c.h ? 0u : pk2(-mu, sd), 0u, 0u};
    }
    SBAR();
  });
  aB = MFMA(as_bf((u32x4){we[1][0], we[1][1], 0u, 0u}), as_bf(xe), aB);
  aA = MFMA(as_bf((u32x4){we[0][0], we[0][1], 0u, 0u}), as_bf(xe), aA);
  f32x16 aC;
#pragma unroll
  for (int i = 0; i < 16; ++i) aC[i] = 0.f;
  aC = MODE == 0 ? MFMA(as_bf(xe), as_bf((u32x4){we[2][0], we[2][1], 0u, 0u}), aC) : MFMA(as_bf((u32x4){we[2][0], we[2][1], 0u, 0u}), as_bf(xe), aC);
  constexpr int CD = 5;
  u32x4 fc[CD + 1];
#pragma unroll
  for (int k = 0; k < CD; ++k) fc[k] = wfrag(wl2, k);
  SBAR();
  if (MODE == 1) {
    LAS f32x4* ex = (LAS f32x4*)(c.lds + LD_EX) + c.w * 384 + c.lane;
    put16(ex, aB);
    ex[256] = (f32x4){aA[8], aA[9], aA[10], aA[11]}; ex[320] = (f32x4){aA[12], aA[13], aA[14], aA[15]};
    BARRIER_LDS();
  }
  f32x4 ts[4];
  u32x4 pw;
  const float rq = rstd * (0.25f * 1.4426950408889634f);
  const unsigned vok = c.h * 512 + ((c.r & 19) | ((c.r & 4) << 1) | ((c.r & 8) >> 1)) * 16;
  const unsigned soq = WS_L + l * L_SZ + L_Q + (2 * c.nb * 64 + tt) * 1024;
  static_for<0, 32>([&](auto kc) __attribute__((always_inline)) {
    constexpr int k = decltype(kc)::v;
    aC = MODE == 0 ? MFMA(as_bf(xf[k]), as_bf(fc[k % (CD + 1)]), aC) : MFMA(as_bf(fc[k % (CD + 1)]), as_bf(xf[k]), aC);
    pin16(aC); SBAR();
    if constexpr (k + CD < 32) fc[(k + CD) % (CD + 1)] = wfrag(wl2, k + CD);
    if constexpr (MODE == 1 && (k & 1) == 0 && 8 + k / 2 < 16) w2load(8 + k / 2);
    if constexpr (MODE == 0 && k >= 6 && k <= 18 && (k & 3) == 2)
      st16(c, pw, k < 12 ? vok : c.voff, (k < 12 ? soq + (L_K - L_Q) : soq) + ((k & 4) ? 0 : 65536));
    if constexpr (MODE == 0 && k >= 2 && k < 18) {
      constexpr int i = k - 2;
      const f32x16& a = i < 8 ? aB : aA;
      const float rs = i < 8 ? rstd : rq;
      pw[i & 3] = pk2(a[2 * (i & 7)] * rs, a[2 * (i & 7) + 1] * rs);
      pinu(pw[i & 3]);
    }
    if constexpr (MODE == 0 && k == 29) {
#pragma unroll
      for (int g = 0; g < 4; ++g) ts[g] = *(const LAS f32x4*)((const LAS float*)(c.lds + LD_ST) + c.w * 32 + 8 * g + 4 * c.h);
    }
    SBAR();
  });
  if (MODE == 1) arrive4(c, 14 + l);
  if (MODE == 1) { ln_tile<0, 1>(c, l, LO8(aA), LO8(aA), rstd); if (c.nb < 11) ln_tile<2>(c, l, LO8(aC), HI8(aC), rstd); }
  else {
    unsigned q[8];
#pragma unroll
    for (int g = 0; g < 8; ++g) q[g] = pk2(aC[2 * g] * ts[g >> 1][2 * (g & 1)], aC[2 * g + 1] * ts[g >> 1][2 * (g & 1) + 1]);
    const unsigned vo = (c.r >> 4) * 65536 + c.h * 256 + (c.r & 15) * 16, so = WS_L + l * L_SZ + L_V + (2 * c.nb * 128 + 2 * tt) * 512;
    st16(c, (u32x4){q[0], q[1], q[2], q[3]}, vo, so);
    st16(c, (u32x4){q[4], q[5], q[6], q[7]}, vo, so + 512);
  }
  ln_publish<MODE>(c, l);
}
template <class F>
DI void help_ln(Ctx& c, int l, F&& mid) {
  BARRIER_LDS();
  const LAS f32x4* ex = (const LAS f32x4*)(c.lds + LD_EX) + (c.w & 3) * 384 + c.lane;
  const float rstd = ((const LAS float*)(c.lds + LD_ST))[(c.w & 3) * 32 + c.r];
  const f32x8 b0 = cat8(ex[0], ex[64]), b1 = cat8(ex[128], ex[192]), a1 = cat8(ex[256], ex[320]);
  SBAR();
  ln_tile<1>(c, l, b0, b1, rstd);
  ln_tile<0, 2>(c, l, a1, a1, rstd);
  SBAR();
  mid();
  ln_publish<1>(c, l);
}
DI void colsum_load(const Ctx& c, int mode, float (&v)[32]) {
  const int t = min((c.w - 4) * 64 + c.lane, 95);
#pragma unroll
  for (int i = 0; i < 32; ++i) v[i] = ld4(c, t * 4, WS_CSP + ((mode * 16 + c.nb) * 32 + i) * 384);
  SBAR();
}
DI void colsum_put(const Ctx& c, int mode, const float (&v)[32], unsigned bias) {
  const int t = (c.w - 4) * 64 + c.lane;
  float s = 0.f;
#pragma unroll
  for (int i = 0; i < 32; i += 4) s += (v[i] + v[i + 1]) + (v[i + 2] + v[i + 3]);
  const float s0 = rbf(s), s1 = rbf(s - s0);
  if (t < 96) ((LAS u32x2*)(c.lds + LD_PAR))[mode * 96 + t] = (u32x2){pk2(s0, s1), (pk2(s - s0 - s1, 0.f) & 0xffffu) | (bias << 16)};
}

DI f32x16 reduce_res(Ctx& c, const f32x16 (&a)[4], u32x4 (&res)[2], int ctr) {
  LAS f32x4* red = (LAS f32x4*)(c.lds + LD_EX) + c.lane;
#pragma unroll
  for (int j = 1; j < 4; ++j) {
    LAS f32x4* p = red + ((c.w ^ j) * 3 + j - 1) * 256;
#pragma unroll
    for (int g = 0; g < 4; ++g) p[g * 64] = (f32x4){a[j][4 * g], a[j][4 * g + 1], a[j][4 * g + 2], a[j][4 * g + 3]};
  }
  if (ctr >= 0) { arrive4(c, ctr); wait4(c, ctr); } else BARRIER_LDS();
  const LAS f32x4* q = red + c.w * 768;
  f32x16 v;
#pragma unroll
  for (int g = 0; g < 4; ++g) {
    const f32x4 s = ((f32x4){a[0][4 * g], a[0][4 * g + 1], a[0][4 * g + 2], a[0][4 * g + 3]} + q[g * 64]) + (q[256 + g * 64] + q[512 + g * 64]);
#pragma unroll
    for (int j = 0; j < 4; ++j) v[4 * g + j] = s[j];
  }
  pack16(v, res);
  return v;
}
#define MM4(w0, w1, x0, x1) do { a[0] = MFMA(as_bf(w0), as_bf(x0), a[0]); a[1] = MFMA(as_bf(w1), as_bf(x0), a[1]); \
                                 a[2] = MFMA(as_bf(w0), as_bf(x1), a[2]); a[3] = MFMA(as_bf(w1), as_bf(x1), a[3]); } while (0)

DI f32x16 phase_wo(Ctx& c, int l, const u32x4 (&wo)[16], u32x4 (&res)[2]) {
  const unsigned xs = WS_L + l * L_SZ + L_O + ((2 * c.tb64 + (c.w >> 1)) * 32 + 8 * c.w) * 1024;
  const unsigned xo = WS_L + l * L_SZ + L_O + ((2 * c.tb64 + 1 - (c.w >> 1)) * 32 + 8 * c.w) * 1024;
  u32x4 xf[16];
#pragma unroll
  for (int k = 0; k < 8; ++k) { xf[2 * k] = ld16(c, c.voff, xs + k * 1024); xf[2 * k + 1] = ld16(c, c.voff, xo + k * 1024); }
  asm volatile("s_barrier" ::: "memory");
  SBAR();
  f32x16 a[4];
#pragma unroll
  for (int i = 16; i < 64; ++i) a[i >> 4][i & 15] = 0.f;
  a[0] = unpack16(res);
#pragma unroll
  for (int k = 0; k < 8; ++k) { MM4(wo[2 * k], wo[2 * k + 1], xf[2 * k], xf[2 * k + 1]); PIN(a[0]); PIN(a[1]); PIN(a[2]); PIN(a[3]); SBAR(); }
  return reduce_res(c, a, res, -1);
}

DI f32x16 phase_w2(Ctx& c, int l, const u32x4 (&w2r)[16], u32x4 (&res)[2]) {
  const int k0 = c.w * 21 + (c.w < 2 ? c.w : 2);
  const unsigned xs = WS_L + l * L_SZ + L_H + ((2 * c.tb64 + (c.w >> 1)) * KS_F + k0) * 1024;
  const unsigned xo = WS_L + l * L_SZ + L_H + ((2 * c.tb64 + 1 - (c.w >> 1)) * KS_F + k0) * 1024;
  constexpr int WIN = 10;
  u32x4 xf[2 * WIN];
#pragma unroll
  for (int k = 0; k < WIN; ++k) { xf[2 * k] = ld16(c, c.voff, xs + k * 1024); xf[2 * k + 1] = ld16(c, c.voff, xo + k * 1024); }
  SBAR();
  const LAS u32x4* w0 = (const LAS u32x4*)(c.lds + w2_run(2 * c.w + (c.w & 1))) + c.lane;
  const LAS u32x4* w1 = (const LAS u32x4*)(c.lds + w2_run(2 * c.w + 1 - (c.w & 1))) + c.lane;
  f32x16 a[4];
#pragma unroll
  for (int i = 16; i < 64; ++i) a[i >> 4][i & 15] = 0.f;
  a[0] = unpack16(res);
  u32x4 f0[2], f1[2];
  f0[0] = w0[0]; f1[0] = w1[0];
  static_for<0, 22>([&](auto kc) __attribute__((always_inline)) {
    constexpr int k = decltype(kc)::v;
    const u32x4 g0 = k < 14 ? f0[k & 1] : w2r[k < 14 ? 0 : 2 * (k - 14)], g1 = k < 14 ? f1[k & 1] : w2r[k < 14 ? 0 : 2 * (k - 14) + 1];
    const u32x4 x0 = xf[2 * (k % WIN)], x1 = xf[2 * (k % WIN) + 1];
    constexpr int kn = k - 1 + WIN;
    const int kk = (kn == 21 && c.w >= 2) ? 20 : kn;
    if constexpr (kn == 22) asm volatile("s_barrier" ::: "memory");
    if (k < 21 || c.w < 2) {
      a[0] = MFMA(as_bf(g0), as_bf(x0), a[0]);
      pin16(a[0]); SBAR();
      if constexpr (k >= 1 && kn < 22) xf[2 * ((k - 1) % WIN)] = ld16(c, c.voff, xs + kk * 1024);
      SBAR();
      a[1] = MFMA(as_bf(g1), as_bf(x0), a[1]);
      pin16(a[1]); SBAR();
      if constexpr (k + 1 < 14) f0[(k + 1) & 1] = w0[(k + 1) * 64];
      SBAR();
      a[2] = MFMA(as_bf(g0), as_bf(x1), a[2]);
      pin16(a[2]); SBAR();
      if constexpr (k >= 1 && kn < 22) xf[2 * ((k - 1) % WIN) + 1] = ld16(c, c.voff, xo + kk * 1024);
      SBAR();
      a[3] = MFMA(as_bf(g1), as_bf(x1), a[3]);
      pin16(a[3]); SBAR();
      if constexpr (k + 1 < 14) f1[(k + 1) & 1] = w1[(k + 1) * 64];
      SBAR();
    }
  });
  if (l == 0) { arrive4(c, 16); wait4(c, 16); } else BARRIER_LDS();
  return reduce_res(c, a, res, l == 0 ? 17 : -1);
}

#define SMF(a, b8, c) __builtin_amdgcn_smfmac_f32_32x32x32_bf16((a), __builtin_bit_cast(bf16x16, b8), (c), sidx, 0, 0)
#define LDF(p) (*(const LAS bf16x8*)(p))
DI float max16(const f32x16& s) {
  float a = fmaxf(fmaxf(s[0], s[1]), s[2]), b = fmaxf(fmaxf(s[3], s[4]), s[5]);
  a = fmaxf(fmaxf(a, s[6]), s[7]); b = fmaxf(fmaxf(b, s[8]), s[9]);
  a = fmaxf(fmaxf(a, s[10]), s[11]); b = fmaxf(fmaxf(b, s[12]), s[13]);
  a = fmaxf(fmaxf(a, s[14]), s[15]);
  return swap_half_max(fmaxf(a, b));
}
DI void attn_slow(const LAS char* kp, const LAS char* vp, bool isv, bf16x8 q, f32x16& o, float& m) {
  const int sidx = 0xDDDDDDDD;
  f32x16 zero;
#pragma unroll
  for (int i = 0; i < 16; ++i) zero[i] = 0.f;
  o = zero; m = -3.0e38f;
  for (int t = 0; t < 32; ++t) {
    f32x16 s = MFMA(LDF(kp + t * 1024), q, zero);
    const float mn = fmaxf(m, max16(s));
    const float al = __builtin_amdgcn_exp2f(m - mn);
    m = mn;
#pragma unroll
    for (int i = 0; i < 16; ++i) { o[i] *= al; s[i] = __builtin_amdgcn_exp2f(s[i] - mn); }
    const int vo = isv ? t * 1024 : 0;
    o = SMF(LDF(vp + vo), LO8(s), o);
    o = SMF(LDF(vp + vo + 512), HI8(s), o);
  }
}

struct In { const unsigned short *x, *wqkv, *wo, *w1, *b1, *w2; unsigned short* out; };
struct Jobs { unsigned e[4][8], dst[4]; bool ok[4]; };
DI void jobs_load(const Ctx& c, const In& in, Jobs& jb) {
  const int t0 = c.nb < 11 ? 3 * c.nb : 33 + 2 * (c.nb - 11), nt1 = c.nb < 11 ? 3 : 2;
  const unsigned short* W[4]; int N[4];
#pragma unroll
  for (int u = 0; u < 4; ++u) {
    const int j = c.w + 4 * u;
    int nt, ks;
    if (j < 6) { W[u] = in.w1; N[u] = F_; jb.ok[u] = (j >> 1) < nt1; nt = t0 + (jb.ok[u] ? j >> 1 : 0); ks = 2 * c.tb + (j & 1); jb.dst[u] = WS_W1 + (nt * 32 + ks) * 1024; }
    else if (j < 8) { W[u] = in.wo; N[u] = D_; jb.ok[u] = true; nt = 2 * c.nb8 + (j & 1); ks = c.tb64; jb.dst[u] = WS_WO + (nt * 32 + ks) * 1024; }
    else { W[u] = in.w2; N[u] = D_; ks = c.tb64 + 32 * ((j - 8) >> 1); jb.ok[u] = ks < KS_F; if (!jb.ok[u]) ks = 0; nt = 2 * c.nb8 + (j & 1); jb.dst[u] = WS_W2 + (nt * KS_F + ks) * 1024; }
    W[u] += 16 * ks * N[u] + 32 * nt;
    asm volatile("" : "+s"(W[u]));
    asm volatile("" : "+s"(N[u]));
  }
#pragma unroll
  for (int u = 0; u < 4; ++u) {
    const __amdgpu_buffer_rsrc_t rw = __builtin_amdgcn_make_buffer_rsrc((void*)W[u], 0, 32 * N[u], 0x00020000);
    const unsigned o = (4 * c.h * N[u] + c.r) * 2;
#pragma unroll
    for (int j = 0; j < 8; ++j) jb.e[u][j] = __builtin_amdgcn_raw_buffer_load_b16(rw, o, (8 * (j >> 2) + (j & 3)) * 2 * N[u], 0);
  }
}
DI void jobs_store(const Ctx& c, Jobs& jb) {
#pragma unroll
  for (int i = 0; i < 32; ++i) PIN(jb.e[i >> 3][i & 7]);
#pragma unroll
  for (int u = 0; u < 4; ++u) {
    const int j = c.w + 4 * u;
    if (jb.ok[u]) wfrag_store(c, jb.e[u], jb.dst[u], j < 6, WS_CSP + ((16 + c.nb) * 32 + 2 * c.tb + (j & 1)) * 384 + (j >> 1) * 128);
  }
}
DI void jobs_publish(const Ctx& c) {
  asm volatile("s_waitcnt vmcnt(0)" ::: "memory");
  unsigned old = 0;
  if (c.lane == 0) old = __hip_atomic_fetch_add((LAS unsigned*)(c.lds + LD_CTR) + 2, 1u, __ATOMIC_RELAXED, __HIP_MEMORY_SCOPE_WORKGROUP);
  if (__builtin_amdgcn_readfirstlane(old) == 3 && c.lane < 2)
    __hip_atomic_store(c.lane ? flag_ptr(c, FL_W1 + c.nb, c.tb) : flag_ptr(c, FL_W2 + c.nb8, c.tb64), c.token, RLX_AGENT);
}

#define EX1(r) "v_exp_f32 v" #r ", v" #r "\n\t"
#define ASTEP(KOFF, VOFF, kc_, kn_, v0_, v1_, nv0_, pv1_)                                                                  \
  asm volatile("s_waitcnt lgkmcnt(0)\n\t" EX1(64) EX1(65)                                                                  \
               "v_smfmac_f32_32x32x32_bf16 %[o1], %[pv1], v[88:95], %[idx]\n\t"                                            \
               "ds_read_b128 %[nv0], %[vp] offset:%[vo]\n\tds_read_b128 %[pv1], %[vp] offset:%[vo1]\n\t"                   \
               EX1(66) EX1(67) EX1(68)                                                                                     \
               "v_mfma_f32_32x32x16_bf16 v[80:95], %[kc], %[q1], %[c1]\n\t"                                                \
               "ds_read_b128 %[kc], %[kp] offset:%[ko]\n\t"                                                                \
               EX1(69) EX1(70) EX1(71) EX1(72) EX1(73)                                                                     \
               "v_smfmac_f32_32x32x32_bf16 %[o0], %[v0], v[64:71], %[idx]\n\t"                                             \
               EX1(74) EX1(75) EX1(76) EX1(77) EX1(78) EX1(79) EX1(80) EX1(81)                                             \
               "v_smfmac_f32_32x32x32_bf16 %[o0], %[v1], v[72:79], %[idx]\n\t"                                             \
               EX1(82) EX1(83) EX1(84)                                                                                     \
               "v_mfma_f32_32x32x16_bf16 v[64:79], %[kn], %[q0], %[c0]\n\t"                                                \
               EX1(85) EX1(86) EX1(87) EX1(88) EX1(89)                                                                     \
               "v_smfmac_f32_32x32x32_bf16 %[o1], %[v0], v[80:87], %[idx]\n\t"                                             \
               EX1(90) EX1(91) EX1(92) EX1(93) EX1(94) "v_exp_f32 v95, v95"                                                \
               : "+{v[64:79]}"(sA), "+{v[80:95]}"(sB), [o0] "+v"(o0), [o1] "+v"(o1), [kc] "+v"(kc_), [nv0] "=&v"(nv0_),    \
                 [pv1] "+v"(pv1_)                                                                                          \
               : [kn] "v"(kn_), [v0] "v"(v0_), [v1] "v"(v1_), [q0] "v"(q0), [q1] "v"(q1), [c0] "v"(cin0), [c1] "v"(cin1),  \
                 [idx] "v"(sidx), [kp] "v"(kpa), [vp] "v"(vpa), [ko] "n"(KOFF), [vo] "n"(VOFF), [vo1] "n"((VOFF) + 512)     \
               : "memory")

template <int half, int l>
DI void phase_attn(Ctx& c, const In& in, u32x4 (&wo)[16], u32x4 (&hw)[28]) {
  const int qt = c.qb * 8 + (c.w & 3) * 2;
  const unsigned lay = WS_L + l * L_SZ;
  const char* kvg = c.ws + lay + ((c.w & 1) ? L_V : L_K) + c.head * 65536 + ((c.w >> 1) & 1) * 32768 + half * 4096;
  const unsigned kvd = c.lbase + ((c.w & 1) ? LD_V : LD_W) + ((c.w >> 1) & 1) * 32768 + half * 4096;
  u32x4 qv0, qv1;
  asm volatile("s_nop 4\n\tglobal_load_dwordx4 %0, %2, %3 sc1\n\tglobal_load_dwordx4 %1, %2, %3 offset:1024 sc1"
               : "=&v"(qv0), "=&v"(qv1) : "v"(c.voff), "s"(c.ws + lay + L_Q + (c.head * 64 + qt) * 1024) : "memory");
#define KV4(u) glds16x4(kvg + (u) * 8192, c.voff, kvd + (u) * 8192)
  glds16x4(kvg, c.voff, kvd);
  if (half == 0) {
    const unsigned o1 = (c.lane & 11) == 0 ? 0x3f803f80u : 0u;
    ((LAS u32x4*)(c.lds + LD_ONE))[c.w * 64 + c.lane] = (u32x4){o1, o1, o1, o1};
  }
  int sidx = 0xDDDDDDDD;
  const bool isv = c.r < 16;
  const LAS char* kp0 = c.lds + LD_W + half * 32768 + c.lane * 16;
  const LAS char* vp0 = c.lds + (isv ? LD_V + half * 32768 : LD_ONE) + c.h * 256 + (c.r & 15) * 16;
  const int vstep = isv ? 4096 : 0;
  if (half == 0) asm volatile("s_waitcnt vmcnt(0) lgkmcnt(0)\n\ts_barrier" : "+v"(qv0), "+v"(qv1)::"memory");
  else asm volatile("s_waitcnt vmcnt(4)\n\ts_barrier" : "+v"(qv0), "+v"(qv1)::"memory");
  if (half == 0) __builtin_amdgcn_s_setprio(1);
  const bf16x8 q0 = as_bf(qv0), q1 = as_bf(qv1);
  f32x16 zero;
#pragma unroll
  for (int i = 0; i < 16; ++i) zero[i] = 0.f;
  f32x16 cin0, cin1, o0 = zero, o1 = zero, sA, sB = zero;
  float m0, m1;
  bf16x8 ka = LDF(kp0), kb = LDF(kp0 + 1024), va0 = LDF(vp0), va1 = LDF(vp0 + 512), vb0, vb1 = as_bf((u32x4){0u, 0u, 0u, 0u});
  {
    const f32x16 s = MFMA(ka, q0, zero), s1 = MFMA(ka, q1, zero);
    m0 = max16(s); m1 = max16(s1);
#pragma unroll
    for (int i = 0; i < 16; ++i) { cin0[i] = -m0; cin1[i] = -m1; sA[i] = s[i] - m0; }
  }
  unsigned kpa = (unsigned)(size_t)kp0, vpa = (unsigned)(size_t)vp0;
  asm volatile("s_nop 1" : "+v"(cin0), "+v"(cin1), "+v"(sA), "+v"(sB), "+v"(o0), "+v"(o1), "+v"(sidx), "+v"(vb1));
#define QUAD(A, B, MID, C, D)                                                        \
  do {                                                                               \
    ASTEP(2048, 1024, ka, kb, va0, va1, vb0, vb1); A;                                \
    ASTEP(3072, 2048, kb, ka, vb0, vb1, va0, va1); B; MID;                           \
    ASTEP(4096, 3072, ka, kb, va0, va1, vb0, vb1); C;                                \
    vpa += vstep;                                                                    \
    ASTEP(5120, 0, kb, ka, vb0, vb1, va0, va1); D;                                   \
    kpa += 4096;                                                                      \
  } while (0)
#define NOP (void)0
#define WBAR(n) asm volatile("s_waitcnt vmcnt(" #n ")\n\ts_barrier" ::: "memory")
  constexpr bool JOBS = half == 0 && l == 0;
  Jobs jb;
  KV4(1);
  QUAD(KV4(2), NOP, WBAR(8), KV4(3), if (JOBS) { jobs_load(c, in, jb); SBAR(); });
  QUAD(NOP, NOP, if (JOBS) WBAR(32); else WBAR(0); if (half == 1 && l == 1) (w2_fetch<0, 21>(c, hw)), NOP,
       if (JOBS) { jobs_store(c, jb); SBAR(); });
  QUAD(if (JOBS) jobs_publish(c), NOP, NOP,
       if (half == 1 && l == 0) { poll_any(c, FL_W2 + c.nb8, c.lane & 31); (w2_fetch<0, 21>(c, hw)); }, NOP);
  for (int it = 3; it < 8; ++it) QUAD(NOP, NOP, NOP, NOP, NOP);
#undef WBAR
  asm volatile("s_nop 1\n\tv_smfmac_f32_32x32x32_bf16 %[o1], %[pv1], v[88:95], %[idx]\n\ts_waitcnt lgkmcnt(0)\n\ts_nop 7\n\ts_nop 7"
               : [o1] "+v"(o1), "+v"(o0), "+v"(ka), "+v"(kb), "+v"(va0), "+v"(va1) : [pv1] "v"(vb1), "{v[80:95]}"(sB), [idx] "v"(sidx));
#undef QUAD
#undef KV4
  if (__any(!(o0[8] < 1.0995116e12f))) attn_slow(kp0, vp0, isv, q0, o0, m0);
  if (__any(!(o1[8] < 1.0995116e12f))) attn_slow(kp0, vp0, isv, q1, o1, m1);
  LAS f32x4* cmb = (LAS f32x4*)(c.lds + LD_CMB) + (c.w & 3) * 320 + c.lane;
  const unsigned w1src = WS_W1 + (c.nb < 11 ? 3 * c.nb : 33 + 2 * (c.nb - 11)) * 32768;
  if (half == 0) {
    __builtin_amdgcn_s_setprio(0);
    cmb[0] = (f32x4){o0[0], o0[1], o0[2], o0[3]}; cmb[64] = (f32x4){o0[4], o0[5], o0[6], o0[7]};
    cmb[128] = (f32x4){o1[0], o1[1], o1[2], o1[3]}; cmb[192] = (f32x4){o1[4], o1[5], o1[6], o1[7]};
    cmb[256] = (f32x4){o0[8], m0, o1[8], m1};
    arrive4(c, 12 + l);
    if (l == 0) poll_any(c, c.lane < 32 ? FL_W1 + c.nb : FL_W2 + c.nb8, c.lane < 32 ? (c.lane & 15) : (c.lane & 31));
    const unsigned wos = WS_WO + ((2 * c.nb8 + (c.w & 1)) * 32 + 8 * c.w) * 1024;
    const unsigned woo = WS_WO + ((2 * c.nb8 + 1 - (c.w & 1)) * 32 + 8 * c.w) * 1024;
#pragma unroll
    for (int k = 0; k < 8; ++k) { wo[2 * k] = ld16(c, c.voff, wos + k * 1024); wo[2 * k + 1] = ld16(c, c.voff, woo + k * 1024); }
    SBAR();
    wait4<true>(c, 12 + l);
    help_dma<8>(c, w1src, LD_W);
    if (c.nb < 11) help_dma<8>(c, w1src + 65536, LD_W + 65536);
  }
  BARRIER_LDS();
  if (half == 1) {
    const f32x4 lm = cmb[256];
#pragma unroll
    for (int u = 0; u < 2; ++u) {
      const f32x16& o = u ? o1 : o0; const float m = u ? m1 : m0;
      const f32x8 p2 = cat8(cmb[128 * u], cmb[128 * u + 64]);
      const float m2 = lm[2 * u + 1];
      const float mm = fmaxf(m, m2);
      const float f1 = __builtin_amdgcn_exp2f(m - mm), f2 = __builtin_amdgcn_exp2f(m2 - mm);
      const float inv = __builtin_amdgcn_rcpf(o[8] * f1 + lm[2 * u] * f2);
      const float g1 = f1 * inv, g2 = f2 * inv;
      unsigned p[4];
#pragma unroll
      for (int i = 0; i < 4; ++i) p[i] = pk2(o[2 * i] * g1 + p2[2 * i] * g2, o[2 * i + 1] * g1 + p2[2 * i + 1] * g2);
      st16(c, (u32x4){p[0], p[1], p[2], p[3]}, c.voff, lay + L_O + ((qt + u) * 32 + c.head) * 1024);
    }
    asm volatile("s_waitcnt vmcnt(0)" ::: "memory");
    if (c.lane < 8) __hip_atomic_store(flag_ptr(c, FL_O + l * 256 + (c.qb * 4 + (c.w & 3)) * 8 + c.lane, c.head), c.token, RLX_AGENT);
  }
}

DI u64 uniform64(u64 v) {
  return ((u64)__builtin_amdgcn_readfirstlane((unsigned)(v >> 32)) << 32) | (unsigned)__builtin_amdgcn_readfirstlane((unsigned)v);
}

DI void run_main(Ctx& c, const In& in) {
  const u64 epoch = __hip_atomic_load((gu64*)c.ws, RLX_AGENT);
  u32x4 res[2];
  {
    if (c.w == 0 && c.lane < 32) ((LAS unsigned*)(c.lds + LD_CTR))[c.lane] = 0u;
    const unsigned short* src = in.x + (size_t)((2 * c.tb64 + (c.w >> 1)) * 32 + c.r) * D_ + 32 * (2 * c.nb8 + (c.w & 1)) + 4 * c.h;
    u32x2 xr[4];
#pragma unroll
    for (int i = 0; i < 4; ++i) xr[i] = *(const u32x2*)(src + 8 * i);
    BARRIER_LDS();
    c.token = uniform64(epoch) + 1;
    res[0] = (u32x4){xr[0][0], xr[0][1], xr[1][0], xr[1][1]}; res[1] = (u32x4){xr[2][0], xr[2][1], xr[3][0], xr[3][1]};
    store_x(c, 0, res, unpack16(res));
    publish(c, 0, 4, FL_X + c.tb * 16, c.nb);
    poll(c, (c.lane & 16) ? FL_WQ + c.nb * 16 + c.tb : FL_X + c.tb * 16 + c.nb, c.lane & 15);
    BARRIER_LDS();
    u32x4 dummy[16];
    fresh(c);
    phase_ln<0, true>(c, 0, 0, dummy);
  }
  for (int l = 0; l < 2; ++l) {
    u32x4 wo[16], w2r[16];
    if (l == 1) {
      fresh(c);
      u32x4 dummy[16];
      phase_ln<0, false>(c, l, 2 * l, dummy);
    }
    fresh(c);
    poll(c, FL_Q + l * 256 + c.head * 8 + c.qb, c.lane & 15);
    BARRIER_LDS();
    { u32x4 unused[28]; if (l == 0) phase_attn<0, 0>(c, in, wo, unused); else phase_attn<0, 1>(c, in, wo, unused); }
    fresh(c);
    poll(c, FL_O + l * 256 + c.tb64 * 8 + c.nb8, c.lane & 31);
    BARRIER_LDS();
    store_x(c, 2 * l + 1, res, phase_wo(c, l, wo, res));
    publish(c, 8 + l, 4, FL_X + (2 * l + 1) * 256 + c.tb * 16, c.nb);
    fresh(c);
    poll(c, FL_X + (2 * l + 1) * 256 + c.tb * 16 + c.nb, c.lane & 15);
    BARRIER_ALL();
    phase_ln<1, false>(c, l, 2 * l + 1, w2r);
    fresh(c);
    poll(c, FL_H + l * 256 + c.tb * 16 + c.nb, c.lane & 15);
    BARRIER_LDS();
    const f32x16 x2 = phase_w2(c, l, w2r, res);
    fresh(c);
    if (l == 0) {
      store_x(c, 2, res, x2);
      publish(c, 10, 4, FL_X + 2 * 256 + c.tb * 16, c.nb);
      poll(c, FL_X + 2 * 256 + c.tb * 16 + c.nb, c.lane & 15);
      BARRIER_LDS();
    }
  }
  {
    auto s0 = __builtin_amdgcn_permlane32_swap(res[0][0], res[0][2], false, false), s1 = __builtin_amdgcn_permlane32_swap(res[0][1], res[0][3], false, false);
    auto s2 = __builtin_amdgcn_permlane32_swap(res[1][0], res[1][2], false, false), s3 = __builtin_amdgcn_permlane32_swap(res[1][1], res[1][3], false, false);
    unsigned short* op = in.out + (size_t)((2 * c.tb64 + (c.w >> 1)) * 32 + c.r) * D_ + 32 * (2 * c.nb8 + (c.w & 1)) + 8 * c.h;
    __builtin_nontemporal_store((u32x4){s0[0], s1[0], s0[1], s1[1]}, (u32x4*)op);
    __builtin_nontemporal_store((u32x4){s2[0], s3[0], s2[1], s3[1]}, (u32x4*)(op + 16));
  }
  if (c.bid == 0 && c.w == 0 && c.lane == 0) __hip_atomic_store((gu64*)c.ws, c.token, RLX_AGENT);
}

DI void run_help(Ctx& c, const In& in) {
  const u64 epoch = __hip_atomic_load((gu64*)c.ws, RLX_AGENT);
  {
    unsigned e0[8], e1[8];
    const int qk = 2 * c.tb + (c.w & 1), qa = (c.w >> 1) & 1;
    wfrag_load(in.wqkv, 3 * D_, qa * 16 + c.nb, qk, c.r, c.h, e0);
    wfrag_load(in.wqkv, 3 * D_, 32 + c.nb, qk, c.r, c.h, e1);
    BARRIER_LDS();
    c.token = uniform64(epoch) + 1;
    wfrag_store(c, e0, WS_WQKV + ((qa * 16 + c.nb) * 32 + qk) * 1024, true, WS_CSP + (c.nb * 32 + qk) * 384 + qa * 128);
    if (qa == 0) wfrag_store(c, e1, WS_WQKV + ((32 + c.nb) * 32 + qk) * 1024, true, WS_CSP + (c.nb * 32 + qk) * 384 + 256);
    publish(c, 1, 4, FL_WQ + c.nb * 16, c.tb);
    BARRIER_LDS();
    fresh(c);
#define WQC(q) _Pragma("unroll") for (int t_ = 0; t_ < 3; ++t_) \
    glds16x2(c.ws + WS_WQKV + ((16 * t_ + c.nb) * 32 + 8 * (q) + 2 * (c.w & 3)) * 1024, c.voff, c.lbase + LD_W + t_ * 32768 + (8 * (q) + 2 * (c.w & 3)) * 1024)
    WQC(0); WQC(1);
    asm volatile("s_waitcnt vmcnt(6)\n\ts_barrier" ::: "memory");
    WQC(2);
    asm volatile("s_waitcnt vmcnt(6)\n\ts_barrier" ::: "memory");
    WQC(3);
    asm volatile("s_waitcnt vmcnt(6)\n\ts_barrier" ::: "memory");
    if (c.w < 6) {
      float cv[32];
      colsum_load(c, 0, cv);
      asm volatile("s_waitcnt vmcnt(32)\n\ts_barrier" ::: "memory");
      colsum_put(c, 0, cv, 0u);
    } else {
      asm volatile("s_waitcnt vmcnt(0)\n\ts_barrier" ::: "memory");
    }
    BARRIER_LDS();
#undef WQC
  }
  for (int l = 0; l < 2; ++l) {
    u32x4 wo[16];
    fresh(c);
    BARRIER_LDS();
    fresh(c);
    u32x4 hw[28];
    if (l == 0) phase_attn<1, 0>(c, in, wo, hw); else phase_attn<1, 1>(c, in, wo, hw);
    fresh(c);
    BARRIER_LDS();
    asm volatile("s_barrier" ::: "memory");
    help_dma<8>(c, WS_W1 + (c.nb < 11 ? 3 * c.nb + 1 : 34 + 2 * (c.nb - 11)) * 32768, LD_W + 32768);
    w2_fetch<21, 28>(c, hw);
    asm volatile("s_barrier" ::: "memory");
    if (l == 0 && c.w < 6) {
      const int bcol = 32 * (c.nb < 11 ? 3 * c.nb : 33 + 2 * (c.nb - 11)) + (c.w - 4) * 64 + c.lane;
      const unsigned bv = in.b1[bcol < F_ ? bcol : 0];
      float cv[32];
      colsum_load(c, 1, cv);
      colsum_put(c, 1, cv, bv);
    }
    BARRIER_ALL();
    w2_put<21, 28>(c, hw);
    help_ln(c, l, [&]() __attribute__((always_inline)) { wait4<true>(c, 14 + l); w2_put<0, 12>(c, hw); });
    w2_put<12, 21>(c, hw);
    BARRIER_LDS();
    asm volatile("s_barrier" ::: "memory");
    if (l == 0) {
      fresh(c);
#pragma unroll
      for (int u = 0; u < 24; ++u) hw[u] = ld16(c, c.voff, WS_WQKV + (((u >> 3) * 16 + c.nb) * 32 + (u & 7) * 4 + (c.w & 3)) * 1024);
      SBAR();
      wait4<true>(c, 16);
#pragma unroll
      for (int u = 0; u < 24; ++u) *(LAS u32x4*)(c.lds + LD_W + (u * 4 + (c.w & 3)) * 1024 + c.voff) = hw[u];
      BARRIER_LDS();
    } else {
      asm volatile("s_barrier\n\ts_barrier" ::: "memory");
    }
  }
}

__global__ __launch_bounds__(512, 2) void mega(const unsigned short* __restrict__ x, const unsigned short* __restrict__ wqkv,
                                               const unsigned short* __restrict__ wo_g, const unsigned short* __restrict__ w1,
                                               const unsigned short* __restrict__ b1, const unsigned short* __restrict__ w2,
                                               char* __restrict__ ws, unsigned short* __restrict__ out) {
  extern __shared__ __attribute__((aligned(16))) char lds_[];
  Ctx c;
  c.ws = ws; c.lds = (LAS char*)lds_; c.rs = __builtin_amdgcn_make_buffer_rsrc(ws, 0, WS_END, 0x00020000);
  c.lbase = (unsigned)(size_t)c.lds;
  c.bid = blockIdx.x; c.lane = threadIdx.x & 63; c.w = __builtin_amdgcn_readfirstlane(threadIdx.x >> 6); c.dead = false;
  fresh(c);
  const In in = {x, wqkv, wo_g, w1, b1, w2, out};
  if (c.w < 4) run_main(c, in); else run_help(c, in);
}

extern "C" void kernel_launch(void* const* d_in, const int* in_sizes, int n_in, void* d_out, int out_size, void* d_ws,
                              size_t ws_size, hipStream_t stream) {
  (void)hipFuncSetAttribute((const void*)mega, hipFuncAttributeMaxDynamicSharedMemorySize, LDS_BYTES);
  mega<<<256, 512, LDS_BYTES, stream>>>((const unsigned short*)d_in[0], (const unsigned short*)d_in[1], (const unsigned short*)d_in[2],
                                        (const unsigned short*)d_in[4], (const unsigned short*)d_in[5], (const unsigned short*)d_in[6],
                                        (char*)d_ws, (unsigned short*)d_out);
}
```

```cpp
#include <hip/hip_runtime.h>
#include <hip/hip_bf16.h>

typedef __attribute__((ext_vector_type(2))) __bf16 bf16x2;
typedef __attribute__((ext_vector_type(8))) __bf16 bf16x8;
typedef __attribute__((ext_vector_type(16))) __bf16 bf16x16;
typedef __attribute__((ext_vector_type(16))) float f32x16;
typedef __attribute__((ext_vector_type(8))) float f32x8;
typedef __attribute__((ext_vector_type(4))) float f32x4;
typedef __attribute__((ext_vector_type(2))) float f32x2;
typedef __attribute__((ext_vector_type(4))) unsigned u32x4;
typedef __attribute__((ext_vector_type(2))) unsigned u32x2;
typedef unsigned long long u64;
#define LAS __attribute__((address_space(3)))
#define GAS __attribute__((address_space(1)))
typedef GAS u64 gu64;
#define DI __device__ __forceinline__
#define RLX_AGENT __ATOMIC_RELAXED, __HIP_MEMORY_SCOPE_AGENT

constexpr int D_ = 512, F_ = 1376;
constexpr int KS_F = F_ / 16;

constexpr unsigned MB = 1u << 20;
constexpr unsigned WS_FLAG = 0;
constexpr unsigned WS_WQKV = 1024 * 1024;
constexpr unsigned WS_WO = WS_WQKV + 48 * 32 * 1024;
constexpr unsigned WS_W1 = WS_WO + 16 * 32 * 1024;
constexpr unsigned WS_W2 = WS_W1 + 43 * 32 * 1024;
constexpr unsigned WS_CSP = WS_W2 + 16 * 86 * 1024;
constexpr unsigned WS_ST = WS_CSP + 2 * 16 * 32 * 384;
constexpr unsigned WS_X = WS_ST + 4 * 64 * 4096;
constexpr unsigned WS_L = WS_X + 8 * MB;
constexpr unsigned L_Q = 0, L_K = 2 * MB, L_V = 4 * MB, L_O = 6 * MB, L_H = 8 * MB, L_SZ = 14 * MB;
constexpr unsigned WS_END = WS_L + 2 * L_SZ;
constexpr int FL_X = 1;
constexpr int FL_H = FL_X + 1024;
constexpr int FL_Q = FL_H + 512;
constexpr int FL_O = FL_Q + 512;
constexpr int FL_WQ = FL_O + 512;
constexpr int FL_W1 = FL_WQ + 256;
constexpr int FL_W2 = FL_W1 + 16;
static_assert((FL_W2 + 8) * 256 <= WS_WQKV, "flag region");

constexpr int LD_W = 0;
constexpr int LD_V = 65536;
constexpr int LD_EX = 98304;
constexpr int LD_ONE = 131072;
constexpr int LD_CMB = 135168;
constexpr int LD_W2E = 122880;
constexpr int LD_SC = 151552;
constexpr int LD_ST = 155648;
constexpr int LD_PAR = 157696;
constexpr int LD_CTR = LD_PAR + 1536;
constexpr int LDS_BYTES = 163840;

#define MFMA(a, b, c) __builtin_amdgcn_mfma_f32_32x32x16_bf16((a), (b), (c), 0, 0, 0)
#define SBAR() __builtin_amdgcn_sched_barrier(0)
#define PIN(x) asm volatile("" : "+v"(x))
#define BARRIER_LDS() asm volatile("s_waitcnt lgkmcnt(0)\n\ts_barrier" ::: "memory")
#define BARRIER_ALL() asm volatile("s_waitcnt vmcnt(0) lgkmcnt(0)\n\ts_barrier" ::: "memory")

DI unsigned pk2(float lo, float hi) {
  bf16x2 v; v[0] = (__bf16)lo; v[1] = (__bf16)hi;
  return __builtin_bit_cast(unsigned, v);
}
DI float bf_lo(unsigned u) { return __uint_as_float(u << 16); }
DI float bf_hi(unsigned u) { return __uint_as_float(u & 0xffff0000u); }
DI float rbf(float x) { return (float)(__bf16)x; }
DI bf16x8 as_bf(u32x4 v) { return __builtin_bit_cast(bf16x8, v); }
DI float swap_half_sum(float x) {
  auto r = __builtin_amdgcn_permlane32_swap(__float_as_uint(x), __float_as_uint(x), false, false);
  return __uint_as_float(r[0]) + __uint_as_float(r[1]);
}
DI float swap_half_max(float x) {
  auto r = __builtin_amdgcn_permlane32_swap(__float_as_uint(x), __float_as_uint(x), false, false);
  return fmaxf(__uint_as_float(r[0]), __uint_as_float(r[1]));
}
DI void glds16x2(const char* sbase, unsigned voff, unsigned lds_base) {
  unsigned sv;
  asm volatile("s_mov_b32 %0, m0\n\ts_mov_b32 m0, %3\n\ts_nop 2\n\tglobal_load_lds_dwordx4 %1, %2 sc1\n\t"
               "global_load_lds_dwordx4 %1, %2 offset:1024 sc1\n\ts_mov_b32 m0, %0"
               : "=&s"(sv) : "v"(voff), "s"(sbase), "s"(lds_base) : "memory");
}
DI void glds16x4(const char* sbase, unsigned voff, unsigned lds_base) {
  unsigned sv;
  asm volatile("s_mov_b32 %0, m0\n\ts_mov_b32 m0, %3\n\ts_nop 2\n\tglobal_load_lds_dwordx4 %1, %2 sc1\n\t"
               "global_load_lds_dwordx4 %1, %2 offset:1024 sc1\n\tglobal_load_lds_dwordx4 %1, %2 offset:2048 sc1\n\t"
               "global_load_lds_dwordx4 %1, %2 offset:3072 sc1\n\ts_mov_b32 m0, %0"
               : "=&s"(sv) : "v"(voff), "s"(sbase), "s"(lds_base) : "memory");
}
template <int I> struct IC { static constexpr int v = I; };
template <int I, int N, class F> DI void static_for(F&& f) {
  if constexpr (I < N) { f(IC<I>{}); static_for<I + 1, N>(f); }
}

struct Ctx {
  char* ws; LAS char* lds; __amdgpu_buffer_rsrc_t rs;
  u64 token; unsigned lbase, voff;
  int bid, lane, w, r, h, tb, nb, tb64, nb8, head, qb;
  bool dead;
};
DI void fresh(Ctx& c) {
  asm volatile("" : "+s"(c.bid));
  asm volatile("" : "+s"(c.w));
  asm volatile("" : "+v"(c.lane));
  const int g = c.bid & 7, idx = c.bid >> 3;
  c.r = c.lane & 31; c.h = c.lane >> 5; c.voff = c.lane * 16;
  c.tb = 2 * g + (idx >> 4); c.nb = idx & 15; c.tb64 = 4 * g + (idx >> 3); c.nb8 = idx & 7;
  c.head = 4 * g + (idx & 3); c.qb = idx >> 2;
}
DI u32x4 ld16(const Ctx& c, unsigned voff, unsigned soff) { return __builtin_amdgcn_raw_buffer_load_b128(c.rs, voff, soff, 16); }
DI float ld4(const Ctx& c, unsigned voff, unsigned soff) { return __uint_as_float(__builtin_amdgcn_raw_buffer_load_b32(c.rs, voff, soff, 16)); }
DI void st16(const Ctx& c, u32x4 v, unsigned voff, unsigned soff) { __builtin_amdgcn_raw_buffer_store_b128(v, c.rs, voff + soff, 0, 16); }
DI void st4(const Ctx& c, float v, unsigned voff, unsigned soff) { __builtin_amdgcn_raw_buffer_store_b32(__float_as_uint(v), c.rs, voff, soff, 16); }
DI gu64* flag_ptr(const Ctx& c, int line, int slot) { return (gu64*)(c.ws + WS_FLAG) + line * 32 + slot; }

DI void publish(const Ctx& c, int ctr, unsigned n, int line0, int slot) {
  asm volatile("s_waitcnt vmcnt(0)" ::: "memory");
  unsigned old = 0;
  if (c.lane == 0) old = __hip_atomic_fetch_add((LAS unsigned*)(c.lds + LD_CTR) + ctr, 1u, __ATOMIC_RELAXED, __HIP_MEMORY_SCOPE_WORKGROUP);
  if (__builtin_amdgcn_readfirstlane(old) == n - 1 && c.lane < 16) __hip_atomic_store(flag_ptr(c, line0 + c.lane, slot), c.token, RLX_AGENT);
}
DI void poll_any(Ctx& c, int line, int slot) {
  if (!c.dead) {
    gu64* p = flag_ptr(c, line, slot);
    unsigned spins = 0;
    while (!__all(__hip_atomic_load(p, RLX_AGENT) == c.token)) {
      if (++spins > (1u << 18)) { c.dead = true; break; }
    }
    __builtin_amdgcn_fence(__ATOMIC_ACQUIRE, "wavefront");
  }
}
template <bool NAP = false> DI void wait4(const Ctx& c, int ctr) {
  while (__hip_atomic_load((LAS unsigned*)(c.lds + LD_CTR) + ctr, __ATOMIC_RELAXED, __HIP_MEMORY_SCOPE_WORKGROUP) < 4u)
    if (NAP) __builtin_amdgcn_s_sleep(2);
  asm volatile("" ::: "memory");
}
DI void arrive4(const Ctx& c, int ctr) {
  asm volatile("s_waitcnt lgkmcnt(0)" ::: "memory");
  if (c.lane == 0) __hip_atomic_fetch_add((LAS unsigned*)(c.lds + LD_CTR) + ctr, 1u, __ATOMIC_RELAXED, __HIP_MEMORY_SCOPE_WORKGROUP);
}
template <int NAP> DI void poll(Ctx& c, int line, int slot) {
  if (c.w == 0) {
    __builtin_amdgcn_s_sleep(NAP);
    poll_any(c, line, slot);
  }
}
template <int NQ> DI void help_dma(const Ctx& c, unsigned src, int dst) {
#pragma unroll
  for (int u = 0; u < NQ / 4; ++u) {
    const int p = (c.w & 3) + 4 * u;
    glds16x4(c.ws + src + p * 4096, c.voff, c.lbase + dst + p * 4096);
  }
}
DI int w2_run(int run) { return run < 6 ? LD_W + run * 14336 : LD_W2E + (run - 6) * 14336; }
template <int U0, int U1> DI void w2_fetch(const Ctx& c, u32x4 (&h)[28]) {
#pragma unroll
  for (int u = U0; u < U1; ++u) {
    const int f = (c.w & 3) + 4 * u, run = f / 14, kq = run >> 1;
    h[u] = ld16(c, c.voff, WS_W2 + ((2 * c.nb8 + (run & 1)) * KS_F + kq * 21 + (kq < 2 ? kq : 2) + f % 14) * 1024);
  }
  SBAR();
}
template <int U0, int U1> DI void w2_put(const Ctx& c, const u32x4 (&h)[28]) {
#pragma unroll
  for (int u = U0; u < U1; ++u) {
    const int f = (c.w & 3) + 4 * u;
    *(LAS u32x4*)(c.lds + (u < 21 ? LD_W : LD_W2E - 84 * 1024) + f * 1024 + c.voff) = h[u];
  }
}

DI f32x16 unpack16(const u32x4 (&res)[2]) {
  f32x16 v;
#pragma unroll
  for (int i = 0; i < 8; ++i) { const unsigned u = res[i >> 2][i & 3]; v[2 * i] = bf_lo(u); v[2 * i + 1] = bf_hi(u); }
  return v;
}
DI void pack16(const f32x16& v, u32x4 (&res)[2]) {
#pragma unroll
  for (int i = 0; i < 8; ++i) res[i >> 2][i & 3] = pk2(v[2 * i], v[2 * i + 1]);
}
DI void store_x(const Ctx& c, int inst, u32x4 (&res)[2], const f32x16& v) {
  const int tt = 2 * c.tb64 + (c.w >> 1), nt = 2 * c.nb8 + (c.w & 1);
  st16(c, res[0], c.voff, WS_X + inst * (2 * MB) + (tt * 32 + 2 * nt) * 1024);
  st16(c, res[1], c.voff, WS_X + inst * (2 * MB) + (tt * 32 + 2 * nt + 1) * 1024);
  SBAR();
  f32x2 a1 = {v[0], v[1]}, a2 = a1 * a1;
#pragma unroll
  for (int i = 1; i < 8; ++i) { const f32x2 t = {v[2 * i], v[2 * i + 1]}; a1 += t; a2 += t * t; }
  auto r1 = __builtin_amdgcn_permlane32_swap(__float_as_uint(a1[0] + a1[1]), __float_as_uint(a2[0] + a2[1]), false, false);
  st4(c, __uint_as_float(r1[0]) + __uint_as_float(r1[1]), c.lane * 4, WS_ST + inst * (64 * 4096) + tt * 4096 + nt * 256);
  PIN(res[0]); PIN(res[1]);
}

DI void wfrag_load(const unsigned short* W, int N, int nt, int ks, int r, int h, unsigned (&e)[8]) {
#pragma unroll
  for (int j = 0; j < 8; ++j) e[j] = W[(size_t)(16 * ks + 8 * (j >> 2) + 4 * h + (j & 3)) * N + 32 * nt + r];
}
DI void wfrag_store(const Ctx& c, const unsigned (&e)[8], unsigned dst, bool docs, unsigned csdst) {
  u32x4 v;
#pragma unroll
  for (int j = 0; j < 4; ++j) v[j] = e[2 * j] | (e[2 * j + 1] << 16);
  st16(c, v, c.voff, dst);
  if (docs) {
    float s = 0.f;
#pragma unroll
    for (int j = 0; j < 8; ++j) s += __uint_as_float(e[j] << 16);
    s = swap_half_sum(s);
    if (c.h == 0) st4(c, s, c.r * 4, csdst);
  }
}

DI u32x4 mish8(const f32x8& a, float rs) {
  float v[8];
#pragma unroll
  for (int j = 0; j < 8; ++j) {
    const float u = __builtin_amdgcn_exp2f(a[j] * (rs * 1.4426950408889634f));
    v[j] = a[j] * fmaf(-2.f * rs, __builtin_amdgcn_rcpf(fmaf(u, u + 2.f, 2.f)), rs);
  }
  return (u32x4){pk2(v[0], v[1]), pk2(v[2], v[3]), pk2(v[4], v[5]), pk2(v[6], v[7])};
}
#define LO8(s) __builtin_shufflevector(s, s, 0, 1, 2, 3, 4, 5, 6, 7)
#define HI8(s) __builtin_shufflevector(s, s, 8, 9, 10, 11, 12, 13, 14, 15)
DI f32x8 cat8(f32x4 a, f32x4 b) { return __builtin_shufflevector(a, b, 0, 1, 2, 3, 4, 5, 6, 7); }

template <int T, int PARTS = 3>
DI void ln_tile(const Ctx& c, int l, const f32x8& lo, const f32x8& hi, float rstd) {
  const int tt = 4 * c.tb + (c.w & 3);
  const unsigned so = WS_L + l * L_SZ + L_H + (tt * KS_F + 2 * (c.nb < 11 ? 3 * c.nb : 33 + 2 * (c.nb - 11)) + 2 * T) * 1024;
  if (PARTS & 1) st16(c, mish8(lo, rstd), c.voff, so);
  if (PARTS & 2) st16(c, mish8(hi, rstd), c.voff, so + 1024);
}
template <int MODE> DI void ln_publish(const Ctx& c, int l) {
  if (MODE == 0) publish(c, 4 + l, 4, FL_Q + l * 256 + c.nb * 16, c.tb);
  else publish(c, 6 + l, 8, FL_H + l * 256 + c.tb * 16, c.nb);
}

DI void pin16(f32x16& v) { PIN(v); }
DI void pinu(unsigned v) { asm volatile("" ::"v"(v)); }
DI u32x4 wfrag(const LAS char* base, int ks) { return *(const LAS u32x4*)(base + ks * 1024); }
DI void put16(LAS f32x4* ex, const f32x16& a) {
#pragma unroll
  for (int g = 0; g < 4; ++g) ex[g * 64] = (f32x4){a[4 * g], a[4 * g + 1], a[4 * g + 2], a[4 * g + 3]};
}

template <int MODE, bool FIRST>
DI void phase_ln(Ctx& c, int l, int inst, u32x4 (&w2r)[16]) {
  const int tt = 4 * c.tb + c.w;
  const unsigned xs = WS_X + inst * (2 * MB) + tt * 32768;
  constexpr int PD = 14;
  u32x4 xf[32], sp[4];
#pragma unroll
  for (int k = 0; k < PD; ++k) xf[k] = ld16(c, c.voff, xs + k * 1024);
  SBAR();
  const LAS char* wl = c.lds + LD_W + c.lane * 16;
  const LAS char* wl2 = wl + 65536;
  asm volatile("" : "+v"(wl2));
  f32x16 aA, aB;
#pragma unroll
  for (int i = 0; i < 16; ++i) { aA[i] = 0.f; aB[i] = 0.f; }
  u32x4 fa[3], fb[3];
  if (FIRST) BARRIER_LDS();
#pragma unroll
  for (int k = 0; k < 2; ++k) { fa[k] = wfrag(wl, k); fb[k] = wfrag(wl + 32768, k); }
  float rstd = 0.f, sc4[4];
  u32x2 we[3]; u32x4 xe;
  LAS float* sc = (LAS float*)(c.lds + LD_SC) + c.w * 256;
  const int k0q = c.w * 21 + (c.w < 2 ? c.w : 2);
  const unsigned w2s = WS_W2 + ((2 * c.nb8 + (c.w & 1)) * KS_F + k0q + 14) * 1024;
  const unsigned w2o = WS_W2 + ((2 * c.nb8 + 1 - (c.w & 1)) * KS_F + k0q + 14) * 1024;
  auto w2load = [&](int i) __attribute__((always_inline)) {
    w2r[i] = ld16(c, c.voff, ((i & 1) ? w2o : w2s) + (((i >> 1) == 7 && c.w >= 2) ? 6 : (i >> 1)) * 1024);
  };
  static_for<0, 32>([&](auto kc) __attribute__((always_inline)) {
    constexpr int k = decltype(kc)::v;
    if constexpr (FIRST && (((k & 7) == 6 && k < 24) || k == 31)) asm volatile("s_barrier" ::: "memory");
    aA = MFMA(as_bf(fa[k % 3]), as_bf(xf[k]), aA);
    pin16(aA); SBAR();
    if constexpr (k + 2 < 32) fa[(k + 2) % 3] = wfrag(wl, k + 2);
    if constexpr (k + PD < 32) xf[k + PD] = ld16(c, c.voff, xs + (k + PD) * 1024);
    if constexpr (k + PD >= 32 && k + PD < 36) sp[k + PD - 32] = ld16(c, c.voff, WS_ST + inst * (64 * 4096) + tt * 4096 + (k + PD - 32) * 1024);
    if constexpr (MODE == 1 && k >= 24) w2load(k - 24);
    SBAR();
    aB = MFMA(as_bf(fb[k % 3]), as_bf(xf[k]), aB);
    pin16(aB); SBAR();
    if constexpr (k + 2 < 32) fb[(k + 2) % 3] = wfrag(wl + 32768, k + 2);
    if constexpr (k == 29) {
      ((LAS f32x4*)sc)[c.lane] = (__builtin_bit_cast(f32x4, sp[0]) + __builtin_bit_cast(f32x4, sp[1])) +
                                 (__builtin_bit_cast(f32x4, sp[2]) + __builtin_bit_cast(f32x4, sp[3]));
#pragma unroll
      for (int i = 0; i < 4; ++i) sc4[i] = sc[i * 64 + c.lane];
    }
    if constexpr (k == (FIRST ? 31 : 30)) {
#pragma unroll
      for (int i = 0; i < 3; ++i) we[i] = ((const LAS u32x2*)(c.lds + LD_PAR))[MODE * 96 + 32 * i + c.r];
    }
    if constexpr (k == 31) {
      const float s = (sc4[0] + sc4[1]) + (sc4[2] + sc4[3]);
      auto rr = __builtin_amdgcn_permlane32_swap(__float_as_uint(s), __float_as_uint(s), false, false);
      const float mu = __uint_as_float(rr[0]) * (1.f / D_);
      const float var = fmaxf(__uint_as_float(rr[1]) * (1.f / D_) - mu * mu, 0.f);
      const float sd = rbf(__builtin_amdgcn_sqrtf(rbf(rbf(var) + 1e-5f)));
      rstd = __builtin_amdgcn_rcpf(sd);
      ((LAS float*)(c.lds + LD_ST))[c.w * 32 + c.r] = rstd;
      xe = (u32x4){c.h ? 0u : pk2(-mu, -mu), c.h ? 0u : pk2(-mu, sd), 0u, 0u};
    }
    SBAR();
  });
  aB = MFMA(as_bf((u32x4){we[1][0], we[1][1], 0u, 0u}), as_bf(xe), aB);
  aA = MFMA(as_bf((u32x4){we[0][0], we[0][1], 0u, 0u}), as_bf(xe), aA);
  f32x16 aC;
#pragma unroll
  for (int i = 0; i < 16; ++i) aC[i] = 0.f;
  aC = MODE == 0 ? MFMA(as_bf(xe), as_bf((u32x4){we[2][0], we[2][1], 0u, 0u}), aC) : MFMA(as_bf((u32x4){we[2][0], we[2][1], 0u, 0u}), as_bf(xe), aC);
  constexpr int CD = 5;
  u32x4 fc[CD + 1];
#pragma unroll
  for (int k = 0; k < CD; ++k) fc[k] = wfrag(wl2, k);
  SBAR();
  if (MODE == 1) {
    LAS f32x4* ex = (LAS f32x4*)(c.lds + LD_EX) + c.w * 384 + c.lane;
    put16(ex, aB);
    ex[256] = (f32x4){aA[8], aA[9], aA[10], aA[11]}; ex[320] = (f32x4){aA[12], aA[13], aA[14], aA[15]};
    BARRIER_LDS();
  }
  f32x4 ts[4];
  u32x4 pw;
  const float rq = rstd * (0.25f * 1.4426950408889634f);
  const unsigned vok = c.h * 512 + ((c.r & 19) | ((c.r & 4) << 1) | ((c.r & 8) >> 1)) * 16;
  const unsigned soq = WS_L + l * L_SZ + L_Q + (2 * c.nb * 64 + tt) * 1024;
  static_for<0, 32>([&](auto kc) __attribute__((always_inline)) {
    constexpr int k = decltype(kc)::v;
    aC = MODE == 0 ? MFMA(as_bf(xf[k]), as_bf(fc[k % (CD + 1)]), aC) : MFMA(as_bf(fc[k % (CD + 1)]), as_bf(xf[k]), aC);
    pin16(aC); SBAR();
    if constexpr (k + CD < 32) fc[(k + CD) % (CD + 1)] = wfrag(wl2, k + CD);
    if constexpr (MODE == 1 && (k & 1) == 0 && 8 + k / 2 < 16) w2load(8 + k / 2);
    if constexpr (MODE == 0 && k >= 6 && k <= 18 && (k & 3) == 2)
      st16(c, pw, k < 12 ? vok : c.voff, (k < 12 ? soq + (L_K - L_Q) : soq) + ((k & 4) ? 0 : 65536));
    if constexpr (MODE == 0 && k >= 2 && k < 18) {
      constexpr int i = k - 2;
      const f32x16& a = i < 8 ? aB : aA;
      const float rs = i < 8 ? rstd : rq;
      pw[i & 3] = pk2(a[2 * (i & 7)] * rs, a[2 * (i & 7) + 1] * rs);
      pinu(pw[i & 3]);
    }
    if constexpr (MODE == 0 && k == 29) {
#pragma unroll
      for (int g = 0; g < 4; ++g) ts[g] = *(const LAS f32x4*)((const LAS float*)(c.lds + LD_ST) + c.w * 32 + 8 * g + 4 * c.h);
    }
    SBAR();
  });
  if (MODE == 1) arrive4(c, 14 + l);
  if (MODE == 1) { ln_tile<0, 1>(c, l, LO8(aA), LO8(aA), rstd); if (c.nb < 11) ln_tile<2>(c, l, LO8(aC), HI8(aC), rstd); }
  else {
    unsigned q[8];
#pragma unroll
    for (int g = 0; g < 8; ++g) q[g] = pk2(aC[2 * g] * ts[g >> 1][2 * (g & 1)], aC[2 * g + 1] * ts[g >> 1][2 * (g & 1) + 1]);
    const unsigned vo = (c.r >> 4) * 65536 + c.h * 256 + (c.r & 15) * 16, so = WS_L + l * L_SZ + L_V + (2 * c.nb * 128 + 2 * tt) * 512;
    st16(c, (u32x4){q[0], q[1], q[2], q[3]}, vo, so);
    st16(c, (u32x4){q[4], q[5], q[6], q[7]}, vo, so + 512);
  }
  ln_publish<MODE>(c, l);
}
template <class F>
DI void help_ln(Ctx& c, int l, F&& mid) {
  BARRIER_LDS();
  const LAS f32x4* ex = (const LAS f32x4*)(c.lds + LD_EX) + (c.w & 3) * 384 + c.lane;
  const float rstd = ((const LAS float*)(c.lds + LD_ST))[(c.w & 3) * 32 + c.r];
  const f32x8 b0 = cat8(ex[0], ex[64]), b1 = cat8(ex[128], ex[192]), a1 = cat8(ex[256], ex[320]);
  SBAR();
  ln_tile<1>(c, l, b0, b1, rstd);
  ln_tile<0, 2>(c, l, a1, a1, rstd);
  SBAR();
  mid();
  ln_publish<1>(c, l);
}
DI void colsum_load(const Ctx& c, int mode, float (&v)[32]) {
  const int t = min((c.w - 4) * 64 + c.lane, 95);
#pragma unroll
  for (int i = 0; i < 32; ++i) v[i] = ld4(c, t * 4, WS_CSP + ((mode * 16 + c.nb) * 32 + i) * 384);
  SBAR();
}
DI void colsum_put(const Ctx& c, int mode, const float (&v)[32], unsigned bias) {
  const int t = (c.w - 4) * 64 + c.lane;
  float s = 0.f;
#pragma unroll
  for (int i = 0; i < 32; i += 4) s += (v[i] + v[i + 1]) + (v[i + 2] + v[i + 3]);
  const float s0 = rbf(s), s1 = rbf(s - s0);
  if (t < 96) ((LAS u32x2*)(c.lds + LD_PAR))[mode * 96 + t] = (u32x2){pk2(s0, s1), (pk2(s - s0 - s1, 0.f) & 0xffffu) | (bias << 16)};
}

DI f32x16 reduce_res(Ctx& c, const f32x16 (&a)[4], u32x4 (&res)[2], int ctr) {
  LAS f32x4* red = (LAS f32x4*)(c.lds + LD_EX) + c.lane;
#pragma unroll
  for (int j = 1; j < 4; ++j) {
    LAS f32x4* p = red + ((c.w ^ j) * 3 + j - 1) * 256;
#pragma unroll
    for (int g = 0; g < 4; ++g) p[g * 64] = (f32x4){a[j][4 * g], a[j][4 * g + 1], a[j][4 * g + 2], a[j][4 * g + 3]};
  }
  if (ctr >= 0) { arrive4(c, ctr); wait4(c, ctr); } else BARRIER_LDS();
  const LAS f32x4* q = red + c.w * 768;
  f32x16 v;
#pragma unroll
  for (int g = 0; g < 4; ++g) {
    const f32x4 s = ((f32x4){a[0][4 * g], a[0][4 * g + 1], a[0][4 * g + 2], a[0][4 * g + 3]} + q[g * 64]) + (q[256 + g * 64] + q[512 + g * 64]);
#pragma unroll
    for (int j = 0; j < 4; ++j) v[4 * g + j] = s[j];
  }
  pack16(v, res);
  return v;
}
#define MM4(w0, w1, x0, x1) do { a[0] = MFMA(as_bf(w0), as_bf(x0), a[0]); a[1] = MFMA(as_bf(w1), as_bf(x0), a[1]); \
                                 a[2] = MFMA(as_bf(w0), as_bf(x1), a[2]); a[3] = MFMA(as_bf(w1), as_bf(x1), a[3]); } while (0)

DI f32x16 phase_wo(Ctx& c, int l, const u32x4 (&wo)[16], u32x4 (&res)[2]) {
  const unsigned xs = WS_L + l * L_SZ + L_O + ((2 * c.tb64 + (c.w >> 1)) * 32 + 8 * c.w) * 1024;
  const unsigned xo = WS_L + l * L_SZ + L_O + ((2 * c.tb64 + 1 - (c.w >> 1)) * 32 + 8 * c.w) * 1024;
  u32x4 xf[16];
#pragma unroll
  for (int k = 0; k < 8; ++k) { xf[2 * k] = ld16(c, c.voff, xs + k * 1024); xf[2 * k + 1] = ld16(c, c.voff, xo + k * 1024); }
  asm volatile("s_barrier" ::: "memory");
  SBAR();
  f32x16 a[4];
#pragma unroll
  for (int i = 16; i < 64; ++i) a[i >> 4][i & 15] = 0.f;
  a[0] = unpack16(res);
#pragma unroll
  for (int k = 0; k < 8; ++k) { MM4(wo[2 * k], wo[2 * k + 1], xf[2 * k], xf[2 * k + 1]); PIN(a[0]); PIN(a[1]); PIN(a[2]); PIN(a[3]); SBAR(); }
  return reduce_res(c, a, res, -1);
}

DI f32x16 phase_w2(Ctx& c, int l, const u32x4 (&w2r)[16], u32x4 (&res)[2]) {
  const int k0 = c.w * 21 + (c.w < 2 ? c.w : 2);
  const unsigned xs = WS_L + l * L_SZ + L_H + ((2 * c.tb64 + (c.w >> 1)) * KS_F + k0) * 1024;
  const unsigned xo = WS_L + l * L_SZ + L_H + ((2 * c.tb64 + 1 - (c.w >> 1)) * KS_F + k0) * 1024;
  constexpr int WIN = 10;
  u32x4 xf[2 * WIN];
#pragma unroll
  for (int k = 0; k < WIN; ++k) { xf[2 * k] = ld16(c, c.voff, xs + k * 1024); xf[2 * k + 1] = ld16(c, c.voff, xo + k * 1024); }
  SBAR();
  const LAS u32x4* w0 = (const LAS u32x4*)(c.lds + w2_run(2 * c.w + (c.w & 1))) + c.lane;
  const LAS u32x4* w1 = (const LAS u32x4*)(c.lds + w2_run(2 * c.w + 1 - (c.w & 1))) + c.lane;
  f32x16 a[4];
#pragma unroll
  for (int i = 16; i < 64; ++i) a[i >> 4][i & 15] = 0.f;
  a[0] = unpack16(res);
  u32x4 f0[2], f1[2];
  f0[0] = w0[0]; f1[0] = w1[0];
  static_for<0, 22>([&](auto kc) __attribute__((always_inline)) {
    constexpr int k = decltype(kc)::v;
    const u32x4 g0 = k < 14 ? f0[k & 1] : w2r[k < 14 ? 0 : 2 * (k - 14)], g1 = k < 14 ? f1[k & 1] : w2r[k < 14 ? 0 : 2 * (k - 14) + 1];
    const u32x4 x0 = xf[2 * (k % WIN)], x1 = xf[2 * (k % WIN) + 1];
    constexpr int kn = k - 1 + WIN;
    const int kk = (kn == 21 && c.w >= 2) ? 20 : kn;
    if constexpr (kn == 22) asm volatile("s_barrier" ::: "memory");
    if (k < 21 || c.w < 2) {
      a[0] = MFMA(as_bf(g0), as_bf(x0), a[0]);
      pin16(a[0]); SBAR();
      if constexpr (k >= 1 && kn < 22) xf[2 * ((k - 1) % WIN)] = ld16(c, c.voff, xs + kk * 1024);
      SBAR();
      a[1] = MFMA(as_bf(g1), as_bf(x0), a[1]);
      pin16(a[1]); SBAR();
      if constexpr (k + 1 < 14) f0[(k + 1) & 1] = w0[(k + 1) * 64];
      SBAR();
      a[2] = MFMA(as_bf(g0), as_bf(x1), a[2]);
      pin16(a[2]); SBAR();
      if constexpr (k >= 1 && kn < 22) xf[2 * ((k - 1) % WIN) + 1] = ld16(c, c.voff, xo + kk * 1024);
      SBAR();
      a[3] = MFMA(as_bf(g1), as_bf(x1), a[3]);
      pin16(a[3]); SBAR();
      if constexpr (k + 1 < 14) f1[(k + 1) & 1] = w1[(k + 1) * 64];
      SBAR();
    }
  });
  if (l == 0) { arrive4(c, 16); wait4(c, 16); } else BARRIER_LDS();
  return reduce_res(c, a, res, l == 0 ? 17 : -1);
}

#define SMF(a, b8, c) __builtin_amdgcn_smfmac_f32_32x32x32_bf16((a), __builtin_bit_cast(bf16x16, b8), (c), sidx, 0, 0)
#define LDF(p) (*(const LAS bf16x8*)(p))
DI float max16(const f32x16& s) {
  float a = fmaxf(fmaxf(s[0], s[1]), s[2]), b = fmaxf(fmaxf(s[3], s[4]), s[5]);
  a = fmaxf(fmaxf(a, s[6]), s[7]); b = fmaxf(fmaxf(b, s[8]), s[9]);
  a = fmaxf(fmaxf(a, s[10]), s[11]); b = fmaxf(fmaxf(b, s[12]), s[13]);
  a = fmaxf(fmaxf(a, s[14]), s[15]);
  return swap_half_max(fmaxf(a, b));
}
DI void attn_slow(const LAS char* kp, const LAS char* vp, bool isv, bf16x8 q, f32x16& o, float& m) {
  const int sidx = 0xDDDDDDDD;
  f32x16 zero;
#pragma unroll
  for (int i = 0; i < 16; ++i) zero[i] = 0.f;
  o = zero; m = -3.0e38f;
  for (int t = 0; t < 32; ++t) {
    f32x16 s = MFMA(LDF(kp + t * 1024), q, zero);
    const float mn = fmaxf(m, max16(s));
    const float al = __builtin_amdgcn_exp2f(m - mn);
    m = mn;
#pragma unroll
    for (int i = 0; i < 16; ++i) { o[i] *= al; s[i] = __builtin_amdgcn_exp2f(s[i] - mn); }
    const int vo = isv ? t * 1024 : 0;
    o = SMF(LDF(vp + vo), LO8(s), o);
    o = SMF(LDF(vp + vo + 512), HI8(s), o);
  }
}

struct In { const unsigned short *x, *wqkv, *wo, *w1, *b1, *w2; unsigned short* out; };
struct Jobs { unsigned e[4][8], dst[4]; bool ok[4]; };
DI void jobs_load(const Ctx& c, const In& in, Jobs& jb) {
  const int t0 = c.nb < 11 ? 3 * c.nb : 33 + 2 * (c.nb - 11), nt1 = c.nb < 11 ? 3 : 2;
  const unsigned short* W[4]; int N[4];
#pragma unroll
  for (int u = 0; u < 4; ++u) {
    const int j = c.w + 4 * u;
    int nt, ks;
    if (j < 6) { W[u] = in.w1; N[u] = F_; jb.ok[u] = (j >> 1) < nt1; nt = t0 + (jb.ok[u] ? j >> 1 : 0); ks = 2 * c.tb + (j & 1); jb.dst[u] = WS_W1 + (nt * 32 + ks) * 1024; }
    else if (j < 8) { W[u] = in.wo; N[u] = D_; jb.ok[u] = true; nt = 2 * c.nb8 + (j & 1); ks = c.tb64; jb.dst[u] = WS_WO + (nt * 32 + ks) * 1024; }
    else { W[u] = in.w2; N[u] = D_; ks = c.tb64 + 32 * ((j - 8) >> 1); jb.ok[u] = ks < KS_F; if (!jb.ok[u]) ks = 0; nt = 2 * c.nb8 + (j & 1); jb.dst[u] = WS_W2 + (nt * KS_F + ks) * 1024; }
    W[u] += 16 * ks * N[u] + 32 * nt;
    asm volatile("" : "+s"(W[u]));
    asm volatile("" : "+s"(N[u]));
  }
#pragma unroll
  for (int u = 0; u < 4; ++u) {
    const __amdgpu_buffer_rsrc_t rw = __builtin_amdgcn_make_buffer_rsrc((void*)W[u], 0, 32 * N[u], 0x00020000);
    const unsigned o = (4 * c.h * N[u] + c.r) * 2;
#pragma unroll
    for (int j = 0; j < 8; ++j) jb.e[u][j] = __builtin_amdgcn_raw_buffer_load_b16(rw, o, (8 * (j >> 2) + (j & 3)) * 2 * N[u], 0);
  }
}
DI void jobs_put(const Ctx& c, Jobs& jb) {
#pragma unroll
  for (int i = 0; i < 32; ++i) PIN(jb.e[i >> 3][i & 7]);
#pragma unroll
  for (int u = 0; u < 4; ++u) {
    const int j = c.w + 4 * u;
    if (jb.ok[u]) wfrag_store(c, jb.e[u], jb.dst[u], j < 6, WS_CSP + ((16 + c.nb) * 32 + 2 * c.tb + (j & 1)) * 384 + (j >> 1) * 128);
  }
}
DI void jobs_publish(const Ctx& c) {
  asm volatile("s_waitcnt vmcnt(0)" ::: "memory");
  unsigned old = 0;
  if (c.lane == 0) old = __hip_atomic_fetch_add((LAS unsigned*)(c.lds + LD_CTR) + 2, 1u, __ATOMIC_RELAXED, __HIP_MEMORY_SCOPE_WORKGROUP);
  if (__builtin_amdgcn_readfirstlane(old) == 3 && c.lane < 2)
    __hip_atomic_store(c.lane ? flag_ptr(c, FL_W1 + c.nb, c.tb) : flag_ptr(c, FL_W2 + c.nb8, c.tb64), c.token, RLX_AGENT);
}

#define EX1(r) "v_exp_f32 v" #r ", v" #r "\n\t"
#define ASTEP(KOFF, VOFF, kc_, kn_, v0_, v1_, nv0_, pv1_)                                                                  \
  asm volatile("s_waitcnt lgkmcnt(0)\n\t" EX1(64) EX1(65)                                                                  \
               "v_smfmac_f32_32x32x32_bf16 %[o1], %[pv1], v[88:95], %[idx]\n\t"                                            \
               "ds_read_b128 %[nv0], %[vp] offset:%[vo]\n\tds_read_b128 %[pv1], %[vp] offset:%[vo1]\n\t"                   \
               EX1(66) EX1(67) EX1(68)                                                                                     \
               "v_mfma_f32_32x32x16_bf16 v[80:95], %[kc], %[q1], %[c1]\n\t"                                                \
               "ds_read_b128 %[kc], %[kp] offset:%[ko]\n\t"                                                                \
               EX1(69) EX1(70) EX1(71) EX1(72) EX1(73)                                                                     \
               "v_smfmac_f32_32x32x32_bf16 %[o0], %[v0], v[64:71], %[idx]\n\t"                                             \
               EX1(74) EX1(75) EX1(76) EX1(77) EX1(78) EX1(79) EX1(80) EX1(81)                                             \
               "v_smfmac_f32_32x32x32_bf16 %[o0], %[v1], v[72:79], %[idx]\n\t"                                             \
               EX1(82) EX1(83) EX1(84)                                                                                     \
               "v_mfma_f32_32x32x16_bf16 v[64:79], %[kn], %[q0], %[c0]\n\t"                                                \
               EX1(85) EX1(86) EX1(87) EX1(88) EX1(89)                                                                     \
               "v_smfmac_f32_32x32x32_bf16 %[o1], %[v0], v[80:87], %[idx]\n\t"                                             \
               EX1(90) EX1(91) EX1(92) EX1(93) EX1(94) "v_exp_f32 v95, v95"                                                \
               : "+{v[64:79]}"(sA), "+{v[80:95]}"(sB), [o0] "+v"(o0), [o1] "+v"(o1), [kc] "+v"(kc_), [nv0] "=&v"(nv0_),    \
                 [pv1] "+v"(pv1_)                                                                                          \
               : [kn] "v"(kn_), [v0] "v"(v0_), [v1] "v"(v1_), [q0] "v"(q0), [q1] "v"(q1), [c0] "v"(cin0), [c1] "v"(cin1),  \
                 [idx] "v"(sidx), [kp] "v"(kpa), [vp] "v"(vpa), [ko] "n"(KOFF), [vo] "n"(VOFF), [vo1] "n"((VOFF) + 512)     \
               : "memory")

template <int half, int l>
DI void phase_attn(Ctx& c, const In& in, u32x4 (&wo)[16], u32x4 (&hw)[28]) {
  const int qt = c.qb * 8 + (c.w & 3) * 2;
  const unsigned lay = WS_L + l * L_SZ;
  const char* kvg = c.ws + lay + ((c.w & 1) ? L_V : L_K) + c.head * 65536 + ((c.w >> 1) & 1) * 32768 + half * 4096;
  const unsigned kvd = c.lbase + ((c.w & 1) ? LD_V : LD_W) + ((c.w >> 1) & 1) * 32768 + half * 4096;
  u32x4 qv0, qv1;
  asm volatile("s_nop 4\n\tglobal_load_dwordx4 %0, %2, %3 sc1\n\tglobal_load_dwordx4 %1, %2, %3 offset:1024 sc1"
               : "=&v"(qv0), "=&v"(qv1) : "v"(c.voff), "s"(c.ws + lay + L_Q + (c.head * 64 + qt) * 1024) : "memory");
#define KV4(u) glds16x4(kvg + (u) * 8192, c.voff, kvd + (u) * 8192)
  glds16x4(kvg, c.voff, kvd);
  if (half == 0) {
    const unsigned o1 = (c.lane & 11) == 0 ? 0x3f803f80u : 0u;
    ((LAS u32x4*)(c.lds + LD_ONE))[c.w * 64 + c.lane] = (u32x4){o1, o1, o1, o1};
  }
  int sidx = 0xDDDDDDDD;
  const bool isv = c.r < 16;
  const LAS char* kp0 = c.lds + LD_W + half * 32768 + c.lane * 16;
  const LAS char* vp0 = c.lds + (isv ? LD_V + half * 32768 : LD_ONE) + c.h * 256 + (c.r & 15) * 16;
  const int vstep = isv ? 4096 : 0;
  if (half == 0) asm volatile("s_waitcnt vmcnt(0) lgkmcnt(0)\n\ts_barrier" : "+v"(qv0), "+v"(qv1)::"memory");
  else asm volatile("s_waitcnt vmcnt(4)\n\ts_barrier" : "+v"(qv0), "+v"(qv1)::"memory");
  if (half == 0) __builtin_amdgcn_s_setprio(1);
  const bf16x8 q0 = as_bf(qv0), q1 = as_bf(qv1);
  f32x16 zero;
#pragma unroll
  for (int i = 0; i < 16; ++i) zero[i] = 0.f;
  f32x16 cin0, cin1, o0 = zero, o1 = zero, sA, sB = zero;
  float m0, m1;
  bf16x8 ka = LDF(kp0), kb = LDF(kp0 + 1024), va0 = LDF(vp0), va1 = LDF(vp0 + 512), vb0, vb1 = as_bf((u32x4){0u, 0u, 0u, 0u});
  {
    const f32x16 s = MFMA(ka, q0, zero), s1 = MFMA(ka, q1, zero);
    m0 = max16(s); m1 = max16(s1);
#pragma unroll
    for (int i = 0; i < 16; ++i) { cin0[i] = -m0; cin1[i] = -m1; sA[i] = s[i] - m0; }
  }
  unsigned kpa = (unsigned)(size_t)kp0, vpa = (unsigned)(size_t)vp0;
  asm volatile("s_nop 1" : "+v"(cin0), "+v"(cin1), "+v"(sA), "+v"(sB), "+v"(o0), "+v"(o1), "+v"(sidx), "+v"(vb1));
#define QUAD(A, B, MID, C, D)                                                        \
  do {                                                                               \
    ASTEP(2048, 1024, ka, kb, va0, va1, vb0, vb1); A;                                \
    ASTEP(3072, 2048, kb, ka, vb0, vb1, va0, va1); B; MID;                           \
    ASTEP(4096, 3072, ka, kb, va0, va1, vb0, vb1); C;                                \
    vpa += vstep;                                                                    \
    ASTEP(5120, 0, kb, ka, vb0, vb1, va0, va1); D;                                   \
    kpa += 4096;                                                                      \
  } while (0)
#define NOP (void)0
#define WBAR(n) asm volatile("s_waitcnt vmcnt(" #n ")\n\ts_barrier" ::: "memory")
  constexpr bool JOBS = half == 0 && l == 0;
  Jobs jb;
  KV4(1);
  QUAD(KV4(2), NOP, WBAR(8), KV4(3), if (JOBS) { jobs_load(c, in, jb); SBAR(); });
  QUAD(NOP, NOP, if (JOBS) WBAR(32); else WBAR(0); if (half == 1 && l == 1) (w2_fetch<0, 21>(c, hw)), NOP,
       if (JOBS) { jobs_put(c, jb); SBAR(); });
  QUAD(if (JOBS) jobs_publish(c), NOP, NOP,
       if (half == 1 && l == 0) { poll_any(c, FL_W2 + c.nb8, c.lane & 31); (w2_fetch<0, 21>(c, hw)); }, NOP);
  for (int it = 3; it < 8; ++it) QUAD(NOP, NOP, NOP, NOP, NOP);
#undef WBAR
  asm volatile("s_nop 1\n\tv_smfmac_f32_32x32x32_bf16 %[o1], %[pv1], v[88:95], %[idx]\n\ts_waitcnt lgkmcnt(0)\n\ts_nop 7\n\ts_nop 7"
               : [o1] "+v"(o1), "+v"(o0), "+v"(ka), "+v"(kb), "+v"(va0), "+v"(va1) : [pv1] "v"(vb1), "{v[80:95]}"(sB), [idx] "v"(sidx));
#undef QUAD
#undef KV4
  if (__any(!(o0[8] < 1.0995116e12f))) attn_slow(kp0, vp0, isv, q0, o0, m0);
  if (__any(!(o1[8] < 1.0995116e12f))) attn_slow(kp0, vp0, isv, q1, o1, m1);
  LAS f32x4* cmb = (LAS f32x4*)(c.lds + LD_CMB) + (c.w & 3) * 320 + c.lane;
  const unsigned w1src = WS_W1 + (c.nb < 11 ? 3 * c.nb : 33 + 2 * (c.nb - 11)) * 32768;
  if (half == 0) {
    __builtin_amdgcn_s_setprio(0);
    cmb[0] = (f32x4){o0[0], o0[1], o0[2], o0[3]}; cmb[64] = (f32x4){o0[4], o0[5], o0[6], o0[7]};
    cmb[128] = (f32x4){o1[0], o1[1], o1[2], o1[3]}; cmb[192] = (f32x4){o1[4], o1[5], o1[6], o1[7]};
    cmb[256] = (f32x4){o0[8], m0, o1[8], m1};
    arrive4(c, 12 + l);
    if (l == 0) poll_any(c, c.lane < 32 ? FL_W1 + c.nb : FL_W2 + c.nb8, c.lane < 32 ? (c.lane & 15) : (c.lane & 31));
    const unsigned wos = WS_WO + ((2 * c.nb8 + (c.w & 1)) * 32 + 8 * c.w) * 1024;
    const unsigned woo = WS_WO + ((2 * c.nb8 + 1 - (c.w & 1)) * 32 + 8 * c.w) * 1024;
#pragma unroll
    for (int k = 0; k < 8; ++k) { wo[2 * k] = ld16(c, c.voff, wos + k * 1024); wo[2 * k + 1] = ld16(c, c.voff, woo + k * 1024); }
    SBAR();
    wait4<true>(c, 12 + l);
    help_dma<8>(c, w1src, LD_W);
    if (c.nb < 11) help_dma<8>(c, w1src + 65536, LD_W + 65536);
  }
  BARRIER_LDS();
  if (half == 1) {
    const f32x4 lm = cmb[256];
#pragma unroll
    for (int u = 0; u < 2; ++u) {
      const f32x16& o = u ? o1 : o0; const float m = u ? m1 : m0;
      const f32x8 p2 = cat8(cmb[128 * u], cmb[128 * u + 64]);
      const float m2 = lm[2 * u + 1];
      const float mm = fmaxf(m, m2);
      const float f1 = __builtin_amdgcn_exp2f(m - mm), f2 = __builtin_amdgcn_exp2f(m2 - mm);
      const float inv = __builtin_amdgcn_rcpf(o[8] * f1 + lm[2 * u] * f2);
      const float g1 = f1 * inv, g2 = f2 * inv;
      unsigned p[4];
#pragma unroll
      for (int i = 0; i < 4; ++i) p[i] = pk2(o[2 * i] * g1 + p2[2 * i] * g2, o[2 * i + 1] * g1 + p2[2 * i + 1] * g2);
      st16(c, (u32x4){p[0], p[1], p[2], p[3]}, c.voff, lay + L_O + ((qt + u) * 32 + c.head) * 1024);
    }
    asm volatile("s_waitcnt vmcnt(0)" ::: "memory");
    if (c.lane < 8) __hip_atomic_store(flag_ptr(c, FL_O + l * 256 + (c.qb * 4 + (c.w & 3)) * 8 + c.lane, c.head), c.token, RLX_AGENT);
  }
}

DI u64 uniform64(u64 v) {
  return ((u64)__builtin_amdgcn_readfirstlane((unsigned)(v >> 32)) << 32) | (unsigned)__builtin_amdgcn_readfirstlane((unsigned)v);
}

DI void run_main(Ctx& c, const In& in) {
  const u64 epoch = __hip_atomic_load((gu64*)c.ws, RLX_AGENT);
  u32x4 res[2];
  {
    if (c.w == 0 && c.lane < 32) ((LAS unsigned*)(c.lds + LD_CTR))[c.lane] = 0u;
    const unsigned short* src = in.x + (size_t)((2 * c.tb64 + (c.w >> 1)) * 32 + c.r) * D_ + 32 * (2 * c.nb8 + (c.w & 1)) + 4 * c.h;
    u32x2 xr[4];
#pragma unroll
    for (int i = 0; i < 4; ++i) xr[i] = *(const u32x2*)(src + 8 * i);
    BARRIER_LDS();
    c.token = uniform64(epoch) + 1;
    res[0] = (u32x4){xr[0][0], xr[0][1], xr[1][0], xr[1][1]}; res[1] = (u32x4){xr[2][0], xr[2][1], xr[3][0], xr[3][1]};
    store_x(c, 0, res, unpack16(res));
    publish(c, 0, 4, FL_X + c.tb * 16, c.nb);
    poll<20>(c, (c.lane & 16) ? FL_WQ + c.nb * 16 + c.tb : FL_X + c.tb * 16 + c.nb, c.lane & 15);
    BARRIER_LDS();
    u32x4 dummy[16];
    fresh(c);
    phase_ln<0, true>(c, 0, 0, dummy);
  }
  for (int l = 0; l < 2; ++l) {
    u32x4 wo[16], w2r[16];
    if (l == 1) {
      fresh(c);
      u32x4 dummy[16];
      phase_ln<0, false>(c, l, 2 * l, dummy);
    }
    fresh(c);
    poll<10>(c, FL_Q + l * 256 + c.head * 8 + c.qb, c.lane & 15);
    BARRIER_LDS();
    { u32x4 unused[28]; if (l == 0) phase_attn<0, 0>(c, in, wo, unused); else phase_attn<0, 1>(c, in, wo, unused); }
    fresh(c);
    poll<32>(c, FL_O + l * 256 + c.tb64 * 8 + c.nb8, c.lane & 31);
    BARRIER_LDS();
    store_x(c, 2 * l + 1, res, phase_wo(c, l, wo, res));
    publish(c, 8 + l, 4, FL_X + (2 * l + 1) * 256 + c.tb * 16, c.nb);
    fresh(c);
    poll<12>(c, FL_X + (2 * l + 1) * 256 + c.tb * 16 + c.nb, c.lane & 15);
    BARRIER_ALL();
    phase_ln<1, false>(c, l, 2 * l + 1, w2r);
    fresh(c);
    poll<22>(c, FL_H + l * 256 + c.tb * 16 + c.nb, c.lane & 15);
    BARRIER_LDS();
    const f32x16 x2 = phase_w2(c, l, w2r, res);
    fresh(c);
    if (l == 0) {
      store_x(c, 2, res, x2);
      publish(c, 10, 4, FL_X + 2 * 256 + c.tb * 16, c.nb);
      poll<12>(c, FL_X + 2 * 256 + c.tb * 16 + c.nb, c.lane & 15);
      BARRIER_LDS();
    }
  }
  {
    auto s0 = __builtin_amdgcn_permlane32_swap(res[0][0], res[0][2], false, false), s1 = __builtin_amdgcn_permlane32_swap(res[0][1], res[0][3], false, false);
    auto s2 = __builtin_amdgcn_permlane32_swap(res[1][0], res[1][2], false, false), s3 = __builtin_amdgcn_permlane32_swap(res[1][1], res[1][3], false, false);
    unsigned short* op = in.out + (size_t)((2 * c.tb64 + (c.w >> 1)) * 32 + c.r) * D_ + 32 * (2 * c.nb8 + (c.w & 1)) + 8 * c.h;
    __builtin_nontemporal_store((u32x4){s0[0], s1[0], s0[1], s1[1]}, (u32x4*)op);
    __builtin_nontemporal_store((u32x4){s2[0], s3[0], s2[1], s3[1]}, (u32x4*)(op + 16));
  }
  if (c.bid == 0 && c.w == 0 && c.lane == 0) __hip_atomic_store((gu64*)c.ws, c.token, RLX_AGENT);
}

DI void run_help(Ctx& c, const In& in) {
  const u64 epoch = __hip_atomic_load((gu64*)c.ws, RLX_AGENT);
  {
    unsigned e0[8], e1[8];
    const int qk = 2 * c.tb + (c.w & 1), qa = (c.w >> 1) & 1;
    wfrag_load(in.wqkv, 3 * D_, qa * 16 + c.nb, qk, c.r, c.h, e0);
    wfrag_load(in.wqkv, 3 * D_, 32 + c.nb, qk, c.r, c.h, e1);
    BARRIER_LDS();
    c.token = uniform64(epoch) + 1;
    wfrag_store(c, e0, WS_WQKV + ((qa * 16 + c.nb) * 32 + qk) * 1024, true, WS_CSP + (c.nb * 32 + qk) * 384 + qa * 128);
    if (qa == 0) wfrag_store(c, e1, WS_WQKV + ((32 + c.nb) * 32 + qk) * 1024, true, WS_CSP + (c.nb * 32 + qk) * 384 + 256);
    publish(c, 1, 4, FL_WQ + c.nb * 16, c.tb);
    BARRIER_LDS();
    fresh(c);
#define WQC(q) _Pragma("unroll") for (int t_ = 0; t_ < 3; ++t_) \
    glds16x2(c.ws + WS_WQKV + ((16 * t_ + c.nb) * 32 + 8 * (q) + 2 * (c.w & 3)) * 1024, c.voff, c.lbase + LD_W + t_ * 32768 + (8 * (q) + 2 * (c.w & 3)) * 1024)
    WQC(0); WQC(1);
    asm volatile("s_waitcnt vmcnt(6)\n\ts_barrier" ::: "memory");
    WQC(2);
    asm volatile("s_waitcnt vmcnt(6)\n\ts_barrier" ::: "memory");
    WQC(3);
    asm volatile("s_waitcnt vmcnt(6)\n\ts_barrier" ::: "memory");
    if (c.w < 6) {
      float cv[32];
      colsum_load(c, 0, cv);
      asm volatile("s_waitcnt vmcnt(32)\n\ts_barrier" ::: "memory");
      colsum_put(c, 0, cv, 0u);
    } else {
      asm volatile("s_waitcnt vmcnt(0)\n\ts_barrier" ::: "memory");
    }
    BARRIER_LDS();
#undef WQC
  }
  for (int l = 0; l < 2; ++l) {
    u32x4 wo[16];
    fresh(c);
    BARRIER_LDS();
    fresh(c);
    u32x4 hw[28];
    if (l == 0) phase_attn<1, 0>(c, in, wo, hw); else phase_attn<1, 1>(c, in, wo, hw);
    fresh(c);
    BARRIER_LDS();
    asm volatile("s_barrier" ::: "memory");
    help_dma<8>(c, WS_W1 + (c.nb < 11 ? 3 * c.nb + 1 : 34 + 2 * (c.nb - 11)) * 32768, LD_W + 32768);
    w2_fetch<21, 28>(c, hw);
    asm volatile("s_barrier" ::: "memory");
    if (l == 0 && c.w < 6) {
      const int bcol = 32 * (c.nb < 11 ? 3 * c.nb : 33 + 2 * (c.nb - 11)) + (c.w - 4) * 64 + c.lane;
      const unsigned bv = in.b1[bcol < F_ ? bcol : 0];
      float cv[32];
      colsum_load(c, 1, cv);
      colsum_put(c, 1, cv, bv);
    }
    BARRIER_ALL();
    w2_put<21, 28>(c, hw);
    help_ln(c, l, [&]() __attribute__((always_inline)) { wait4<true>(c, 14 + l); w2_put<0, 12>(c, hw); });
    w2_put<12, 21>(c, hw);
    BARRIER_LDS();
    asm volatile("s_barrier" ::: "memory");
    if (l == 0) {
      fresh(c);
#pragma unroll
      for (int u = 0; u < 24; ++u) hw[u] = ld16(c, c.voff, WS_WQKV + (((u >> 3) * 16 + c.nb) * 32 + (u & 7) * 4 + (c.w & 3)) * 1024);
      SBAR();
      wait4<true>(c, 16);
#pragma unroll
      for (int u = 0; u < 24; ++u) *(LAS u32x4*)(c.lds + LD_W + (u * 4 + (c.w & 3)) * 1024 + c.voff) = hw[u];
      BARRIER_LDS();
    } else {
      asm volatile("s_barrier\n\ts_barrier" ::: "memory");
    }
  }
}

__global__ __launch_bounds__(512, 2) void mega(const unsigned short* __restrict__ x, const unsigned short* __restrict__ wqkv,
                                               const unsigned short* __restrict__ wo_g, const unsigned short* __restrict__ w1,
                                               const unsigned short* __restrict__ b1, const unsigned short* __restrict__ w2,
                                               char* __restrict__ ws, unsigned short* __restrict__ out) {
  extern __shared__ __attribute__((aligned(16))) char lds_[];
  Ctx c;
  c.ws = ws; c.lds = (LAS char*)lds_; c.rs = __builtin_amdgcn_make_buffer_rsrc(ws, 0, WS_END, 0x00020000);
  c.lbase = (unsigned)(size_t)c.lds;
  c.bid = blockIdx.x; c.lane = threadIdx.x & 63; c.w = __builtin_amdgcn_readfirstlane(threadIdx.x >> 6); c.dead = false;
  fresh(c);
  const In in = {x, wqkv, wo_g, w1, b1, w2, out};
  if (c.w == 7) {
    const int i = c.lane & 15;
    const int line = i < 4 ? FL_X + i * 256 + c.tb * 16 + c.nb : i < 6 ? FL_H + (i & 1) * 256 + c.tb * 16 + c.nb
                   : i < 8 ? FL_Q + (i & 1) * 256 + c.head * 8 + c.qb : i < 12 ? FL_O + (i & 1) * 256 + c.tb64 * 8 + c.nb8
                   : i == 12 ? FL_WQ + c.nb * 16 + c.tb : i == 13 ? FL_W1 + c.nb : FL_W2 + c.nb8;
    unsigned sv;
    asm volatile("s_mov_b32 %0, m0\n\ts_mov_b32 m0, %3\n\ts_nop 2\n\tglobal_load_lds_dword %1, %2 sc1\n\ts_mov_b32 m0, %0"
                 : "=&s"(sv) : "v"(line * 256 + ((i == 10 || i == 11 || i == 15) ? 128 : 0)), "s"(ws), "s"(c.lbase + LD_SC) : "memory");
  }
  if (c.w < 4) run_main(c, in); else run_help(c, in);
}

extern "C" void kernel_launch(void* const* d_in, const int* in_sizes, int n_in, void* d_out, int out_size, void* d_ws,
                              size_t ws_size, hipStream_t stream) {
  (void)hipFuncSetAttribute((const void*)mega, hipFuncAttributeMaxDynamicSharedMemorySize, LDS_BYTES);
  mega<<<256, 512, LDS_BYTES, stream>>>((const unsigned short*)d_in[0], (const unsigned short*)d_in[1], (const unsigned short*)d_in[2],
                                        (const unsigned short*)d_in[4], (const unsigned short*)d_in[5], (const unsigned short*)d_in[6],
                                        (char*)d_ws, (unsigned short*)d_out);
}
```

```cpp
#include <hip/hip_runtime.h>
#include <hip/hip_bf16.h>

typedef __attribute__((ext_vector_type(2))) __bf16 bf16x2;
typedef __attribute__((ext_vector_type(8))) __bf16 bf16x8;
typedef __attribute__((ext_vector_type(16))) __bf16 bf16x16;
typedef __attribute__((ext_vector_type(16))) float f32x16;
typedef __attribute__((ext_vector_type(8))) float f32x8;
typedef __attribute__((ext_vector_type(4))) float f32x4;
typedef __attribute__((ext_vector_type(2))) float f32x2;
typedef __attribute__((ext_vector_type(4))) unsigned u32x4;
typedef __attribute__((ext_vector_type(2))) unsigned u32x2;
typedef unsigned long long u64;
#define LAS __attribute__((address_space(3)))
#define GAS __attribute__((address_space(1)))
typedef GAS u64 gu64;
#define DI __device__ __forceinline__
#define RLX_AGENT __ATOMIC_RELAXED, __HIP_MEMORY_SCOPE_AGENT

constexpr int D_ = 512, F_ = 1376;
constexpr int KS_F = F_ / 16;

constexpr unsigned MB = 1u << 20;
constexpr unsigned WS_FLAG = 0;
constexpr unsigned WS_WQKV = 1024 * 1024;
constexpr unsigned WS_WO = WS_WQKV + 48 * 32 * 1024;
constexpr unsigned WS_W1 = WS_WO + 16 * 32 * 1024;
constexpr unsigned WS_W2 = WS_W1 + 43 * 32 * 1024;
constexpr unsigned WS_CSP = WS_W2 + 16 * 86 * 1024;
constexpr unsigned WS_ST = WS_CSP + 2 * 16 * 32 * 384;
constexpr unsigned WS_X = WS_ST + 4 * 64 * 4096;
constexpr unsigned WS_L = WS_X + 8 * MB;
constexpr unsigned L_Q = 0, L_K = 2 * MB, L_V = 4 * MB, L_O = 6 * MB, L_H = 8 * MB, L_SZ = 14 * MB;
constexpr unsigned WS_END = WS_L + 2 * L_SZ;
constexpr int FL_X = 1;
constexpr int FL_H = FL_X + 1024;
constexpr int FL_Q = FL_H + 512;
constexpr int FL_O = FL_Q + 512;
constexpr int FL_WQ = FL_O + 512;
constexpr int FL_W1 = FL_WQ + 256;
constexpr int FL_W2 = FL_W1 + 16;
static_assert((FL_W2 + 8) * 256 <= WS_WQKV, "flag region");

constexpr int LD_W = 0;
constexpr int LD_V = 65536;
constexpr int LD_EX = 98304;
constexpr int LD_ONE = 131072;
constexpr int LD_CMB = 135168;
constexpr int LD_W2E = 122880;
constexpr int LD_SC = 151552;
constexpr int LD_ST = 155648;
constexpr int LD_PAR = 157696;
constexpr int LD_CTR = LD_PAR + 1536;
constexpr int LDS_BYTES = 163840;

#define MFMA(a, b, c) __builtin_amdgcn_mfma_f32_32x32x16_bf16((a), (b), (c), 0, 0, 0)
#define SBAR() __builtin_amdgcn_sched_barrier(0)
#define PIN(x) asm volatile("" : "+v"(x))
#define BARRIER_LDS() asm volatile("s_waitcnt lgkmcnt(0)\n\ts_barrier" ::: "memory")
#define BARRIER_ALL() asm volatile("s_waitcnt vmcnt(0) lgkmcnt(0)\n\ts_barrier" ::: "memory")

DI unsigned pk2(float lo, float hi) {
  bf16x2 v; v[0] = (__bf16)lo; v[1] = (__bf16)hi;
  return __builtin_bit_cast(unsigned, v);
}
DI float bf_lo(unsigned u) { return __uint_as_float(u << 16); }
DI float bf_hi(unsigned u) { return __uint_as_float(u & 0xffff0000u); }
DI float rbf(float x) { return (float)(__bf16)x; }
DI bf16x8 as_bf(u32x4 v) { return __builtin_bit_cast(bf16x8, v); }
DI float swap_half_sum(float x) {
  auto r = __builtin_amdgcn_permlane32_swap(__float_as_uint(x), __float_as_uint(x), false, false);
  return __uint_as_float(r[0]) + __uint_as_float(r[1]);
}
DI float swap_half_max(float x) {
  auto r = __builtin_amdgcn_permlane32_swap(__float_as_uint(x), __float_as_uint(x), false, false);
  return fmaxf(__uint_as_float(r[0]), __uint_as_float(r[1]));
}
DI void glds16x2(const char* sbase, unsigned voff, unsigned lds_base) {
  unsigned sv;
  asm volatile("s_mov_b32 %0, m0\n\ts_mov_b32 m0, %3\n\ts_nop 2\n\tglobal_load_lds_dwordx4 %1, %2 sc1\n\t"
               "global_load_lds_dwordx4 %1, %2 offset:1024 sc1\n\ts_mov_b32 m0, %0"
               : "=&s"(sv) : "v"(voff), "s"(sbase), "s"(lds_base) : "memory");
}
DI void glds16x4(const char* sbase, unsigned voff, unsigned lds_base) {
  unsigned sv;
  asm volatile("s_mov_b32 %0, m0\n\ts_mov_b32 m0, %3\n\ts_nop 2\n\tglobal_load_lds_dwordx4 %1, %2 sc1\n\t"
               "global_load_lds_dwordx4 %1, %2 offset:1024 sc1\n\tglobal_load_lds_dwordx4 %1, %2 offset:2048 sc1\n\t"
               "global_load_lds_dwordx4 %1, %2 offset:3072 sc1\n\ts_mov_b32 m0, %0"
               : "=&s"(sv) : "v"(voff), "s"(sbase), "s"(lds_base) : "memory");
}
template <int I> struct IC { static constexpr int v = I; };
template <int I, int N, class F> DI void static_for(F&& f) {
  if constexpr (I < N) { f(IC<I>{}); static_for<I + 1, N>(f); }
}

struct Ctx {
  char* ws; LAS char* lds; __amdgpu_buffer_rsrc_t rs;
  u64 token; unsigned lbase, voff;
  int bid, lane, w, r, h, tb, nb, tb64, nb8, head, qb;
  bool dead;
};
DI void fresh(Ctx& c) {
  asm volatile("" : "+s"(c.bid));
  asm volatile("" : "+s"(c.w));
  asm volatile("" : "+v"(c.lane));
  const int g = c.bid & 7, idx = c.bid >> 3;
  c.r = c.lane & 31; c.h = c.lane >> 5; c.voff = c.lane * 16;
  c.tb = 2 * g + (idx >> 4); c.nb = idx & 15; c.tb64 = 4 * g + (idx >> 3); c.nb8 = idx & 7;
  c.head = 4 * g + (idx & 3); c.qb = idx >> 2;
}
DI u32x4 ld16(const Ctx& c, unsigned voff, unsigned soff) { return __builtin_amdgcn_raw_buffer_load_b128(c.rs, voff, soff, 16); }
DI float ld4(const Ctx& c, unsigned voff, unsigned soff) { return __uint_as_float(__builtin_amdgcn_raw_buffer_load_b32(c.rs, voff, soff, 16)); }
DI void st16(const Ctx& c, u32x4 v, unsigned voff, unsigned soff) { __builtin_amdgcn_raw_buffer_store_b128(v, c.rs, voff + soff, 0, 16); }
DI void st4(const Ctx& c, float v, unsigned voff, unsigned soff) { __builtin_amdgcn_raw_buffer_store_b32(__float_as_uint(v), c.rs, voff, soff, 16); }
DI gu64* flag_ptr(const Ctx& c, int line, int slot) { return (gu64*)(c.ws + WS_FLAG) + line * 32 + slot; }

DI void publish(const Ctx& c, int ctr, unsigned n, int line0, int slot) {
  asm volatile("s_waitcnt vmcnt(0)" ::: "memory");
  unsigned old = 0;
  if (c.lane == 0) old = __hip_atomic_fetch_add((LAS unsigned*)(c.lds + LD_CTR) + ctr, 1u, __ATOMIC_RELAXED, __HIP_MEMORY_SCOPE_WORKGROUP);
  if (__builtin_amdgcn_readfirstlane(old) == n - 1 && c.lane < 16) __hip_atomic_store(flag_ptr(c, line0 + c.lane, slot), c.token, RLX_AGENT);
}
DI void poll_any(Ctx& c, int line, int slot) {
  if (!c.dead) {
    gu64* p = flag_ptr(c, line, slot);
    unsigned spins = 0;
    while (!__all(__hip_atomic_load(p, RLX_AGENT) == c.token)) {
      if (++spins > (1u << 18)) { c.dead = true; break; }
    }
    __builtin_amdgcn_fence(__ATOMIC_ACQUIRE, "wavefront");
  }
}
template <bool NAP = false> DI void wait4(const Ctx& c, int ctr) {
  while (__hip_atomic_load((LAS unsigned*)(c.lds + LD_CTR) + ctr, __ATOMIC_RELAXED, __HIP_MEMORY_SCOPE_WORKGROUP) < 4u)
    if (NAP) __builtin_amdgcn_s_sleep(2);
  asm volatile("" ::: "memory");
}
DI void arrive4(const Ctx& c, int ctr) {
  asm volatile("s_waitcnt lgkmcnt(0)" ::: "memory");
  if (c.lane == 0) __hip_atomic_fetch_add((LAS unsigned*)(c.lds + LD_CTR) + ctr, 1u, __ATOMIC_RELAXED, __HIP_MEMORY_SCOPE_WORKGROUP);
}
template <int NAP> DI void poll(Ctx& c, int line, int slot) {
  if (c.w == 0) {
    __builtin_amdgcn_s_sleep(NAP);
    poll_any(c, line, slot);
  }
}
template <int NQ> DI void help_dma(const Ctx& c, unsigned src, int dst) {
#pragma unroll
  for (int u = 0; u < NQ / 4; ++u) {
    const int p = (c.w & 3) + 4 * u;
    glds16x4(c.ws + src + p * 4096, c.voff, c.lbase + dst + p * 4096);
  }
}
DI int w2_run(int run) { return run < 6 ? LD_W + run * 14336 : LD_W2E + (run - 6) * 14336; }
template <int U0, int U1> DI void w2_fetch(const Ctx& c, u32x4 (&h)[28]) {
#pragma unroll
  for (int u = U0; u < U1; ++u) {
    const int f = (c.w & 3) + 4 * u, run = f / 14, kq = run >> 1;
    h[u] = ld16(c, c.voff, WS_W2 + ((2 * c.nb8 + (run & 1)) * KS_F + kq * 21 + (kq < 2 ? kq : 2) + f % 14) * 1024);
  }
  SBAR();
}
template <int U0, int U1> DI void w2_put(const Ctx& c, const u32x4 (&h)[28]) {
#pragma unroll
  for (int u = U0; u < U1; ++u) {
    const int f = (c.w & 3) + 4 * u;
    *(LAS u32x4*)(c.lds + (u < 21 ? LD_W : LD_W2E - 84 * 1024) + f * 1024 + c.voff) = h[u];
  }
}

DI f32x16 unpack16(const u32x4 (&res)[2]) {
  f32x16 v;
#pragma unroll
  for (int i = 0; i < 8; ++i) { const unsigned u = res[i >> 2][i & 3]; v[2 * i] = bf_lo(u); v[2 * i + 1] = bf_hi(u); }
  return v;
}
DI void pack16(const f32x16& v, u32x4 (&res)[2]) {
#pragma unroll
  for (int i = 0; i < 8; ++i) res[i >> 2][i & 3] = pk2(v[2 * i], v[2 * i + 1]);
}
DI void store_x(const Ctx& c, int inst, u32x4 (&res)[2], const f32x16& v) {
  const int tt = 2 * c.tb64 + (c.w >> 1), nt = 2 * c.nb8 + (c.w & 1);
  st16(c, res[0], c.voff, WS_X + inst * (2 * MB) + (tt * 32 + 2 * nt) * 1024);
  st16(c, res[1], c.voff, WS_X + inst * (2 * MB) + (tt * 32 + 2 * nt + 1) * 1024);
  SBAR();
  f32x2 a1 = {v[0], v[1]}, a2 = a1 * a1;
#pragma unroll
  for (int i = 1; i < 8; ++i) { const f32x2 t = {v[2 * i], v[2 * i + 1]}; a1 += t; a2 += t * t; }
  auto r1 = __builtin_amdgcn_permlane32_swap(__float_as_uint(a1[0] + a1[1]), __float_as_uint(a2[0] + a2[1]), false, false);
  st4(c, __uint_as_float(r1[0]) + __uint_as_float(r1[1]), c.lane * 4, WS_ST + inst * (64 * 4096) + tt * 4096 + nt * 256);
  PIN(res[0]); PIN(res[1]);
}

DI void wfrag_load(const unsigned short* W, int N, int nt, int ks, int r, int h, unsigned (&e)[8]) {
#pragma unroll
  for (int j = 0; j < 8; ++j) e[j] = W[(size_t)(16 * ks + 8 * (j >> 2) + 4 * h + (j & 3)) * N + 32 * nt + r];
}
DI void wfrag_store(const Ctx& c, const unsigned (&e)[8], unsigned dst, bool docs, unsigned csdst) {
  u32x4 v;
#pragma unroll
  for (int j = 0; j < 4; ++j) v[j] = e[2 * j] | (e[2 * j + 1] << 16);
  st16(c, v, c.voff, dst);
  if (docs) {
    float s = 0.f;
#pragma unroll
    for (int j = 0; j < 8; ++j) s += __uint_as_float(e[j] << 16);
    s = swap_half_sum(s);
    if (c.h == 0) st4(c, s, c.r * 4, csdst);
  }
}

DI u32x4 mish8(const f32x8& a, float rs) {
  float v[8];
#pragma unroll
  for (int j = 0; j < 8; ++j) {
    const float u = __builtin_amdgcn_exp2f(a[j] * (rs * 1.4426950408889634f));
    v[j] = a[j] * fmaf(-2.f * rs, __builtin_amdgcn_rcpf(fmaf(u, u + 2.f, 2.f)), rs);
  }
  return (u32x4){pk2(v[0], v[1]), pk2(v[2], v[3]), pk2(v[4], v[5]), pk2(v[6], v[7])};
}
#define LO8(s) __builtin_shufflevector(s, s, 0, 1, 2, 3, 4, 5, 6, 7)
#define HI8(s) __builtin_shufflevector(s, s, 8, 9, 10, 11, 12, 13, 14, 15)
DI f32x8 cat8(f32x4 a, f32x4 b) { return __builtin_shufflevector(a, b, 0, 1, 2, 3, 4, 5, 6, 7); }

template <int T, int PARTS = 3>
DI void ln_tile(const Ctx& c, int l, const f32x8& lo, const f32x8& hi, float rstd) {
  const int tt = 4 * c.tb + (c.w & 3);
  const unsigned so = WS_L + l * L_SZ + L_H + (tt * KS_F + 2 * (c.nb < 11 ? 3 * c.nb : 33 + 2 * (c.nb - 11)) + 2 * T) * 1024;
  if (PARTS & 1) st16(c, mish8(lo, rstd), c.voff, so);
  if (PARTS & 2) st16(c, mish8(hi, rstd), c.voff, so + 1024);
}
template <int MODE> DI void ln_publish(const Ctx& c, int l) {
  if (MODE == 0) publish(c, 4 + l, 4, FL_Q + l * 256 + c.nb * 16, c.tb);
  else publish(c, 6 + l, 8, FL_H + l * 256 + c.tb * 16, c.nb);
}

DI void pin16(f32x16& v) { PIN(v); }
DI void pinu(unsigned v) { asm volatile("" ::"v"(v)); }
DI u32x4 wfrag(const LAS char* base, int ks) { return *(const LAS u32x4*)(base + ks * 1024); }
DI void put16(LAS f32x4* ex, const f32x16& a) {
#pragma unroll
  for (int g = 0; g < 4; ++g) ex[g * 64] = (f32x4){a[4 * g], a[4 * g + 1], a[4 * g + 2], a[4 * g + 3]};
}

template <int MODE, bool FIRST>
DI void phase_ln(Ctx& c, int l, int inst, u32x4 (&w2r)[16]) {
  const int tt = 4 * c.tb + c.w;
  const unsigned xs = WS_X + inst * (2 * MB) + tt * 32768;
  constexpr int PD = 14;
  u32x4 xf[32], sp[4];
#pragma unroll
  for (int k = 0; k < PD; ++k) xf[k] = ld16(c, c.voff, xs + k * 1024);
  SBAR();
  const LAS char* wl = c.lds + LD_W + c.lane * 16;
  const LAS char* wl2 = wl + 65536;
  asm volatile("" : "+v"(wl2));
  f32x16 aA, aB;
#pragma unroll
  for (int i = 0; i < 16; ++i) { aA[i] = 0.f; aB[i] = 0.f; }
  u32x4 fa[3], fb[3];
  if (FIRST) BARRIER_LDS();
#pragma unroll
  for (int k = 0; k < 2; ++k) { fa[k] = wfrag(wl, k); fb[k] = wfrag(wl + 32768, k); }
  float rstd = 0.f, sc4[4];
  u32x2 we[3]; u32x4 xe;
  LAS float* sc = (LAS float*)(c.lds + LD_SC) + c.w * 256;
  const int k0q = c.w * 21 + (c.w < 2 ? c.w : 2);
  const unsigned w2s = WS_W2 + ((2 * c.nb8 + (c.w & 1)) * KS_F + k0q + 14) * 1024;
  const unsigned w2o = WS_W2 + ((2 * c.nb8 + 1 - (c.w & 1)) * KS_F + k0q + 14) * 1024;
  auto w2load = [&](int i) __attribute__((always_inline)) {
    w2r[i] = ld16(c, c.voff, ((i & 1) ? w2o : w2s) + (((i >> 1) == 7 && c.w >= 2) ? 6 : (i >> 1)) * 1024);
  };
  static_for<0, 32>([&](auto kc) __attribute__((always_inline)) {
    constexpr int k = decltype(kc)::v;
    if constexpr (FIRST && (((k & 7) == 6 && k < 24) || k == 31)) asm volatile("s_barrier" ::: "memory");
    aA = MFMA(as_bf(fa[k % 3]), as_bf(xf[k]), aA);
    pin16(aA); SBAR();
    if constexpr (k + 2 < 32) fa[(k + 2) % 3] = wfrag(wl, k + 2);
    if constexpr (k + PD < 32) xf[k + PD] = ld16(c, c.voff, xs + (k + PD) * 1024);
    if constexpr (k + PD >= 32 && k + PD < 36) sp[k + PD - 32] = ld16(c, c.voff, WS_ST + inst * (64 * 4096) + tt * 4096 + (k + PD - 32) * 1024);
    if constexpr (MODE == 1 && k >= 24) w2load(k - 24);
    SBAR();
    aB = MFMA(as_bf(fb[k % 3]), as_bf(xf[k]), aB);
    pin16(aB); SBAR();
    if constexpr (k + 2 < 32) fb[(k + 2) % 3] = wfrag(wl + 32768, k + 2);
    if constexpr (k == 29) {
      ((LAS f32x4*)sc)[c.lane] = (__builtin_bit_cast(f32x4, sp[0]) + __builtin_bit_cast(f32x4, sp[1])) +
                                 (__builtin_bit_cast(f32x4, sp[2]) + __builtin_bit_cast(f32x4, sp[3]));
#pragma unroll
      for (int i = 0; i < 4; ++i) sc4[i] = sc[i * 64 + c.lane];
    }
    if constexpr (k == (FIRST ? 31 : 30)) {
#pragma unroll
      for (int i = 0; i < 3; ++i) we[i] = ((const LAS u32x2*)(c.lds + LD_PAR))[MODE * 96 + 32 * i + c.r];
    }
    if constexpr (k == 31) {
      const float s = (sc4[0] + sc4[1]) + (sc4[2] + sc4[3]);
      auto rr = __builtin_amdgcn_permlane32_swap(__float_as_uint(s), __float_as_uint(s), false, false);
      const float mu = __uint_as_float(rr[0]) * (1.f / D_);
      const float var = fmaxf(__uint_as_float(rr[1]) * (1.f / D_) - mu * mu, 0.f);
      const float sd = rbf(__builtin_amdgcn_sqrtf(rbf(rbf(var) + 1e-5f)));
      rstd = __builtin_amdgcn_rcpf(sd);
      ((LAS float*)(c.lds + LD_ST))[c.w * 32 + c.r] = rstd;
      xe = (u32x4){c.h ? 0u : pk2(-mu, -mu), c.h ? 0u : pk2(-mu, sd), 0u, 0u};
    }
    SBAR();
  });
  aB = MFMA(as_bf((u32x4){we[1][0], we[1][1], 0u, 0u}), as_bf(xe), aB);
  aA = MFMA(as_bf((u32x4){we[0][0], we[0][1], 0u, 0u}), as_bf(xe), aA);
  f32x16 aC;
#pragma unroll
  for (int i = 0; i < 16; ++i) aC[i] = 0.f;
  aC = MODE == 0 ? MFMA(as_bf(xe), as_bf((u32x4){we[2][0], we[2][1], 0u, 0u}), aC) : MFMA(as_bf((u32x4){we[2][0], we[2][1], 0u, 0u}), as_bf(xe), aC);
  constexpr int CD = 5;
  u32x4 fc[CD + 1];
#pragma unroll
  for (int k = 0; k < CD; ++k) fc[k] = wfrag(wl2, k);
  SBAR();
  if (MODE == 1) {
    LAS f32x4* ex = (LAS f32x4*)(c.lds + LD_EX) + c.w * 384 + c.lane;
    put16(ex, aB);
    ex[256] = (f32x4){aA[8], aA[9], aA[10], aA[11]}; ex[320] = (f32x4){aA[12], aA[13], aA[14], aA[15]};
    BARRIER_LDS();
  }
  f32x4 ts[4];
  u32x4 pw;
  const float rq = rstd * (0.25f * 1.4426950408889634f);
  const unsigned vok = c.h * 512 + ((c.r & 19) | ((c.r & 4) << 1) | ((c.r & 8) >> 1)) * 16;
  const unsigned soq = WS_L + l * L_SZ + L_Q + (2 * c.nb * 64 + tt) * 1024;
  static_for<0, 32>([&](auto kc) __attribute__((always_inline)) {
    constexpr int k = decltype(kc)::v;
    aC = MODE == 0 ? MFMA(as_bf(xf[k]), as_bf(fc[k % (CD + 1)]), aC) : MFMA(as_bf(fc[k % (CD + 1)]), as_bf(xf[k]), aC);
    pin16(aC); SBAR();
    if constexpr (k + CD < 32) fc[(k + CD) % (CD + 1)] = wfrag(wl2, k + CD);
    if constexpr (MODE == 1 && (k & 1) == 0 && 8 + k / 2 < 16) w2load(8 + k / 2);
    if constexpr (MODE == 0 && k >= 6 && k <= 18 && (k & 3) == 2)
      st16(c, pw, k < 12 ? vok : c.voff, (k < 12 ? soq + (L_K - L_Q) : soq) + ((k & 4) ? 0 : 65536));
    if constexpr (MODE == 0 && k >= 2 && k < 18) {
      constexpr int i = k - 2;
      const f32x16& a = i < 8 ? aB : aA;
      const float rs = i < 8 ? rstd : rq;
      pw[i & 3] = pk2(a[2 * (i & 7)] * rs, a[2 * (i & 7) + 1] * rs);
      pinu(pw[i & 3]);
    }
    if constexpr (MODE == 0 && k == 29) {
#pragma unroll
      for (int g = 0; g < 4; ++g) ts[g] = *(const LAS f32x4*)((const LAS float*)(c.lds + LD_ST) + c.w * 32 + 8 * g + 4 * c.h);
    }
    SBAR();
  });
  if (MODE == 1) arrive4(c, 14 + l);
  if (MODE == 1) { ln_tile<0, 1>(c, l, LO8(aA), LO8(aA), rstd); if (c.nb < 11) ln_tile<2>(c, l, LO8(aC), HI8(aC), rstd); }
  else {
    unsigned q[8];
#pragma unroll
    for (int g = 0; g < 8; ++g) q[g] = pk2(aC[2 * g] * ts[g >> 1][2 * (g & 1)], aC[2 * g + 1] * ts[g >> 1][2 * (g & 1) + 1]);
    const unsigned vo = (c.r >> 4) * 65536 + c.h * 256 + (c.r & 15) * 16, so = WS_L + l * L_SZ + L_V + (2 * c.nb * 128 + 2 * tt) * 512;
    st16(c, (u32x4){q[0], q[1], q[2], q[3]}, vo, so);
    st16(c, (u32x4){q[4], q[5], q[6], q[7]}, vo, so + 512);
  }
  ln_publish<MODE>(c, l);
}
template <class F>
DI void help_ln(Ctx& c, int l, F&& mid) {
  BARRIER_LDS();
  const LAS f32x4* ex = (const LAS f32x4*)(c.lds + LD_EX) + (c.w & 3) * 384 + c.lane;
  const float rstd = ((const LAS float*)(c.lds + LD_ST))[(c.w & 3) * 32 + c.r];
  const f32x8 b0 = cat8(ex[0], ex[64]), b1 = cat8(ex[128], ex[192]), a1 = cat8(ex[256], ex[320]);
  SBAR();
  ln_tile<1>(c, l, b0, b1, rstd);
  ln_tile<0, 2>(c, l, a1, a1, rstd);
  SBAR();
  mid();
  ln_publish<1>(c, l);
}
DI void colsum_load(const Ctx& c, int mode, float (&v)[32]) {
  const int t = min((c.w - 4) * 64 + c.lane, 95);
#pragma unroll
  for (int i = 0; i < 32; ++i) v[i] = ld4(c, t * 4, WS_CSP + ((mode * 16 + c.nb) * 32 + i) * 384);
  SBAR();
}
DI void colsum_put(const Ctx& c, int mode, const float (&v)[32], unsigned bias) {
  const int t = (c.w - 4) * 64 + c.lane;
  float s = 0.f;
#pragma unroll
  for (int i = 0; i < 32; i += 4) s += (v[i] + v[i + 1]) + (v[i + 2] + v[i + 3]);
  const float s0 = rbf(s), s1 = rbf(s - s0);
  if (t < 96) ((LAS u32x2*)(c.lds + LD_PAR))[mode * 96 + t] = (u32x2){pk2(s0, s1), (pk2(s - s0 - s1, 0.f) & 0xffffu) | (bias << 16)};
}

DI f32x16 reduce_res(Ctx& c, const f32x16 (&a)[4], u32x4 (&res)[2], int ctr) {
  LAS f32x4* red = (LAS f32x4*)(c.lds + LD_EX) + c.lane;
#pragma unroll
  for (int j = 1; j < 4; ++j) {
    LAS f32x4* p = red + ((c.w ^ j) * 3 + j - 1) * 256;
#pragma unroll
    for (int g = 0; g < 4; ++g) p[g * 64] = (f32x4){a[j][4 * g], a[j][4 * g + 1], a[j][4 * g + 2], a[j][4 * g + 3]};
  }
  if (ctr >= 0) { arrive4(c, ctr); wait4(c, ctr); } else BARRIER_LDS();
  const LAS f32x4* q = red + c.w * 768;
  f32x16 v;
#pragma unroll
  for (int g = 0; g < 4; ++g) {
    const f32x4 s = ((f32x4){a[0][4 * g], a[0][4 * g + 1], a[0][4 * g + 2], a[0][4 * g + 3]} + q[g * 64]) + (q[256 + g * 64] + q[512 + g * 64]);
#pragma unroll
    for (int j = 0; j < 4; ++j) v[4 * g + j] = s[j];
  }
  pack16(v, res);
  return v;
}
#define MM4(w0, w1, x0, x1) do { a[0] = MFMA(as_bf(w0), as_bf(x0), a[0]); a[1] = MFMA(as_bf(w1), as_bf(x0), a[1]); \
                                 a[2] = MFMA(as_bf(w0), as_bf(x1), a[2]); a[3] = MFMA(as_bf(w1), as_bf(x1), a[3]); } while (0)

DI f32x16 phase_wo(Ctx& c, int l, const u32x4 (&wo)[16], u32x4 (&res)[2]) {
  const unsigned xs = WS_L + l * L_SZ + L_O + ((2 * c.tb64 + (c.w >> 1)) * 32 + 8 * c.w) * 1024;
  const unsigned xo = WS_L + l * L_SZ + L_O + ((2 * c.tb64 + 1 - (c.w >> 1)) * 32 + 8 * c.w) * 1024;
  u32x4 xf[16];
#pragma unroll
  for (int k = 0; k < 8; ++k) { xf[2 * k] = ld16(c, c.voff, xs + k * 1024); xf[2 * k + 1] = ld16(c, c.voff, xo + k * 1024); }
  asm volatile("s_barrier" ::: "memory");
  SBAR();
  f32x16 a[4];
#pragma unroll
  for (int i = 16; i < 64; ++i) a[i >> 4][i & 15] = 0.f;
  a[0] = unpack16(res);
#pragma unroll
  for (int k = 0; k < 8; ++k) { MM4(wo[2 * k], wo[2 * k + 1], xf[2 * k], xf[2 * k + 1]); PIN(a[0]); PIN(a[1]); PIN(a[2]); PIN(a[3]); SBAR(); }
  return reduce_res(c, a, res, -1);
}

DI f32x16 phase_w2(Ctx& c, int l, const u32x4 (&w2r)[16], u32x4 (&res)[2]) {
  const int k0 = c.w * 21 + (c.w < 2 ? c.w : 2);
  const unsigned xs = WS_L + l * L_SZ + L_H + ((2 * c.tb64 + (c.w >> 1)) * KS_F + k0) * 1024;
  const unsigned xo = WS_L + l * L_SZ + L_H + ((2 * c.tb64 + 1 - (c.w >> 1)) * KS_F + k0) * 1024;
  constexpr int WIN = 10;
  u32x4 xf[2 * WIN];
#pragma unroll
  for (int k = 0; k < WIN; ++k) { xf[2 * k] = ld16(c, c.voff, xs + k * 1024); xf[2 * k + 1] = ld16(c, c.voff, xo + k * 1024); }
  SBAR();
  const LAS u32x4* w0 = (const LAS u32x4*)(c.lds + w2_run(2 * c.w + (c.w & 1))) + c.lane;
  const LAS u32x4* w1 = (const LAS u32x4*)(c.lds + w2_run(2 * c.w + 1 - (c.w & 1))) + c.lane;
  f32x16 a[4];
#pragma unroll
  for (int i = 16; i < 64; ++i) a[i >> 4][i & 15] = 0.f;
  a[0] = unpack16(res);
  u32x4 f0[2], f1[2];
  f0[0] = w0[0]; f1[0] = w1[0];
  static_for<0, 22>([&](auto kc) __attribute__((always_inline)) {
    constexpr int k = decltype(kc)::v;
    const u32x4 g0 = k < 14 ? f0[k & 1] : w2r[k < 14 ? 0 : 2 * (k - 14)], g1 = k < 14 ? f1[k & 1] : w2r[k < 14 ? 0 : 2 * (k - 14) + 1];
    const u32x4 x0 = xf[2 * (k % WIN)], x1 = xf[2 * (k % WIN) + 1];
    constexpr int kn = k - 1 + WIN;
    const int kk = (kn == 21 && c.w >= 2) ? 20 : kn;
    if constexpr (kn == 22) asm volatile("s_barrier" ::: "memory");
    if (k < 21 || c.w < 2) {
      a[0] = MFMA(as_bf(g0), as_bf(x0), a[0]);
      pin16(a[0]); SBAR();
      if constexpr (k >= 1 && kn < 22) xf[2 * ((k - 1) % WIN)] = ld16(c, c.voff, xs + kk * 1024);
      SBAR();
      a[1] = MFMA(as_bf(g1), as_bf(x0), a[1]);
      pin16(a[1]); SBAR();
      if constexpr (k + 1 < 14) f0[(k + 1) & 1] = w0[(k + 1) * 64];
      SBAR();
      a[2] = MFMA(as_bf(g0), as_bf(x1), a[2]);
      pin16(a[2]); SBAR();
      if constexpr (k >= 1 && kn < 22) xf[2 * ((k - 1) % WIN) + 1] = ld16(c, c.voff, xo + kk * 1024);
      SBAR();
      a[3] = MFMA(as_bf(g1), as_bf(x1), a[3]);
      pin16(a[3]); SBAR();
      if constexpr (k + 1 < 14) f1[(k + 1) & 1] = w1[(k + 1) * 64];
      SBAR();
    }
  });
  if (l == 0) { arrive4(c, 16); wait4(c, 16); } else BARRIER_LDS();
  return reduce_res(c, a, res, l == 0 ? 17 : -1);
}

#define SMF(a, b8, c) __builtin_amdgcn_smfmac_f32_32x32x32_bf16((a), __builtin_bit_cast(bf16x16, b8), (c), sidx, 0, 0)
#define LDF(p) (*(const LAS bf16x8*)(p))
DI float max16(const f32x16& s) {
  float a = fmaxf(fmaxf(s[0], s[1]), s[2]), b = fmaxf(fmaxf(s[3], s[4]), s[5]);
  a = fmaxf(fmaxf(a, s[6]), s[7]); b = fmaxf(fmaxf(b, s[8]), s[9]);
  a = fmaxf(fmaxf(a, s[10]), s[11]); b = fmaxf(fmaxf(b, s[12]), s[13]);
  a = fmaxf(fmaxf(a, s[14]), s[15]);
  return swap_half_max(fmaxf(a, b));
}
DI void attn_slow(const LAS char* kp, const LAS char* vp, bool isv, bf16x8 q, f32x16& o, float& m) {
  const int sidx = 0xDDDDDDDD;
  f32x16 zero;
#pragma unroll
  for (int i = 0; i < 16; ++i) zero[i] = 0.f;
  o = zero; m = -3.0e38f;
  for (int t = 0; t < 32; ++t) {
    f32x16 s = MFMA(LDF(kp + t * 1024), q, zero);
    const float mn = fmaxf(m, max16(s));
    const float al = __builtin_amdgcn_exp2f(m - mn);
    m = mn;
#pragma unroll
    for (int i = 0; i < 16; ++i) { o[i] *= al; s[i] = __builtin_amdgcn_exp2f(s[i] - mn); }
    const int vo = isv ? t * 1024 : 0;
    o = SMF(LDF(vp + vo), LO8(s), o);
    o = SMF(LDF(vp + vo + 512), HI8(s), o);
  }
}

struct In { const unsigned short *x, *wqkv, *wo, *w1, *b1, *w2; unsigned short* out; };
struct Jobs { unsigned e[4][8], dst[4]; bool ok[4]; };
DI void jobs_load(const Ctx& c, const In& in, Jobs& jb) {
  const int t0 = c.nb < 11 ? 3 * c.nb : 33 + 2 * (c.nb - 11), nt1 = c.nb < 11 ? 3 : 2;
  const unsigned short* W[4]; int N[4];
#pragma unroll
  for (int u = 0; u < 4; ++u) {
    const int j = c.w + 4 * u;
    int nt, ks;
    if (j < 6) { W[u] = in.w1; N[u] = F_; jb.ok[u] = (j >> 1) < nt1; nt = t0 + (jb.ok[u] ? j >> 1 : 0); ks = 2 * c.tb + (j & 1); jb.dst[u] = WS_W1 + (nt * 32 + ks) * 1024; }
    else if (j < 8) { W[u] = in.wo; N[u] = D_; jb.ok[u] = true; nt = 2 * c.nb8 + (j & 1); ks = c.tb64; jb.dst[u] = WS_WO + (nt * 32 + ks) * 1024; }
    else { W[u] = in.w2; N[u] = D_; ks = c.tb64 + 32 * ((j - 8) >> 1); jb.ok[u] = ks < KS_F; if (!jb.ok[u]) ks = 0; nt = 2 * c.nb8 + (j & 1); jb.dst[u] = WS_W2 + (nt * KS_F + ks) * 1024; }
    W[u] += 16 * ks * N[u] + 32 * nt;
    asm volatile("" : "+s"(W[u]));
    asm volatile("" : "+s"(N[u]));
  }
#pragma unroll
  for (int u = 0; u < 4; ++u) {
    const __amdgpu_buffer_rsrc_t rw = __builtin_amdgcn_make_buffer_rsrc((void*)W[u], 0, 32 * N[u], 0x00020000);
    const unsigned o = (4 * c.h * N[u] + c.r) * 2;
#pragma unroll
    for (int j = 0; j < 8; ++j) jb.e[u][j] = __builtin_amdgcn_raw_buffer_load_b16(rw, o, (8 * (j >> 2) + (j & 3)) * 2 * N[u], 0);
  }
}
DI void jobs_put(const Ctx& c, Jobs& jb) {
#pragma unroll
  for (int i = 0; i < 32; ++i) PIN(jb.e[i >> 3][i & 7]);
#pragma unroll
  for (int u = 0; u < 4; ++u) {
    const int j = c.w + 4 * u;
    if (jb.ok[u]) wfrag_store(c, jb.e[u], jb.dst[u], j < 6, WS_CSP + ((16 + c.nb) * 32 + 2 * c.tb + (j & 1)) * 384 + (j >> 1) * 128);
  }
}
DI void jobs_publish(const Ctx& c) {
  asm volatile("s_waitcnt vmcnt(0)" ::: "memory");
  unsigned old = 0;
  if (c.lane == 0) old = __hip_atomic_fetch_add((LAS unsigned*)(c.lds + LD_CTR) + 2, 1u, __ATOMIC_RELAXED, __HIP_MEMORY_SCOPE_WORKGROUP);
  if (__builtin_amdgcn_readfirstlane(old) == 3 && c.lane < 2)
    __hip_atomic_store(c.lane ? flag_ptr(c, FL_W1 + c.nb, c.tb) : flag_ptr(c, FL_W2 + c.nb8, c.tb64), c.token, RLX_AGENT);
}

#define EX1(r) "v_exp_f32 v" #r ", v" #r "\n\t"
#define ASTEP(KOFF, VOFF, kc_, kn_, v0_, v1_, nv0_, pv1_)                                                                  \
  asm volatile("s_waitcnt lgkmcnt(0)\n\t" EX1(64) EX1(65)                                                                  \
               "v_smfmac_f32_32x32x32_bf16 %[o1], %[pv1], v[88:95], %[idx]\n\t"                                            \
               "ds_read_b128 %[nv0], %[vp] offset:%[vo]\n\tds_read_b128 %[pv1], %[vp] offset:%[vo1]\n\t"                   \
               EX1(66) EX1(67) EX1(68)                                                                                     \
               "v_mfma_f32_32x32x16_bf16 v[80:95], %[kc], %[q1], %[c1]\n\t"                                                \
               "ds_read_b128 %[kc], %[kp] offset:%[ko]\n\t"                                                                \
               EX1(69) EX1(70) EX1(71) EX1(72) EX1(73)                                                                     \
               "v_smfmac_f32_32x32x32_bf16 %[o0], %[v0], v[64:71], %[idx]\n\t"                                             \
               EX1(74) EX1(75) EX1(76) EX1(77) EX1(78) EX1(79) EX1(80) EX1(81)                                             \
               "v_smfmac_f32_32x32x32_bf16 %[o0], %[v1], v[72:79], %[idx]\n\t"                                             \
               EX1(82) EX1(83) EX1(84)                                                                                     \
               "v_mfma_f32_32x32x16_bf16 v[64:79], %[kn], %[q0], %[c0]\n\t"                                                \
               EX1(85) EX1(86) EX1(87) EX1(88) EX1(89)                                                                     \
               "v_smfmac_f32_32x32x32_bf16 %[o1], %[v0], v[80:87], %[idx]\n\t"                                             \
               EX1(90) EX1(91) EX1(92) EX1(93) EX1(94) "v_exp_f32 v95, v95"                                                \
               : "+{v[64:79]}"(sA), "+{v[80:95]}"(sB), [o0] "+v"(o0), [o1] "+v"(o1), [kc] "+v"(kc_), [nv0] "=&v"(nv0_),    \
                 [pv1] "+v"(pv1_)                                                                                          \
               : [kn] "v"(kn_), [v0] "v"(v0_), [v1] "v"(v1_), [q0] "v"(q0), [q1] "v"(q1), [c0] "v"(cin0), [c1] "v"(cin1),  \
                 [idx] "v"(sidx), [kp] "v"(kpa), [vp] "v"(vpa), [ko] "n"(KOFF), [vo] "n"(VOFF), [vo1] "n"((VOFF) + 512)     \
               : "memory")

template <int half, int l>
DI void phase_attn(Ctx& c, const In& in, u32x4 (&wo)[16], u32x4 (&hw)[28]) {
  const int qt = c.qb * 8 + (c.w & 3) * 2;
  const unsigned lay = WS_L + l * L_SZ;
  const char* kvg = c.ws + lay + ((c.w & 1) ? L_V : L_K) + c.head * 65536 + ((c.w >> 1) & 1) * 32768 + half * 4096;
  const unsigned kvd = c.lbase + ((c.w & 1) ? LD_V : LD_W) + ((c.w >> 1) & 1) * 32768 + half * 4096;
  u32x4 qv0, qv1;
  asm volatile("s_nop 4\n\tglobal_load_dwordx4 %0, %2, %3 sc1\n\tglobal_load_dwordx4 %1, %2, %3 offset:1024 sc1"
               : "=&v"(qv0), "=&v"(qv1) : "v"(c.voff), "s"(c.ws + lay + L_Q + (c.head * 64 + qt) * 1024) : "memory");
#define KV4(u) glds16x4(kvg + (u) * 8192, c.voff, kvd + (u) * 8192)
  glds16x4(kvg, c.voff, kvd);
  if (half == 0) {
    const unsigned o1 = (c.lane & 11) == 0 ? 0x3f803f80u : 0u;
    ((LAS u32x4*)(c.lds + LD_ONE))[c.w * 64 + c.lane] = (u32x4){o1, o1, o1, o1};
  }
  int sidx = 0xDDDDDDDD;
  const bool isv = c.r < 16;
  const LAS char* kp0 = c.lds + LD_W + half * 32768 + c.lane * 16;
  const LAS char* vp0 = c.lds + (isv ? LD_V + half * 32768 : LD_ONE) + c.h * 256 + (c.r & 15) * 16;
  const int vstep = isv ? 4096 : 0;
  if (half == 0) asm volatile("s_waitcnt vmcnt(0) lgkmcnt(0)\n\ts_barrier" : "+v"(qv0), "+v"(qv1)::"memory");
  else asm volatile("s_waitcnt vmcnt(4)\n\ts_barrier" : "+v"(qv0), "+v"(qv1)::"memory");
  if (half == 0) __builtin_amdgcn_s_setprio(1);
  const bf16x8 q0 = as_bf(qv0), q1 = as_bf(qv1);
  f32x16 zero;
#pragma unroll
  for (int i = 0; i < 16; ++i) zero[i] = 0.f;
  f32x16 cin0, cin1, o0 = zero, o1 = zero, sA, sB = zero;
  float m0, m1;
  bf16x8 ka = LDF(kp0), kb = LDF(kp0 + 1024), va0 = LDF(vp0), va1 = LDF(vp0 + 512), vb0, vb1 = as_bf((u32x4){0u, 0u, 0u, 0u});
  {
    const f32x16 s = MFMA(ka, q0, zero), s1 = MFMA(ka, q1, zero);
    m0 = max16(s); m1 = max16(s1);
#pragma unroll
    for (int i = 0; i < 16; ++i) { cin0[i] = -m0; cin1[i] = -m1; sA[i] = s[i] - m0; }
  }
  unsigned kpa = (unsigned)(size_t)kp0, vpa = (unsigned)(size_t)vp0;
  asm volatile("s_nop 1" : "+v"(cin0), "+v"(cin1), "+v"(sA), "+v"(sB), "+v"(o0), "+v"(o1), "+v"(sidx), "+v"(vb1));
#define QUAD(A, B, MID, C, D)                                                        \
  do {                                                                               \
    ASTEP(2048, 1024, ka, kb, va0, va1, vb0, vb1); A;                                \
    ASTEP(3072, 2048, kb, ka, vb0, vb1, va0, va1); B; MID;                           \
    ASTEP(4096, 3072, ka, kb, va0, va1, vb0, vb1); C;                                \
    vpa += vstep;                                                                    \
    ASTEP(5120, 0, kb, ka, vb0, vb1, va0, va1); D;                                   \
    kpa += 4096;                                                                      \
  } while (0)
#define NOP (void)0
#define WBAR(n) asm volatile("s_waitcnt vmcnt(" #n ")\n\ts_barrier" ::: "memory")
  constexpr bool JOBS = half == 0 && l == 0;
  Jobs jb;
  KV4(1);
  QUAD(KV4(2), NOP, WBAR(8), KV4(3), if (JOBS) { jobs_load(c, in, jb); SBAR(); });
  QUAD(NOP, NOP, if (JOBS) WBAR(32); else WBAR(0); if (half == 1 && l == 1) (w2_fetch<0, 21>(c, hw)), NOP,
       if (JOBS) { jobs_put(c, jb); SBAR(); });
  QUAD(if (JOBS) jobs_publish(c), NOP, NOP,
       if (half == 1 && l == 0) { poll_any(c, FL_W2 + c.nb8, c.lane & 31); (w2_fetch<0, 21>(c, hw)); }, NOP);
  for (int it = 3; it < 8; ++it) QUAD(NOP, NOP, NOP, NOP, NOP);
#undef WBAR
  asm volatile("s_nop 1\n\tv_smfmac_f32_32x32x32_bf16 %[o1], %[pv1], v[88:95], %[idx]\n\ts_waitcnt lgkmcnt(0)\n\ts_nop 7\n\ts_nop 7"
               : [o1] "+v"(o1), "+v"(o0), "+v"(ka), "+v"(kb), "+v"(va0), "+v"(va1) : [pv1] "v"(vb1), "{v[80:95]}"(sB), [idx] "v"(sidx));
#undef QUAD
#undef KV4
  if (__any(!(o0[8] < 1.0995116e12f))) attn_slow(kp0, vp0, isv, q0, o0, m0);
  if (__any(!(o1[8] < 1.0995116e12f))) attn_slow(kp0, vp0, isv, q1, o1, m1);
  LAS f32x4* cmb = (LAS f32x4*)(c.lds + LD_CMB) + (c.w & 3) * 320 + c.lane;
  const unsigned w1src = WS_W1 + (c.nb < 11 ? 3 * c.nb : 33 + 2 * (c.nb - 11)) * 32768;
  if (half == 0) {
    __builtin_amdgcn_s_setprio(0);
    cmb[0] = (f32x4){o0[0], o0[1], o0[2], o0[3]}; cmb[64] = (f32x4){o0[4], o0[5], o0[6], o0[7]};
    cmb[128] = (f32x4){o1[0], o1[1], o1[2], o1[3]}; cmb[192] = (f32x4){o1[4], o1[5], o1[6], o1[7]};
    cmb[256] = (f32x4){o0[8], m0, o1[8], m1};
    arrive4(c, 12 + l);
    if (l == 0) poll_any(c, c.lane < 32 ? FL_W1 + c.nb : FL_W2 + c.nb8, c.lane < 32 ? (c.lane & 15) : (c.lane & 31));
    const unsigned wos = WS_WO + ((2 * c.nb8 + (c.w & 1)) * 32 + 8 * c.w) * 1024;
    const unsigned woo = WS_WO + ((2 * c.nb8 + 1 - (c.w & 1)) * 32 + 8 * c.w) * 1024;
#pragma unroll
    for (int k = 0; k < 8; ++k) { wo[2 * k] = ld16(c, c.voff, wos + k * 1024); wo[2 * k + 1] = ld16(c, c.voff, woo + k * 1024); }
    SBAR();
    wait4<true>(c, 12 + l);
    help_dma<8>(c, w1src, LD_W);
    if (c.nb < 11) help_dma<8>(c, w1src + 65536, LD_W + 65536);
  }
  BARRIER_LDS();
  if (half == 1) {
    const f32x4 lm = cmb[256];
#pragma unroll
    for (int u = 0; u < 2; ++u) {
      const f32x16& o = u ? o1 : o0; const float m = u ? m1 : m0;
      const f32x8 p2 = cat8(cmb[128 * u], cmb[128 * u + 64]);
      const float m2 = lm[2 * u + 1];
      const float mm = fmaxf(m, m2);
      const float f1 = __builtin_amdgcn_exp2f(m - mm), f2 = __builtin_amdgcn_exp2f(m2 - mm);
      const float inv = __builtin_amdgcn_rcpf(o[8] * f1 + lm[2 * u] * f2);
      const float g1 = f1 * inv, g2 = f2 * inv;
      unsigned p[4];
#pragma unroll
      for (int i = 0; i < 4; ++i) p[i] = pk2(o[2 * i] * g1 + p2[2 * i] * g2, o[2 * i + 1] * g1 + p2[2 * i + 1] * g2);
      st16(c, (u32x4){p[0], p[1], p[2], p[3]}, c.voff, lay + L_O + ((qt + u) * 32 + c.head) * 1024);
    }
    asm volatile("s_waitcnt vmcnt(0)" ::: "memory");
    if (c.lane < 8) __hip_atomic_store(flag_ptr(c, FL_O + l * 256 + (c.qb * 4 + (c.w & 3)) * 8 + c.lane, c.head), c.token, RLX_AGENT);
  }
}

DI u64 uniform64(u64 v) {
  return ((u64)__builtin_amdgcn_readfirstlane((unsigned)(v >> 32)) << 32) | (unsigned)__builtin_amdgcn_readfirstlane((unsigned)v);
}

DI void run_main(Ctx& c, const In& in) {
  const u64 epoch = __hip_atomic_load((gu64*)c.ws, RLX_AGENT);
  u32x4 res[2];
  {
    if (c.w == 0 && c.lane < 32) ((LAS unsigned*)(c.lds + LD_CTR))[c.lane] = 0u;
    const unsigned short* src = in.x + (size_t)((2 * c.tb64 + (c.w >> 1)) * 32 + c.r) * D_ + 32 * (2 * c.nb8 + (c.w & 1)) + 4 * c.h;
    u32x2 xr[4];
#pragma unroll
    for (int i = 0; i < 4; ++i) xr[i] = *(const u32x2*)(src + 8 * i);
    BARRIER_LDS();
    c.token = uniform64(epoch) + 1;
    res[0] = (u32x4){xr[0][0], xr[0][1], xr[1][0], xr[1][1]}; res[1] = (u32x4){xr[2][0], xr[2][1], xr[3][0], xr[3][1]};
    store_x(c, 0, res, unpack16(res));
    publish(c, 0, 4, FL_X + c.tb * 16, c.nb);
    poll<20>(c, (c.lane & 16) ? FL_WQ + c.nb * 16 + c.tb : FL_X + c.tb * 16 + c.nb, c.lane & 15);
    BARRIER_LDS();
    u32x4 dummy[16];
    fresh(c);
    phase_ln<0, true>(c, 0, 0, dummy);
  }
  for (int l = 0; l < 2; ++l) {
    u32x4 wo[16], w2r[16];
    if (l == 1) {
      fresh(c);
      u32x4 dummy[16];
      phase_ln<0, false>(c, l, 2 * l, dummy);
    }
    fresh(c);
    poll<10>(c, FL_Q + l * 256 + c.head * 8 + c.qb, c.lane & 15);
    BARRIER_LDS();
    { u32x4 unused[28]; if (l == 0) phase_attn<0, 0>(c, in, wo, unused); else phase_attn<0, 1>(c, in, wo, unused); }
    fresh(c);
    poll<32>(c, FL_O + l * 256 + c.tb64 * 8 + c.nb8, c.lane & 31);
    BARRIER_LDS();
    store_x(c, 2 * l + 1, res, phase_wo(c, l, wo, res));
    publish(c, 8 + l, 4, FL_X + (2 * l + 1) * 256 + c.tb * 16, c.nb);
    fresh(c);
    poll<12>(c, FL_X + (2 * l + 1) * 256 + c.tb * 16 + c.nb, c.lane & 15);
    BARRIER_ALL();
    phase_ln<1, false>(c, l, 2 * l + 1, w2r);
    fresh(c);
    poll<22>(c, FL_H + l * 256 + c.tb * 16 + c.nb, c.lane & 15);
    BARRIER_LDS();
    const f32x16 x2 = phase_w2(c, l, w2r, res);
    fresh(c);
    if (l == 0) {
      store_x(c, 2, res, x2);
      publish(c, 10, 4, FL_X + 2 * 256 + c.tb * 16, c.nb);
      poll<12>(c, FL_X + 2 * 256 + c.tb * 16 + c.nb, c.lane & 15);
      BARRIER_LDS();
    }
  }
  {
    auto s0 = __builtin_amdgcn_permlane32_swap(res[0][0], res[0][2], false, false), s1 = __builtin_amdgcn_permlane32_swap(res[0][1], res[0][3], false, false);
    auto s2 = __builtin_amdgcn_permlane32_swap(res[1][0], res[1][2], false, false), s3 = __builtin_amdgcn_permlane32_swap(res[1][1], res[1][3], false, false);
    unsigned short* op = in.out + (size_t)((2 * c.tb64 + (c.w >> 1)) * 32 + c.r) * D_ + 32 * (2 * c.nb8 + (c.w & 1)) + 8 * c.h;
    __builtin_nontemporal_store((u32x4){s0[0], s1[0], s0[1], s1[1]}, (u32x4*)op);
    __builtin_nontemporal_store((u32x4){s2[0], s3[0], s2[1], s3[1]}, (u32x4*)(op + 16));
  }
  if (c.bid == 0 && c.w == 0 && c.lane == 0) __hip_atomic_store((gu64*)c.ws, c.token, RLX_AGENT);
}

DI void run_help(Ctx& c, const In& in) {
  const u64 epoch = __hip_atomic_load((gu64*)c.ws, RLX_AGENT);
  {
    unsigned e0[8], e1[8];
    const int qk = 2 * c.tb + (c.w & 1), qa = (c.w >> 1) & 1;
    wfrag_load(in.wqkv, 3 * D_, qa * 16 + c.nb, qk, c.r, c.h, e0);
    wfrag_load(in.wqkv, 3 * D_, 32 + c.nb, qk, c.r, c.h, e1);
    BARRIER_LDS();
    c.token = uniform64(epoch) + 1;
    wfrag_store(c, e0, WS_WQKV + ((qa * 16 + c.nb) * 32 + qk) * 1024, true, WS_CSP + (c.nb * 32 + qk) * 384 + qa * 128);
    if (qa == 0) wfrag_store(c, e1, WS_WQKV + ((32 + c.nb) * 32 + qk) * 1024, true, WS_CSP + (c.nb * 32 + qk) * 384 + 256);
    publish(c, 1, 4, FL_WQ + c.nb * 16, c.tb);
    BARRIER_LDS();
    fresh(c);
#define WQC(q) _Pragma("unroll") for (int t_ = 0; t_ < 3; ++t_) \
    glds16x2(c.ws + WS_WQKV + ((16 * t_ + c.nb) * 32 + 8 * (q) + 2 * (c.w & 3)) * 1024, c.voff, c.lbase + LD_W + t_ * 32768 + (8 * (q) + 2 * (c.w & 3)) * 1024)
    WQC(0); WQC(1);
    asm volatile("s_waitcnt vmcnt(6)\n\ts_barrier" ::: "memory");
    WQC(2);
    asm volatile("s_waitcnt vmcnt(6)\n\ts_barrier" ::: "memory");
    WQC(3);
    asm volatile("s_waitcnt vmcnt(6)\n\ts_barrier" ::: "memory");
    if (c.w < 6) {
      float cv[32];
      colsum_load(c, 0, cv);
      asm volatile("s_waitcnt vmcnt(32)\n\ts_barrier" ::: "memory");
      colsum_put(c, 0, cv, 0u);
    } else {
      asm volatile("s_waitcnt vmcnt(0)\n\ts_barrier" ::: "memory");
    }
    BARRIER_LDS();
#undef WQC
  }
  for (int l = 0; l < 2; ++l) {
    u32x4 wo[16];
    fresh(c);
    BARRIER_LDS();
    fresh(c);
    u32x4 hw[28];
    if (l == 0) phase_attn<1, 0>(c, in, wo, hw); else phase_attn<1, 1>(c, in, wo, hw);
    fresh(c);
    BARRIER_LDS();
    asm volatile("s_barrier" ::: "memory");
    help_dma<8>(c, WS_W1 + (c.nb < 11 ? 3 * c.nb + 1 : 34 + 2 * (c.nb - 11)) * 32768, LD_W + 32768);
    w2_fetch<21, 28>(c, hw);
    asm volatile("s_barrier" ::: "memory");
    if (l == 0 && c.w < 6) {
      const int bcol = 32 * (c.nb < 11 ? 3 * c.nb : 33 + 2 * (c.nb - 11)) + (c.w - 4) * 64 + c.lane;
      const unsigned bv = in.b1[bcol < F_ ? bcol : 0];
      float cv[32];
      colsum_load(c, 1, cv);
      colsum_put(c, 1, cv, bv);
    }
    BARRIER_ALL();
    w2_put<21, 28>(c, hw);
    help_ln(c, l, [&]() __attribute__((always_inline)) { wait4<true>(c, 14 + l); w2_put<0, 12>(c, hw); });
    w2_put<12, 21>(c, hw);
    BARRIER_LDS();
    asm volatile("s_barrier" ::: "memory");
    if (l == 0) {
      fresh(c);
#pragma unroll
      for (int u = 0; u < 24; ++u) hw[u] = ld16(c, c.voff, WS_WQKV + (((u >> 3) * 16 + c.nb) * 32 + (u & 7) * 4 + (c.w & 3)) * 1024);
      SBAR();
      wait4<true>(c, 16);
#pragma unroll
      for (int u = 0; u < 24; ++u) *(LAS u32x4*)(c.lds + LD_W + (u * 4 + (c.w & 3)) * 1024 + c.voff) = hw[u];
      BARRIER_LDS();
    } else {
      asm volatile("s_barrier\n\ts_barrier" ::: "memory");
    }
  }
}

__global__ __launch_bounds__(512, 2) void mega(const unsigned short* __restrict__ x, const unsigned short* __restrict__ wqkv,
                                               const unsigned short* __restrict__ wo_g, const unsigned short* __restrict__ w1,
                                               const unsigned short* __restrict__ b1, const unsigned short* __restrict__ w2,
                                               char* __restrict__ ws, unsigned short* __restrict__ out) {
  extern __shared__ __attribute__((aligned(16))) char lds_[];
  Ctx c;
  c.ws = ws; c.lds = (LAS char*)lds_; c.rs = __builtin_amdgcn_make_buffer_rsrc(ws, 0, WS_END, 0x00020000);
  c.lbase = (unsigned)(size_t)c.lds;
  c.bid = blockIdx.x; c.lane = threadIdx.x & 63; c.w = __builtin_amdgcn_readfirstlane(threadIdx.x >> 6); c.dead = false;
  fresh(c);
  const In in = {x, wqkv, wo_g, w1, b1, w2, out};
  if (c.w == 6) {
    const char* pc = (const char*)__builtin_amdgcn_s_getpc();
    unsigned sv;
    asm volatile("s_mov_b32 %0, m0\n\ts_mov_b32 m0, %3\n\ts_nop 2\n\tglobal_load_lds_dword %1, %2\n\ts_mov_b32 m0, %0"
                 : "=&s"(sv) : "v"(min((c.bid >> 3) * 1792 + (c.lane & 15) * 128, 56 * 1024)), "s"(pc), "s"(c.lbase + LD_SC + 256) : "memory");
  }
  if (c.w == 7) {
    const int i = c.lane & 15;
    const int line = i < 4 ? FL_X + i * 256 + c.tb * 16 + c.nb : i < 6 ? FL_H + (i & 1) * 256 + c.tb * 16 + c.nb
                   : i < 8 ? FL_Q + (i & 1) * 256 + c.head * 8 + c.qb : i < 12 ? FL_O + (i & 1) * 256 + c.tb64 * 8 + c.nb8
                   : i == 12 ? FL_WQ + c.nb * 16 + c.tb : i == 13 ? FL_W1 + c.nb : FL_W2 + c.nb8;
    unsigned sv;
    asm volatile("s_mov_b32 %0, m0\n\ts_mov_b32 m0, %3\n\ts_nop 2\n\tglobal_load_lds_dword %1, %2 sc1\n\ts_mov_b32 m0, %0"
                 : "=&s"(sv) : "v"(line * 256 + ((i == 10 || i == 11 || i == 15) ? 128 : 0)), "s"(ws), "s"(c.lbase + LD_SC) : "memory");
  }
  if (c.w < 4) run_main(c, in); else run_help(c, in);
}

extern "C" void kernel_launch(void* const* d_in, const int* in_sizes, int n_in, void* d_out, int out_size, void* d_ws,
                              size_t ws_size, hipStream_t stream) {
  (void)hipFuncSetAttribute((const void*)mega, hipFuncAttributeMaxDynamicSharedMemorySize, LDS_BYTES);
  mega<<<256, 512, LDS_BYTES, stream>>>((const unsigned short*)d_in[0], (const unsigned short*)d_in[1], (const unsigned short*)d_in[2],
                                        (const unsigned short*)d_in[4], (const unsigned short*)d_in[5], (const unsigned short*)d_in[6],
                                        (char*)d_ws, (unsigned short*)d_out);
}
```

```cpp
#include <hip/hip_runtime.h>
#include <hip/hip_bf16.h>

typedef __attribute__((ext_vector_type(2))) __bf16 bf16x2;
typedef __attribute__((ext_vector_type(8))) __bf16 bf16x8;
typedef __attribute__((ext_vector_type(16))) __bf16 bf16x16;
typedef __attribute__((ext_vector_type(16))) float f32x16;
typedef __attribute__((ext_vector_type(8))) float f32x8;
typedef __attribute__((ext_vector_type(4))) float f32x4;
typedef __attribute__((ext_vector_type(2))) float f32x2;
typedef __attribute__((ext_vector_type(4))) unsigned u32x4;
typedef __attribute__((ext_vector_type(2))) unsigned u32x2;
typedef unsigned long long u64;
#define LAS __attribute__((address_space(3)))
#define GAS __attribute__((address_space(1)))
typedef GAS u64 gu64;
#define DI __device__ __forceinline__
#define RLX_AGENT __ATOMIC_RELAXED, __HIP_MEMORY_SCOPE_AGENT

constexpr int D_ = 512, F_ = 1376;
constexpr int KS_F = F_ / 16;

constexpr unsigned MB = 1u << 20;
constexpr unsigned WS_FLAG = 0;
constexpr unsigned WS_WQKV = 1024 * 1024;
constexpr unsigned WS_WO = WS_WQKV + 48 * 32 * 1024;
constexpr unsigned WS_W1 = WS_WO + 16 * 32 * 1024;
constexpr unsigned WS_W2 = WS_W1 + 43 * 32 * 1024;
constexpr unsigned WS_CSP = WS_W2 + 16 * 86 * 1024;
constexpr unsigned WS_ST = WS_CSP + 2 * 16 * 32 * 384;
constexpr unsigned WS_X = WS_ST + 4 * 64 * 4096;
constexpr unsigned WS_L = WS_X + 8 * MB;
constexpr unsigned L_Q = 0, L_K = 2 * MB, L_V = 4 * MB, L_O = 6 * MB, L_H = 8 * MB, L_SZ = 14 * MB;
constexpr unsigned WS_END = WS_L + 2 * L_SZ;
constexpr int FL_X = 1;
constexpr int FL_H = FL_X + 1024;
constexpr int FL_Q = FL_H + 512;
constexpr int FL_O = FL_Q + 512;
constexpr int FL_WQ = FL_O + 512;
constexpr int FL_W1 = FL_WQ + 256;
constexpr int FL_W2 = FL_W1 + 16;
static_assert((FL_W2 + 8) * 256 <= WS_WQKV, "flag region");

constexpr int LD_W = 0;
constexpr int LD_V = 65536;
constexpr int LD_EX = 98304;
constexpr int LD_ONE = 131072;
constexpr int LD_CMB = 135168;
constexpr int LD_W2E = 122880;
constexpr int LD_SC = 151552;
constexpr int LD_ST = 155648;
constexpr int LD_PAR = 157696;
constexpr int LD_CTR = LD_PAR + 1536;
constexpr int LDS_BYTES = 163840;

#define MFMA(a, b, c) __builtin_amdgcn_mfma_f32_32x32x16_bf16((a), (b), (c), 0, 0, 0)
#define SBAR() __builtin_amdgcn_sched_barrier(0)
#define PIN(x) asm volatile("" : "+v"(x))
#define BARRIER_LDS() asm volatile("s_waitcnt lgkmcnt(0)\n\ts_barrier" ::: "memory")
#define BARRIER_ALL() asm volatile("s_waitcnt vmcnt(0) lgkmcnt(0)\n\ts_barrier" ::: "memory")

DI unsigned pk2(float lo, float hi) {
  bf16x2 v; v[0] = (__bf16)lo; v[1] = (__bf16)hi;
  return __builtin_bit_cast(unsigned, v);
}
DI float bf_lo(unsigned u) { return __uint_as_float(u << 16); }
DI float bf_hi(unsigned u) { return __uint_as_float(u & 0xffff0000u); }
DI float rbf(float x) { return (float)(__bf16)x; }
DI bf16x8 as_bf(u32x4 v) { return __builtin_bit_cast(bf16x8, v); }
DI float swap_half_sum(float x) {
  auto r = __builtin_amdgcn_permlane32_swap(__float_as_uint(x), __float_as_uint(x), false, false);
  return __uint_as_float(r[0]) + __uint_as_float(r[1]);
}
DI float swap_half_max(float x) {
  auto r = __builtin_amdgcn_permlane32_swap(__float_as_uint(x), __float_as_uint(x), false, false);
  return fmaxf(__uint_as_float(r[0]), __uint_as_float(r[1]));
}
DI void glds16x2(const char* sbase, unsigned voff, unsigned lds_base) {
  unsigned sv;
  asm volatile("s_mov_b32 %0, m0\n\ts_mov_b32 m0, %3\n\ts_nop 2\n\tglobal_load_lds_dwordx4 %1, %2 sc1\n\t"
               "global_load_lds_dwordx4 %1, %2 offset:1024 sc1\n\ts_mov_b32 m0, %0"
               : "=&s"(sv) : "v"(voff), "s"(sbase), "s"(lds_base) : "memory");
}
DI void glds16x4(const char* sbase, unsigned voff, unsigned lds_base) {
  unsigned sv;
  asm volatile("s_mov_b32 %0, m0\n\ts_mov_b32 m0, %3\n\ts_nop 2\n\tglobal_load_lds_dwordx4 %1, %2 sc1\n\t"
               "global_load_lds_dwordx4 %1, %2 offset:1024 sc1\n\tglobal_load_lds_dwordx4 %1, %2 offset:2048 sc1\n\t"
               "global_load_lds_dwordx4 %1, %2 offset:3072 sc1\n\ts_mov_b32 m0, %0"
               : "=&s"(sv) : "v"(voff), "s"(sbase), "s"(lds_base) : "memory");
}
template <int I> struct IC { static constexpr int v = I; };
template <int I, int N, class F> DI void static_for(F&& f) {
  if constexpr (I < N) { f(IC<I>{}); static_for<I + 1, N>(f); }
}

struct Ctx {
  char* ws; LAS char* lds; __amdgpu_buffer_rsrc_t rs;
  u64 token; unsigned lbase, voff;
  int bid, lane, w, r, h, tb, nb, tb64, nb8, head, qb;
  bool dead;
};
DI void fresh(Ctx& c) {
  asm volatile("" : "+s"(c.bid));
  asm volatile("" : "+s"(c.w));
  asm volatile("" : "+v"(c.lane));
  const int g = c.bid & 7, idx = c.bid >> 3;
  c.r = c.lane & 31; c.h = c.lane >> 5; c.voff = c.lane * 16;
  c.tb = 2 * g + (idx >> 4); c.nb = idx & 15; c.tb64 = 4 * g + (idx >> 3); c.nb8 = idx & 7;
  c.head = 4 * g + (idx & 3); c.qb = idx >> 2;
}
DI u32x4 ld16(const Ctx& c, unsigned voff, unsigned soff) { return __builtin_amdgcn_raw_buffer_load_b128(c.rs, voff, soff, 16); }
DI float ld4(const Ctx& c, unsigned voff, unsigned soff) { return __uint_as_float(__builtin_amdgcn_raw_buffer_load_b32(c.rs, voff, soff, 16)); }
DI void st16(const Ctx& c, u32x4 v, unsigned voff, unsigned soff) { __builtin_amdgcn_raw_buffer_store_b128(v, c.rs, voff + soff, 0, 16); }
DI void st4(const Ctx& c, float v, unsigned voff, unsigned soff) { __builtin_amdgcn_raw_buffer_store_b32(__float_as_uint(v), c.rs, voff, soff, 16); }
DI gu64* flag_ptr(const Ctx& c, int line, int slot) { return (gu64*)(c.ws + WS_FLAG) + line * 32 + slot; }

DI void publish(const Ctx& c, int ctr, unsigned n, int line0, int slot) {
  asm volatile("s_waitcnt vmcnt(0)" ::: "memory");
  unsigned old = 0;
  if (c.lane == 0) old = __hip_atomic_fetch_add((LAS unsigned*)(c.lds + LD_CTR) + ctr, 1u, __ATOMIC_RELAXED, __HIP_MEMORY_SCOPE_WORKGROUP);
  if (__builtin_amdgcn_readfirstlane(old) == n - 1 && c.lane < 16) __hip_atomic_store(flag_ptr(c, line0 + c.lane, slot), c.token, RLX_AGENT);
}
DI void code_warm(const Ctx& c, u64 pc) {
  unsigned sv;
  asm volatile("s_mov_b32 %0, m0\n\ts_mov_b32 m0, %3\n\ts_nop 2\n\tglobal_load_lds_dword %1, %2\n\ts_mov_b32 m0, %0"
               : "=&s"(sv) : "v"(min((c.bid >> 3) * 1792 + (c.lane & 15) * 128, 56 * 1024)), "s"(pc), "s"(c.lbase + LD_CTR + 256) : "memory");
}
DI void code_rewarm(const Ctx& c) {
  const u64 v = *(const LAS u64*)(c.lds + LD_CTR + 128);
  code_warm(c, ((u64)__builtin_amdgcn_readfirstlane((unsigned)(v >> 32)) << 32) | (unsigned)__builtin_amdgcn_readfirstlane((unsigned)v));
}
DI void poll_any(Ctx& c, int line, int slot) {
  if (!c.dead) {
    gu64* p = flag_ptr(c, line, slot);
    unsigned spins = 0;
    while (!__all(__hip_atomic_load(p, RLX_AGENT) == c.token)) {
      if (++spins > (1u << 18)) { c.dead = true; break; }
    }
    __builtin_amdgcn_fence(__ATOMIC_ACQUIRE, "wavefront");
  }
}
template <bool NAP = false> DI void wait4(const Ctx& c, int ctr) {
  while (__hip_atomic_load((LAS unsigned*)(c.lds + LD_CTR) + ctr, __ATOMIC_RELAXED, __HIP_MEMORY_SCOPE_WORKGROUP) < 4u)
    if (NAP) __builtin_amdgcn_s_sleep(2);
  asm volatile("" ::: "memory");
}
DI void arrive4(const Ctx& c, int ctr) {
  asm volatile("s_waitcnt lgkmcnt(0)" ::: "memory");
  if (c.lane == 0) __hip_atomic_fetch_add((LAS unsigned*)(c.lds + LD_CTR) + ctr, 1u, __ATOMIC_RELAXED, __HIP_MEMORY_SCOPE_WORKGROUP);
}
template <int NAP> DI void poll(Ctx& c, int line, int slot) {
  if (c.w == 0) {
    __builtin_amdgcn_s_sleep(NAP);
    poll_any(c, line, slot);
  }
}
template <int NQ> DI void help_dma(const Ctx& c, unsigned src, int dst) {
#pragma unroll
  for (int u = 0; u < NQ / 4; ++u) {
    const int p = (c.w & 3) + 4 * u;
    glds16x4(c.ws + src + p * 4096, c.voff, c.lbase + dst + p * 4096);
  }
}
DI int w2_run(int run) { return run < 6 ? LD_W + run * 14336 : LD_W2E + (run - 6) * 14336; }
template <int U0, int U1> DI void w2_fetch(const Ctx& c, u32x4 (&h)[28]) {
#pragma unroll
  for (int u = U0; u < U1; ++u) {
    const int f = (c.w & 3) + 4 * u, run = f / 14, kq = run >> 1;
    h[u] = ld16(c, c.voff, WS_W2 + ((2 * c.nb8 + (run & 1)) * KS_F + kq * 21 + (kq < 2 ? kq : 2) + f % 14) * 1024);
  }
  SBAR();
}
template <int U0, int U1> DI void w2_put(const Ctx& c, const u32x4 (&h)[28]) {
#pragma unroll
  for (int u = U0; u < U1; ++u) {
    const int f = (c.w & 3) + 4 * u;
    *(LAS u32x4*)(c.lds + (u < 21 ? LD_W : LD_W2E - 84 * 1024) + f * 1024 + c.voff) = h[u];
  }
}

DI f32x16 unpack16(const u32x4 (&res)[2]) {
  f32x16 v;
#pragma unroll
  for (int i = 0; i < 8; ++i) { const unsigned u = res[i >> 2][i & 3]; v[2 * i] = bf_lo(u); v[2 * i + 1] = bf_hi(u); }
  return v;
}
DI void pack16(const f32x16& v, u32x4 (&res)[2]) {
#pragma unroll
  for (int i = 0; i < 8; ++i) res[i >> 2][i & 3] = pk2(v[2 * i], v[2 * i + 1]);
}
DI void store_x(const Ctx& c, int inst, u32x4 (&res)[2], const f32x16& v) {
  const int tt = 2 * c.tb64 + (c.w >> 1), nt = 2 * c.nb8 + (c.w & 1);
  st16(c, res[0], c.voff, WS_X + inst * (2 * MB) + (tt * 32 + 2 * nt) * 1024);
  st16(c, res[1], c.voff, WS_X + inst * (2 * MB) + (tt * 32 + 2 * nt + 1) * 1024);
  SBAR();
  f32x2 a1 = {v[0], v[1]}, a2 = a1 * a1;
#pragma unroll
  for (int i = 1; i < 8; ++i) { const f32x2 t = {v[2 * i], v[2 * i + 1]}; a1 += t; a2 += t * t; }
  auto r1 = __builtin_amdgcn_permlane32_swap(__float_as_uint(a1[0] + a1[1]), __float_as_uint(a2[0] + a2[1]), false, false);
  st4(c, __uint_as_float(r1[0]) + __uint_as_float(r1[1]), c.lane * 4, WS_ST + inst * (64 * 4096) + tt * 4096 + nt * 256);
  PIN(res[0]); PIN(res[1]);
}

DI void wfrag_load(const unsigned short* W, int N, int nt, int ks, int r, int h, unsigned (&e)[8]) {
#pragma unroll
  for (int j = 0; j < 8; ++j) e[j] = W[(size_t)(16 * ks + 8 * (j >> 2) + 4 * h + (j & 3)) * N + 32 * nt + r];
}
DI void wfrag_store(const Ctx& c, const unsigned (&e)[8], unsigned dst, bool docs, unsigned csdst) {
  u32x4 v;
#pragma unroll
  for (int j = 0; j < 4; ++j) v[j] = e[2 * j] | (e[2 * j + 1] << 16);
  st16(c, v, c.voff, dst);
  if (docs) {
    float s = 0.f;
#pragma unroll
    for (int j = 0; j < 8; ++j) s += __uint_as_float(e[j] << 16);
    s = swap_half_sum(s);
    if (c.h == 0) st4(c, s, c.r * 4, csdst);
  }
}

DI u32x4 mish8(const f32x8& a, float rs) {
  float v[8];
#pragma unroll
  for (int j = 0; j < 8; ++j) {
    const float u = __builtin_amdgcn_exp2f(a[j] * (rs * 1.4426950408889634f));
    v[j] = a[j] * fmaf(-2.f * rs, __builtin_amdgcn_rcpf(fmaf(u, u + 2.f, 2.f)), rs);
  }
  return (u32x4){pk2(v[0], v[1]), pk2(v[2], v[3]), pk2(v[4], v[5]), pk2(v[6], v[7])};
}
#define LO8(s) __builtin_shufflevector(s, s, 0, 1, 2, 3, 4, 5, 6, 7)
#define HI8(s) __builtin_shufflevector(s, s, 8, 9, 10, 11, 12, 13, 14, 15)
DI f32x8 cat8(f32x4 a, f32x4 b) { return __builtin_shufflevector(a, b, 0, 1, 2, 3, 4, 5, 6, 7); }

template <int T, int PARTS = 3>
DI void ln_tile(const Ctx& c, int l, const f32x8& lo, const f32x8& hi, float rstd) {
  const int tt = 4 * c.tb + (c.w & 3);
  const unsigned so = WS_L + l * L_SZ + L_H + (tt * KS_F + 2 * (c.nb < 11 ? 3 * c.nb : 33 + 2 * (c.nb - 11)) + 2 * T) * 1024;
  if (PARTS & 1) st16(c, mish8(lo, rstd), c.voff, so);
  if (PARTS & 2) st16(c, mish8(hi, rstd), c.voff, so + 1024);
}
template <int MODE> DI void ln_publish(const Ctx& c, int l) {
  if (MODE == 0) publish(c, 4 + l, 4, FL_Q + l * 256 + c.nb * 16, c.tb);
  else publish(c, 6 + l, 8, FL_H + l * 256 + c.tb * 16, c.nb);
}

DI void pin16(f32x16& v) { PIN(v); }
DI void pinu(unsigned v) { asm volatile("" ::"v"(v)); }
DI u32x4 wfrag(const LAS char* base, int ks) { return *(const LAS u32x4*)(base + ks * 1024); }
DI void put16(LAS f32x4* ex, const f32x16& a) {
#pragma unroll
  for (int g = 0; g < 4; ++g) ex[g * 64] = (f32x4){a[4 * g], a[4 * g + 1], a[4 * g + 2], a[4 * g + 3]};
}

template <int MODE, bool FIRST>
DI void phase_ln(Ctx& c, int l, int inst, u32x4 (&w2r)[16]) {
  const int tt = 4 * c.tb + c.w;
  const unsigned xs = WS_X + inst * (2 * MB) + tt * 32768;
  constexpr int PD = 14;
  u32x4 xf[32], sp[4];
#pragma unroll
  for (int k = 0; k < PD; ++k) xf[k] = ld16(c, c.voff, xs + k * 1024);
  SBAR();
  const LAS char* wl = c.lds + LD_W + c.lane * 16;
  const LAS char* wl2 = wl + 65536;
  asm volatile("" : "+v"(wl2));
  f32x16 aA, aB;
#pragma unroll
  for (int i = 0; i < 16; ++i) { aA[i] = 0.f; aB[i] = 0.f; }
  u32x4 fa[3], fb[3];
  if (FIRST) BARRIER_LDS();
#pragma unroll
  for (int k = 0; k < 2; ++k) { fa[k] = wfrag(wl, k); fb[k] = wfrag(wl + 32768, k); }
  float rstd = 0.f, sc4[4];
  u32x2 we[3]; u32x4 xe;
  LAS float* sc = (LAS float*)(c.lds + LD_SC) + c.w * 256;
  const int k0q = c.w * 21 + (c.w < 2 ? c.w : 2);
  const unsigned w2s = WS_W2 + ((2 * c.nb8 + (c.w & 1)) * KS_F + k0q + 14) * 1024;
  const unsigned w2o = WS_W2 + ((2 * c.nb8 + 1 - (c.w & 1)) * KS_F + k0q + 14) * 1024;
  auto w2load = [&](int i) __attribute__((always_inline)) {
    w2r[i] = ld16(c, c.voff, ((i & 1) ? w2o : w2s) + (((i >> 1) == 7 && c.w >= 2) ? 6 : (i >> 1)) * 1024);
  };
  static_for<0, 32>([&](auto kc) __attribute__((always_inline)) {
    constexpr int k = decltype(kc)::v;
    if constexpr (FIRST && (((k & 7) == 6 && k < 24) || k == 31)) asm volatile("s_barrier" ::: "memory");
    aA = MFMA(as_bf(fa[k % 3]), as_bf(xf[k]), aA);
    pin16(aA); SBAR();
    if constexpr (k + 2 < 32) fa[(k + 2) % 3] = wfrag(wl, k + 2);
    if constexpr (k + PD < 32) xf[k + PD] = ld16(c, c.voff, xs + (k + PD) * 1024);
    if constexpr (k + PD >= 32 && k + PD < 36) sp[k + PD - 32] = ld16(c, c.voff, WS_ST + inst * (64 * 4096) + tt * 4096 + (k + PD - 32) * 1024);
    if constexpr (MODE == 1 && k >= 24) w2load(k - 24);
    SBAR();
    aB = MFMA(as_bf(fb[k % 3]), as_bf(xf[k]), aB);
    pin16(aB); SBAR();
    if constexpr (k + 2 < 32) fb[(k + 2) % 3] = wfrag(wl + 32768, k + 2);
    if constexpr (k == 29) {
      ((LAS f32x4*)sc)[c.lane] = (__builtin_bit_cast(f32x4, sp[0]) + __builtin_bit_cast(f32x4, sp[1])) +
                                 (__builtin_bit_cast(f32x4, sp[2]) + __builtin_bit_cast(f32x4, sp[3]));
#pragma unroll
      for (int i = 0; i < 4; ++i) sc4[i] = sc[i * 64 + c.lane];
    }
    if constexpr (k == (FIRST ? 31 : 30)) {
#pragma unroll
      for (int i = 0; i < 3; ++i) we[i] = ((const LAS u32x2*)(c.lds + LD_PAR))[MODE * 96 + 32 * i + c.r];
    }
    if constexpr (k == 31) {
      const float s = (sc4[0] + sc4[1]) + (sc4[2] + sc4[3]);
      auto rr = __builtin_amdgcn_permlane32_swap(__float_as_uint(s), __float_as_uint(s), false, false);
      const float mu = __uint_as_float(rr[0]) * (1.f / D_);
      const float var = fmaxf(__uint_as_float(rr[1]) * (1.f / D_) - mu * mu, 0.f);
      const float sd = rbf(__builtin_amdgcn_sqrtf(rbf(rbf(var) + 1e-5f)));
      rstd = __builtin_amdgcn_rcpf(sd);
      ((LAS float*)(c.lds + LD_ST))[c.w * 32 + c.r] = rstd;
      xe = (u32x4){c.h ? 0u : pk2(-mu, -mu), c.h ? 0u : pk2(-mu, sd), 0u, 0u};
    }
    SBAR();
  });
  aB = MFMA(as_bf((u32x4){we[1][0], we[1][1], 0u, 0u}), as_bf(xe), aB);
  aA = MFMA(as_bf((u32x4){we[0][0], we[0][1], 0u, 0u}), as_bf(xe), aA);
  f32x16 aC;
#pragma unroll
  for (int i = 0; i < 16; ++i) aC[i] = 0.f;
  aC = MODE == 0 ? MFMA(as_bf(xe), as_bf((u32x4){we[2][0], we[2][1], 0u, 0u}), aC) : MFMA(as_bf((u32x4){we[2][0], we[2][1], 0u, 0u}), as_bf(xe), aC);
  constexpr int CD = 5;
  u32x4 fc[CD + 1];
#pragma unroll
  for (int k = 0; k < CD; ++k) fc[k] = wfrag(wl2, k);
  SBAR();
  if (MODE == 1) {
    LAS f32x4* ex = (LAS f32x4*)(c.lds + LD_EX) + c.w * 384 + c.lane;
    put16(ex, aB);
    ex[256] = (f32x4){aA[8], aA[9], aA[10], aA[11]}; ex[320] = (f32x4){aA[12], aA[13], aA[14], aA[15]};
    BARRIER_LDS();
  }
  f32x4 ts[4];
  u32x4 pw;
  const float rq = rstd * (0.25f * 1.4426950408889634f);
  const unsigned vok = c.h * 512 + ((c.r & 19) | ((c.r & 4) << 1) | ((c.r & 8) >> 1)) * 16;
  const unsigned soq = WS_L + l * L_SZ + L_Q + (2 * c.nb * 64 + tt) * 1024;
  static_for<0, 32>([&](auto kc) __attribute__((always_inline)) {
    constexpr int k = decltype(kc)::v;
    aC = MODE == 0 ? MFMA(as_bf(xf[k]), as_bf(fc[k % (CD + 1)]), aC) : MFMA(as_bf(fc[k % (CD + 1)]), as_bf(xf[k]), aC);
    pin16(aC); SBAR();
    if constexpr (k + CD < 32) fc[(k + CD) % (CD + 1)] = wfrag(wl2, k + CD);
    if constexpr (MODE == 1 && (k & 1) == 0 && 8 + k / 2 < 16) w2load(8 + k / 2);
    if constexpr (MODE == 0 && k >= 6 && k <= 18 && (k & 3) == 2)
      st16(c, pw, k < 12 ? vok : c.voff, (k < 12 ? soq + (L_K - L_Q) : soq) + ((k & 4) ? 0 : 65536));
    if constexpr (MODE == 0 && k >= 2 && k < 18) {
      constexpr int i = k - 2;
      const f32x16& a = i < 8 ? aB : aA;
      const float rs = i < 8 ? rstd : rq;
      pw[i & 3] = pk2(a[2 * (i & 7)] * rs, a[2 * (i & 7) + 1] * rs);
      pinu(pw[i & 3]);
    }
    if constexpr (MODE == 0 && k == 29) {
#pragma unroll
      for (int g = 0; g < 4; ++g) ts[g] = *(const LAS f32x4*)((const LAS float*)(c.lds + LD_ST) + c.w * 32 + 8 * g + 4 * c.h);
    }
    SBAR();
  });
  if (MODE == 1) arrive4(c, 14 + l);
  if (MODE == 1) { ln_tile<0, 1>(c, l, LO8(aA), LO8(aA), rstd); if (c.nb < 11) ln_tile<2>(c, l, LO8(aC), HI8(aC), rstd); }
  else {
    unsigned q[8];
#pragma unroll
    for (int g = 0; g < 8; ++g) q[g] = pk2(aC[2 * g] * ts[g >> 1][2 * (g & 1)], aC[2 * g + 1] * ts[g >> 1][2 * (g & 1) + 1]);
    const unsigned vo = (c.r >> 4) * 65536 + c.h * 256 + (c.r & 15) * 16, so = WS_L + l * L_SZ + L_V + (2 * c.nb * 128 + 2 * tt) * 512;
    st16(c, (u32x4){q[0], q[1], q[2], q[3]}, vo, so);
    st16(c, (u32x4){q[4], q[5], q[6], q[7]}, vo, so + 512);
  }
  ln_publish<MODE>(c, l);
}
template <class F>
DI void help_ln(Ctx& c, int l, F&& mid) {
  BARRIER_LDS();
  const LAS f32x4* ex = (const LAS f32x4*)(c.lds + LD_EX) + (c.w & 3) * 384 + c.lane;
  const float rstd = ((const LAS float*)(c.lds + LD_ST))[(c.w & 3) * 32 + c.r];
  const f32x8 b0 = cat8(ex[0], ex[64]), b1 = cat8(ex[128], ex[192]), a1 = cat8(ex[256], ex[320]);
  SBAR();
  ln_tile<1>(c, l, b0, b1, rstd);
  ln_tile<0, 2>(c, l, a1, a1, rstd);
  SBAR();
  mid();
  ln_publish<1>(c, l);
}
DI void colsum_load(const Ctx& c, int mode, float (&v)[32]) {
  const int t = min((c.w - 4) * 64 + c.lane, 95);
#pragma unroll
  for (int i = 0; i < 32; ++i) v[i] = ld4(c, t * 4, WS_CSP + ((mode * 16 + c.nb) * 32 + i) * 384);
  SBAR();
}
DI void colsum_put(const Ctx& c, int mode, const float (&v)[32], unsigned bias) {
  const int t = (c.w - 4) * 64 + c.lane;
  float s = 0.f;
#pragma unroll
  for (int i = 0; i < 32; i += 4) s += (v[i] + v[i + 1]) + (v[i + 2] + v[i + 3]);
  const float s0 = rbf(s), s1 = rbf(s - s0);
  if (t < 96) ((LAS u32x2*)(c.lds + LD_PAR))[mode * 96 + t] = (u32x2){pk2(s0, s1), (pk2(s - s0 - s1, 0.f) & 0xffffu) | (bias << 16)};
}

DI f32x16 reduce_res(Ctx& c, const f32x16 (&a)[4], u32x4 (&res)[2], int ctr) {
  LAS f32x4* red = (LAS f32x4*)(c.lds + LD_EX) + c.lane;
#pragma unroll
  for (int j = 1; j < 4; ++j) {
    LAS f32x4* p = red + ((c.w ^ j) * 3 + j - 1) * 256;
#pragma unroll
    for (int g = 0; g < 4; ++g) p[g * 64] = (f32x4){a[j][4 * g], a[j][4 * g + 1], a[j][4 * g + 2], a[j][4 * g + 3]};
  }
  if (ctr >= 0) { arrive4(c, ctr); wait4(c, ctr); } else BARRIER_LDS();
  const LAS f32x4* q = red + c.w * 768;
  f32x16 v;
#pragma unroll
  for (int g = 0; g < 4; ++g) {
    const f32x4 s = ((f32x4){a[0][4 * g], a[0][4 * g + 1], a[0][4 * g + 2], a[0][4 * g + 3]} + q[g * 64]) + (q[256 + g * 64] + q[512 + g * 64]);
#pragma unroll
    for (int j = 0; j < 4; ++j) v[4 * g + j] = s[j];
  }
  pack16(v, res);
  return v;
}
#define MM4(w0, w1, x0, x1) do { a[0] = MFMA(as_bf(w0), as_bf(x0), a[0]); a[1] = MFMA(as_bf(w1), as_bf(x0), a[1]); \
                                 a[2] = MFMA(as_bf(w0), as_bf(x1), a[2]); a[3] = MFMA(as_bf(w1), as_bf(x1), a[3]); } while (0)

DI f32x16 phase_wo(Ctx& c, int l, const u32x4 (&wo)[16], u32x4 (&res)[2]) {
  const unsigned xs = WS_L + l * L_SZ + L_O + ((2 * c.tb64 + (c.w >> 1)) * 32 + 8 * c.w) * 1024;
  const unsigned xo = WS_L + l * L_SZ + L_O + ((2 * c.tb64 + 1 - (c.w >> 1)) * 32 + 8 * c.w) * 1024;
  u32x4 xf[16];
#pragma unroll
  for (int k = 0; k < 8; ++k) { xf[2 * k] = ld16(c, c.voff, xs + k * 1024); xf[2 * k + 1] = ld16(c, c.voff, xo + k * 1024); }
  asm volatile("s_barrier" ::: "memory");
  SBAR();
  f32x16 a[4];
#pragma unroll
  for (int i = 16; i < 64; ++i) a[i >> 4][i & 15] = 0.f;
  a[0] = unpack16(res);
#pragma unroll
  for (int k = 0; k < 8; ++k) { MM4(wo[2 * k], wo[2 * k + 1], xf[2 * k], xf[2 * k + 1]); PIN(a[0]); PIN(a[1]); PIN(a[2]); PIN(a[3]); SBAR(); }
  return reduce_res(c, a, res, -1);
}

DI f32x16 phase_w2(Ctx& c, int l, const u32x4 (&w2r)[16], u32x4 (&res)[2]) {
  const int k0 = c.w * 21 + (c.w < 2 ? c.w : 2);
  const unsigned xs = WS_L + l * L_SZ + L_H + ((2 * c.tb64 + (c.w >> 1)) * KS_F + k0) * 1024;
  const unsigned xo = WS_L + l * L_SZ + L_H + ((2 * c.tb64 + 1 - (c.w >> 1)) * KS_F + k0) * 1024;
  constexpr int WIN = 10;
  u32x4 xf[2 * WIN];
#pragma unroll
  for (int k = 0; k < WIN; ++k) { xf[2 * k] = ld16(c, c.voff, xs + k * 1024); xf[2 * k + 1] = ld16(c, c.voff, xo + k * 1024); }
  SBAR();
  const LAS u32x4* w0 = (const LAS u32x4*)(c.lds + w2_run(2 * c.w + (c.w & 1))) + c.lane;
  const LAS u32x4* w1 = (const LAS u32x4*)(c.lds + w2_run(2 * c.w + 1 - (c.w & 1))) + c.lane;
  f32x16 a[4];
#pragma unroll
  for (int i = 16; i < 64; ++i) a[i >> 4][i & 15] = 0.f;
  a[0] = unpack16(res);
  u32x4 f0[2], f1[2];
  f0[0] = w0[0]; f1[0] = w1[0];
  static_for<0, 22>([&](auto kc) __attribute__((always_inline)) {
    constexpr int k = decltype(kc)::v;
    const u32x4 g0 = k < 14 ? f0[k & 1] : w2r[k < 14 ? 0 : 2 * (k - 14)], g1 = k < 14 ? f1[k & 1] : w2r[k < 14 ? 0 : 2 * (k - 14) + 1];
    const u32x4 x0 = xf[2 * (k % WIN)], x1 = xf[2 * (k % WIN) + 1];
    constexpr int kn = k - 1 + WIN;
    const int kk = (kn == 21 && c.w >= 2) ? 20 : kn;
    if constexpr (kn == 22) asm volatile("s_barrier" ::: "memory");
    if (k < 21 || c.w < 2) {
      a[0] = MFMA(as_bf(g0), as_bf(x0), a[0]);
      pin16(a[0]); SBAR();
      if constexpr (k >= 1 && kn < 22) xf[2 * ((k - 1) % WIN)] = ld16(c, c.voff, xs + kk * 1024);
      SBAR();
      a[1] = MFMA(as_bf(g1), as_bf(x0), a[1]);
      pin16(a[1]); SBAR();
      if constexpr (k + 1 < 14) f0[(k + 1) & 1] = w0[(k + 1) * 64];
      SBAR();
      a[2] = MFMA(as_bf(g0), as_bf(x1), a[2]);
      pin16(a[2]); SBAR();
      if constexpr (k >= 1 && kn < 22) xf[2 * ((k - 1) % WIN) + 1] = ld16(c, c.voff, xo + kk * 1024);
      SBAR();
      a[3] = MFMA(as_bf(g1), as_bf(x1), a[3]);
      pin16(a[3]); SBAR();
      if constexpr (k + 1 < 14) f1[(k + 1) & 1] = w1[(k + 1) * 64];
      SBAR();
    }
  });
  if (l == 0) { arrive4(c, 16); wait4(c, 16); } else BARRIER_LDS();
  return reduce_res(c, a, res, l == 0 ? 17 : -1);
}

#define SMF(a, b8, c) __builtin_amdgcn_smfmac_f32_32x32x32_bf16((a), __builtin_bit_cast(bf16x16, b8), (c), sidx, 0, 0)
#define LDF(p) (*(const LAS bf16x8*)(p))
DI float max16(const f32x16& s) {
  float a = fmaxf(fmaxf(s[0], s[1]), s[2]), b = fmaxf(fmaxf(s[3], s[4]), s[5]);
  a = fmaxf(fmaxf(a, s[6]), s[7]); b = fmaxf(fmaxf(b, s[8]), s[9]);
  a = fmaxf(fmaxf(a, s[10]), s[11]); b = fmaxf(fmaxf(b, s[12]), s[13]);
  a = fmaxf(fmaxf(a, s[14]), s[15]);
  return swap_half_max(fmaxf(a, b));
}
DI void attn_slow(const LAS char* kp, const LAS char* vp, bool isv, bf16x8 q, f32x16& o, float& m) {
  const int sidx = 0xDDDDDDDD;
  f32x16 zero;
#pragma unroll
  for (int i = 0; i < 16; ++i) zero[i] = 0.f;
  o = zero; m = -3.0e38f;
  for (int t = 0; t < 32; ++t) {
    f32x16 s = MFMA(LDF(kp + t * 1024), q, zero);
    const float mn = fmaxf(m, max16(s));
    const float al = __builtin_amdgcn_exp2f(m - mn);
    m = mn;
#pragma unroll
    for (int i = 0; i < 16; ++i) { o[i] *= al; s[i] = __builtin_amdgcn_exp2f(s[i] - mn); }
    const int vo = isv ? t * 1024 : 0;
    o = SMF(LDF(vp + vo), LO8(s), o);
    o = SMF(LDF(vp + vo + 512), HI8(s), o);
  }
}

struct In { const unsigned short *x, *wqkv, *wo, *w1, *b1, *w2; unsigned short* out; };
struct Jobs { unsigned e[4][8], dst[4]; bool ok[4]; };
DI void jobs_load(const Ctx& c, const In& in, Jobs& jb) {
  const int t0 = c.nb < 11 ? 3 * c.nb : 33 + 2 * (c.nb - 11), nt1 = c.nb < 11 ? 3 : 2;
  const unsigned short* W[4]; int N[4];
#pragma unroll
  for (int u = 0; u < 4; ++u) {
    const int j = c.w + 4 * u;
    int nt, ks;
    if (j < 6) { W[u] = in.w1; N[u] = F_; jb.ok[u] = (j >> 1) < nt1; nt = t0 + (jb.ok[u] ? j >> 1 : 0); ks = 2 * c.tb + (j & 1); jb.dst[u] = WS_W1 + (nt * 32 + ks) * 1024; }
    else if (j < 8) { W[u] = in.wo; N[u] = D_; jb.ok[u] = true; nt = 2 * c.nb8 + (j & 1); ks = c.tb64; jb.dst[u] = WS_WO + (nt * 32 + ks) * 1024; }
    else { W[u] = in.w2; N[u] = D_; ks = c.tb64 + 32 * ((j - 8) >> 1); jb.ok[u] = ks < KS_F; if (!jb.ok[u]) ks = 0; nt = 2 * c.nb8 + (j & 1); jb.dst[u] = WS_W2 + (nt * KS_F + ks) * 1024; }
    W[u] += 16 * ks * N[u] + 32 * nt;
    asm volatile("" : "+s"(W[u]));
    asm volatile("" : "+s"(N[u]));
  }
#pragma unroll
  for (int u = 0; u < 4; ++u) {
    const __amdgpu_buffer_rsrc_t rw = __builtin_amdgcn_make_buffer_rsrc((void*)W[u], 0, 32 * N[u], 0x00020000);
    const unsigned o = (4 * c.h * N[u] + c.r) * 2;
#pragma unroll
    for (int j = 0; j < 8; ++j) jb.e[u][j] = __builtin_amdgcn_raw_buffer_load_b16(rw, o, (8 * (j >> 2) + (j & 3)) * 2 * N[u], 0);
  }
}
DI void jobs_put(const Ctx& c, Jobs& jb) {
#pragma unroll
  for (int i = 0; i < 32; ++i) PIN(jb.e[i >> 3][i & 7]);
#pragma unroll
  for (int u = 0; u < 4; ++u) {
    const int j = c.w + 4 * u;
    if (jb.ok[u]) wfrag_store(c, jb.e[u], jb.dst[u], j < 6, WS_CSP + ((16 + c.nb) * 32 + 2 * c.tb + (j & 1)) * 384 + (j >> 1) * 128);
  }
}
DI void jobs_publish(const Ctx& c) {
  asm volatile("s_waitcnt vmcnt(0)" ::: "memory");
  unsigned old = 0;
  if (c.lane == 0) old = __hip_atomic_fetch_add((LAS unsigned*)(c.lds + LD_CTR) + 2, 1u, __ATOMIC_RELAXED, __HIP_MEMORY_SCOPE_WORKGROUP);
  if (__builtin_amdgcn_readfirstlane(old) == 3 && c.lane < 2)
    __hip_atomic_store(c.lane ? flag_ptr(c, FL_W1 + c.nb, c.tb) : flag_ptr(c, FL_W2 + c.nb8, c.tb64), c.token, RLX_AGENT);
}

#define EX1(r) "v_exp_f32 v" #r ", v" #r "\n\t"
#define ASTEP(KOFF, VOFF, kc_, kn_, v0_, v1_, nv0_, pv1_)                                                                  \
  asm volatile("s_waitcnt lgkmcnt(0)\n\t" EX1(64) EX1(65)                                                                  \
               "v_smfmac_f32_32x32x32_bf16 %[o1], %[pv1], v[88:95], %[idx]\n\t"                                            \
               "ds_read_b128 %[nv0], %[vp] offset:%[vo]\n\tds_read_b128 %[pv1], %[vp] offset:%[vo1]\n\t"                   \
               EX1(66) EX1(67) EX1(68)                                                                                     \
               "v_mfma_f32_32x32x16_bf16 v[80:95], %[kc], %[q1], %[c1]\n\t"                                                \
               "ds_read_b128 %[kc], %[kp] offset:%[ko]\n\t"                                                                \
               EX1(69) EX1(70) EX1(71) EX1(72) EX1(73)                                                                     \
               "v_smfmac_f32_32x32x32_bf16 %[o0], %[v0], v[64:71], %[idx]\n\t"                                             \
               EX1(74) EX1(75) EX1(76) EX1(77) EX1(78) EX1(79) EX1(80) EX1(81)                                             \
               "v_smfmac_f32_32x32x32_bf16 %[o0], %[v1], v[72:79], %[idx]\n\t"                                             \
               EX1(82) EX1(83) EX1(84)                                                                                     \
               "v_mfma_f32_32x32x16_bf16 v[64:79], %[kn], %[q0], %[c0]\n\t"                                                \
               EX1(85) EX1(86) EX1(87) EX1(88) EX1(89)                                                                     \
               "v_smfmac_f32_32x32x32_bf16 %[o1], %[v0], v[80:87], %[idx]\n\t"                                             \
               EX1(90) EX1(91) EX1(92) EX1(93) EX1(94) "v_exp_f32 v95, v95"                                                \
               : "+{v[64:79]}"(sA), "+{v[80:95]}"(sB), [o0] "+v"(o0), [o1] "+v"(o1), [kc] "+v"(kc_), [nv0] "=&v"(nv0_),    \
                 [pv1] "+v"(pv1_)                                                                                          \
               : [kn] "v"(kn_), [v0] "v"(v0_), [v1] "v"(v1_), [q0] "v"(q0), [q1] "v"(q1), [c0] "v"(cin0), [c1] "v"(cin1),  \
                 [idx] "v"(sidx), [kp] "v"(kpa), [vp] "v"(vpa), [ko] "n"(KOFF), [vo] "n"(VOFF), [vo1] "n"((VOFF) + 512)     \
               : "memory")

template <int half, int l>
DI void phase_attn(Ctx& c, const In& in, u32x4 (&wo)[16], u32x4 (&hw)[28]) {
  const int qt = c.qb * 8 + (c.w & 3) * 2;
  const unsigned lay = WS_L + l * L_SZ;
  const char* kvg = c.ws + lay + ((c.w & 1) ? L_V : L_K) + c.head * 65536 + ((c.w >> 1) & 1) * 32768 + half * 4096;
  const unsigned kvd = c.lbase + ((c.w & 1) ? LD_V : LD_W) + ((c.w >> 1) & 1) * 32768 + half * 4096;
  u32x4 qv0, qv1;
  asm volatile("s_nop 4\n\tglobal_load_dwordx4 %0, %2, %3 sc1\n\tglobal_load_dwordx4 %1, %2, %3 offset:1024 sc1"
               : "=&v"(qv0), "=&v"(qv1) : "v"(c.voff), "s"(c.ws + lay + L_Q + (c.head * 64 + qt) * 1024) : "memory");
#define KV4(u) glds16x4(kvg + (u) * 8192, c.voff, kvd + (u) * 8192)
  glds16x4(kvg, c.voff, kvd);
  if (half == 0) {
    const unsigned o1 = (c.lane & 11) == 0 ? 0x3f803f80u : 0u;
    ((LAS u32x4*)(c.lds + LD_ONE))[c.w * 64 + c.lane] = (u32x4){o1, o1, o1, o1};
  }
  int sidx = 0xDDDDDDDD;
  const bool isv = c.r < 16;
  const LAS char* kp0 = c.lds + LD_W + half * 32768 + c.lane * 16;
  const LAS char* vp0 = c.lds + (isv ? LD_V + half * 32768 : LD_ONE) + c.h * 256 + (c.r & 15) * 16;
  const int vstep = isv ? 4096 : 0;
  if (half == 0) asm volatile("s_waitcnt vmcnt(0) lgkmcnt(0)\n\ts_barrier" : "+v"(qv0), "+v"(qv1)::"memory");
  else asm volatile("s_waitcnt vmcnt(4)\n\ts_barrier" : "+v"(qv0), "+v"(qv1)::"memory");
  if (half == 0) __builtin_amdgcn_s_setprio(1);
  const bf16x8 q0 = as_bf(qv0), q1 = as_bf(qv1);
  f32x16 zero;
#pragma unroll
  for (int i = 0; i < 16; ++i) zero[i] = 0.f;
  f32x16 cin0, cin1, o0 = zero, o1 = zero, sA, sB = zero;
  float m0, m1;
  bf16x8 ka = LDF(kp0), kb = LDF(kp0 + 1024), va0 = LDF(vp0), va1 = LDF(vp0 + 512), vb0, vb1 = as_bf((u32x4){0u, 0u, 0u, 0u});
  {
    const f32x16 s = MFMA(ka, q0, zero), s1 = MFMA(ka, q1, zero);
    m0 = max16(s); m1 = max16(s1);
#pragma unroll
    for (int i = 0; i < 16; ++i) { cin0[i] = -m0; cin1[i] = -m1; sA[i] = s[i] - m0; }
  }
  unsigned kpa = (unsigned)(size_t)kp0, vpa = (unsigned)(size_t)vp0;
  asm volatile("s_nop 1" : "+v"(cin0), "+v"(cin1), "+v"(sA), "+v"(sB), "+v"(o0), "+v"(o1), "+v"(sidx), "+v"(vb1));
#define QUAD(A, B, MID, C, D)                                                        \
  do {                                                                               \
    ASTEP(2048, 1024, ka, kb, va0, va1, vb0, vb1); A;                                \
    ASTEP(3072, 2048, kb, ka, vb0, vb1, va0, va1); B; MID;                           \
    ASTEP(4096, 3072, ka, kb, va0, va1, vb0, vb1); C;                                \
    vpa += vstep;                                                                    \
    ASTEP(5120, 0, kb, ka, vb0, vb1, va0, va1); D;                                   \
    kpa += 4096;                                                                      \
  } while (0)
#define NOP (void)0
#define WBAR(n) asm volatile("s_waitcnt vmcnt(" #n ")\n\ts_barrier" ::: "memory")
  constexpr bool JOBS = half == 0 && l == 0;
  Jobs jb;
  KV4(1);
  QUAD(KV4(2), NOP, WBAR(8), KV4(3), if (JOBS) { jobs_load(c, in, jb); SBAR(); });
  QUAD(NOP, NOP, if (JOBS) WBAR(32); else WBAR(0); if (half == 1 && l == 1) (w2_fetch<0, 21>(c, hw)), NOP,
       if (JOBS) { jobs_put(c, jb); SBAR(); });
  QUAD(if (JOBS) jobs_publish(c), NOP, NOP,
       if (half == 1 && l == 0) { poll_any(c, FL_W2 + c.nb8, c.lane & 31); (w2_fetch<0, 21>(c, hw)); }, NOP);
  for (int it = 3; it < 8; ++it) QUAD(NOP, NOP, NOP, NOP, NOP);
#undef WBAR
  asm volatile("s_nop 1\n\tv_smfmac_f32_32x32x32_bf16 %[o1], %[pv1], v[88:95], %[idx]\n\ts_waitcnt lgkmcnt(0)\n\ts_nop 7\n\ts_nop 7"
               : [o1] "+v"(o1), "+v"(o0), "+v"(ka), "+v"(kb), "+v"(va0), "+v"(va1) : [pv1] "v"(vb1), "{v[80:95]}"(sB), [idx] "v"(sidx));
#undef QUAD
#undef KV4
  if (__any(!(o0[8] < 1.0995116e12f))) attn_slow(kp0, vp0, isv, q0, o0, m0);
  if (__any(!(o1[8] < 1.0995116e12f))) attn_slow(kp0, vp0, isv, q1, o1, m1);
  LAS f32x4* cmb = (LAS f32x4*)(c.lds + LD_CMB) + (c.w & 3) * 320 + c.lane;
  const unsigned w1src = WS_W1 + (c.nb < 11 ? 3 * c.nb : 33 + 2 * (c.nb - 11)) * 32768;
  if (half == 0) {
    __builtin_amdgcn_s_setprio(0);
    cmb[0] = (f32x4){o0[0], o0[1], o0[2], o0[3]}; cmb[64] = (f32x4){o0[4], o0[5], o0[6], o0[7]};
    cmb[128] = (f32x4){o1[0], o1[1], o1[2], o1[3]}; cmb[192] = (f32x4){o1[4], o1[5], o1[6], o1[7]};
    cmb[256] = (f32x4){o0[8], m0, o1[8], m1};
    arrive4(c, 12 + l);
    if (l == 0 && c.w == 3) code_rewarm(c);
    if (l == 0) poll_any(c, c.lane < 32 ? FL_W1 + c.nb : FL_W2 + c.nb8, c.lane < 32 ? (c.lane & 15) : (c.lane & 31));
    const unsigned wos = WS_WO + ((2 * c.nb8 + (c.w & 1)) * 32 + 8 * c.w) * 1024;
    const unsigned woo = WS_WO + ((2 * c.nb8 + 1 - (c.w & 1)) * 32 + 8 * c.w) * 1024;
#pragma unroll
    for (int k = 0; k < 8; ++k) { wo[2 * k] = ld16(c, c.voff, wos + k * 1024); wo[2 * k + 1] = ld16(c, c.voff, woo + k * 1024); }
    SBAR();
    wait4<true>(c, 12 + l);
    help_dma<8>(c, w1src, LD_W);
    if (c.nb < 11) help_dma<8>(c, w1src + 65536, LD_W + 65536);
  }
  BARRIER_LDS();
  if (half == 1) {
    const f32x4 lm = cmb[256];
#pragma unroll
    for (int u = 0; u < 2; ++u) {
      const f32x16& o = u ? o1 : o0; const float m = u ? m1 : m0;
      const f32x8 p2 = cat8(cmb[128 * u], cmb[128 * u + 64]);
      const float m2 = lm[2 * u + 1];
      const float mm = fmaxf(m, m2);
      const float f1 = __builtin_amdgcn_exp2f(m - mm), f2 = __builtin_amdgcn_exp2f(m2 - mm);
      const float inv = __builtin_amdgcn_rcpf(o[8] * f1 + lm[2 * u] * f2);
      const float g1 = f1 * inv, g2 = f2 * inv;
      unsigned p[4];
#pragma unroll
      for (int i = 0; i < 4; ++i) p[i] = pk2(o[2 * i] * g1 + p2[2 * i] * g2, o[2 * i + 1] * g1 + p2[2 * i + 1] * g2);
      st16(c, (u32x4){p[0], p[1], p[2], p[3]}, c.voff, lay + L_O + ((qt + u) * 32 + c.head) * 1024);
    }
    asm volatile("s_waitcnt vmcnt(0)" ::: "memory");
    if (c.lane < 8) __hip_atomic_store(flag_ptr(c, FL_O + l * 256 + (c.qb * 4 + (c.w & 3)) * 8 + c.lane, c.head), c.token, RLX_AGENT);
  }
}

DI u64 uniform64(u64 v) {
  return ((u64)__builtin_amdgcn_readfirstlane((unsigned)(v >> 32)) << 32) | (unsigned)__builtin_amdgcn_readfirstlane((unsigned)v);
}

DI void run_main(Ctx& c, const In& in, u64 pc) {
  const u64 epoch = __hip_atomic_load((gu64*)c.ws, RLX_AGENT);
  u32x4 res[2];
  {
    if (c.w == 0 && c.lane < 32) ((LAS unsigned*)(c.lds + LD_CTR))[c.lane] = 0u;
    const unsigned short* src = in.x + (size_t)((2 * c.tb64 + (c.w >> 1)) * 32 + c.r) * D_ + 32 * (2 * c.nb8 + (c.w & 1)) + 4 * c.h;
    u32x2 xr[4];
#pragma unroll
    for (int i = 0; i < 4; ++i) xr[i] = *(const u32x2*)(src + 8 * i);
    SBAR();
    if (c.w == 2) {
      if (c.lane == 0) *(LAS u64*)(c.lds + LD_CTR + 128) = pc;
      code_warm(c, pc);
    }
    if (c.w == 3) {
      const int i = c.lane & 15;
      const int line = i < 4 ? FL_X + i * 256 + c.tb * 16 + c.nb : i < 6 ? FL_H + (i & 1) * 256 + c.tb * 16 + c.nb
                     : i < 8 ? FL_Q + (i & 1) * 256 + c.head * 8 + c.qb : i < 12 ? FL_O + (i & 1) * 256 + c.tb64 * 8 + c.nb8
                     : i == 12 ? FL_WQ + c.nb * 16 + c.tb : i == 13 ? FL_W1 + c.nb : FL_W2 + c.nb8;
      unsigned sv;
      asm volatile("s_mov_b32 %0, m0\n\ts_mov_b32 m0, %3\n\ts_nop 2\n\tglobal_load_lds_dword %1, %2 sc1\n\ts_mov_b32 m0, %0"
                   : "=&s"(sv) : "v"(line * 256 + ((i == 10 || i == 11 || i == 15) ? 128 : 0)), "s"(c.ws), "s"(c.lbase + LD_SC) : "memory");
    }
    BARRIER_LDS();
    c.token = uniform64(epoch) + 1;
    res[0] = (u32x4){xr[0][0], xr[0][1], xr[1][0], xr[1][1]}; res[1] = (u32x4){xr[2][0], xr[2][1], xr[3][0], xr[3][1]};
    store_x(c, 0, res, unpack16(res));
    publish(c, 0, 4, FL_X + c.tb * 16, c.nb);
    poll<20>(c, (c.lane & 16) ? FL_WQ + c.nb * 16 + c.tb : FL_X + c.tb * 16 + c.nb, c.lane & 15);
    BARRIER_LDS();
    u32x4 dummy[16];
    fresh(c);
    phase_ln<0, true>(c, 0, 0, dummy);
  }
  for (int l = 0; l < 2; ++l) {
    u32x4 wo[16], w2r[16];
    if (l == 1) {
      fresh(c);
      u32x4 dummy[16];
      phase_ln<0, false>(c, l, 2 * l, dummy);
    }
    fresh(c);
    poll<10>(c, FL_Q + l * 256 + c.head * 8 + c.qb, c.lane & 15);
    BARRIER_LDS();
    { u32x4 unused[28]; if (l == 0) phase_attn<0, 0>(c, in, wo, unused); else phase_attn<0, 1>(c, in, wo, unused); }
    fresh(c);
    poll<32>(c, FL_O + l * 256 + c.tb64 * 8 + c.nb8, c.lane & 31);
    BARRIER_LDS();
    store_x(c, 2 * l + 1, res, phase_wo(c, l, wo, res));
    publish(c, 8 + l, 4, FL_X + (2 * l + 1) * 256 + c.tb * 16, c.nb);
    fresh(c);
    poll<12>(c, FL_X + (2 * l + 1) * 256 + c.tb * 16 + c.nb, c.lane & 15);
    BARRIER_ALL();
    phase_ln<1, false>(c, l, 2 * l + 1, w2r);
    fresh(c);
    poll<22>(c, FL_H + l * 256 + c.tb * 16 + c.nb, c.lane & 15);
    BARRIER_LDS();
    const f32x16 x2 = phase_w2(c, l, w2r, res);
    fresh(c);
    if (l == 0) {
      store_x(c, 2, res, x2);
      publish(c, 10, 4, FL_X + 2 * 256 + c.tb * 16, c.nb);
      poll<12>(c, FL_X + 2 * 256 + c.tb * 16 + c.nb, c.lane & 15);
      BARRIER_LDS();
    }
  }
  {
    auto s0 = __builtin_amdgcn_permlane32_swap(res[0][0], res[0][2], false, false), s1 = __builtin_amdgcn_permlane32_swap(res[0][1], res[0][3], false, false);
    auto s2 = __builtin_amdgcn_permlane32_swap(res[1][0], res[1][2], false, false), s3 = __builtin_amdgcn_permlane32_swap(res[1][1], res[1][3], false, false);
    unsigned short* op = in.out + (size_t)((2 * c.tb64 + (c.w >> 1)) * 32 + c.r) * D_ + 32 * (2 * c.nb8 + (c.w & 1)) + 8 * c.h;
    __builtin_nontemporal_store((u32x4){s0[0], s1[0], s0[1], s1[1]}, (u32x4*)op);
    __builtin_nontemporal_store((u32x4){s2[0], s3[0], s2[1], s3[1]}, (u32x4*)(op + 16));
  }
  if (c.bid == 0 && c.w == 0 && c.lane == 0) __hip_atomic_store((gu64*)c.ws, c.token, RLX_AGENT);
}

DI void run_help(Ctx& c, const In& in) {
  const u64 epoch = __hip_atomic_load((gu64*)c.ws, RLX_AGENT);
  {
    unsigned e0[8], e1[8];
    const int qk = 2 * c.tb + (c.w & 1), qa = (c.w >> 1) & 1;
    wfrag_load(in.wqkv, 3 * D_, qa * 16 + c.nb, qk, c.r, c.h, e0);
    wfrag_load(in.wqkv, 3 * D_, 32 + c.nb, qk, c.r, c.h, e1);
    BARRIER_LDS();
    c.token = uniform64(epoch) + 1;
    wfrag_store(c, e0, WS_WQKV + ((qa * 16 + c.nb) * 32 + qk) * 1024, true, WS_CSP + (c.nb * 32 + qk) * 384 + qa * 128);
    if (qa == 0) wfrag_store(c, e1, WS_WQKV + ((32 + c.nb) * 32 + qk) * 1024, true, WS_CSP + (c.nb * 32 + qk) * 384 + 256);
    publish(c, 1, 4, FL_WQ + c.nb * 16, c.tb);
    BARRIER_LDS();
    fresh(c);
#define WQC(q) _Pragma("unroll") for (int t_ = 0; t_ < 3; ++t_) \
    glds16x2(c.ws + WS_WQKV + ((16 * t_ + c.nb) * 32 + 8 * (q) + 2 * (c.w & 3)) * 1024, c.voff, c.lbase + LD_W + t_ * 32768 + (8 * (q) + 2 * (c.w & 3)) * 1024)
    WQC(0); WQC(1);
    asm volatile("s_waitcnt vmcnt(6)\n\ts_barrier" ::: "memory");
    WQC(2);
    asm volatile("s_waitcnt vmcnt(6)\n\ts_barrier" ::: "memory");
    WQC(3);
    asm volatile("s_waitcnt vmcnt(6)\n\ts_barrier" ::: "memory");
    if (c.w < 6) {
      float cv[32];
      colsum_load(c, 0, cv);
      asm volatile("s_waitcnt vmcnt(32)\n\ts_barrier" ::: "memory");
      colsum_put(c, 0, cv, 0u);
    } else {
      asm volatile("s_waitcnt vmcnt(0)\n\ts_barrier" ::: "memory");
    }
    BARRIER_LDS();
#undef WQC
  }
  for (int l = 0; l < 2; ++l) {
    u32x4 wo[16];
    fresh(c);
    BARRIER_LDS();
    fresh(c);
    u32x4 hw[28];
    if (l == 0) phase_attn<1, 0>(c, in, wo, hw); else phase_attn<1, 1>(c, in, wo, hw);
    fresh(c);
    BARRIER_LDS();
    asm volatile("s_barrier" ::: "memory");
    help_dma<8>(c, WS_W1 + (c.nb < 11 ? 3 * c.nb + 1 : 34 + 2 * (c.nb - 11)) * 32768, LD_W + 32768);
    w2_fetch<21, 28>(c, hw);
    asm volatile("s_barrier" ::: "memory");
    if (l == 0 && c.w < 6) {
      const int bcol = 32 * (c.nb < 11 ? 3 * c.nb : 33 + 2 * (c.nb - 11)) + (c.w - 4) * 64 + c.lane;
      const unsigned bv = in.b1[bcol < F_ ? bcol : 0];
      float cv[32];
      colsum_load(c, 1, cv);
      colsum_put(c, 1, cv, bv);
    }
    BARRIER_ALL();
    w2_put<21, 28>(c, hw);
    help_ln(c, l, [&]() __attribute__((always_inline)) { wait4<true>(c, 14 + l); w2_put<0, 12>(c, hw); });
    w2_put<12, 21>(c, hw);
    BARRIER_LDS();
    asm volatile("s_barrier" ::: "memory");
    if (l == 0) {
      fresh(c);
#pragma unroll
      for (int u = 0; u < 24; ++u) hw[u] = ld16(c, c.voff, WS_WQKV + (((u >> 3) * 16 + c.nb) * 32 + (u & 7) * 4 + (c.w & 3)) * 1024);
      SBAR();
      wait4<true>(c, 16);
#pragma unroll
      for (int u = 0; u < 24; ++u) *(LAS u32x4*)(c.lds + LD_W + (u * 4 + (c.w & 3)) * 1024 + c.voff) = hw[u];
      BARRIER_LDS();
    } else {
      asm volatile("s_barrier\n\ts_barrier" ::: "memory");
    }
  }
}

__global__ __launch_bounds__(512, 2) void mega(const unsigned short* __restrict__ x, const unsigned short* __restrict__ wqkv,
                                               const unsigned short* __restrict__ wo_g, const unsigned short* __restrict__ w1,
                                               const unsigned short* __restrict__ b1, const unsigned short* __restrict__ w2,
                                               char* __restrict__ ws, unsigned short* __restrict__ out) {
  extern __shared__ __attribute__((aligned(16))) char lds_[];
  Ctx c;
  c.ws = ws; c.lds = (LAS char*)lds_; c.rs = __builtin_amdgcn_make_buffer_rsrc(ws, 0, WS_END, 0x00020000);
  c.lbase = (unsigned)(size_t)c.lds;
  c.bid = blockIdx.x; c.lane = threadIdx.x & 63; c.w = __builtin_amdgcn_readfirstlane(threadIdx.x >> 6); c.dead = false;
  fresh(c);
  const In in = {x, wqkv, wo_g, w1, b1, w2, out};
  u64 pc;
  asm volatile("s_getpc_b64 %0" : "=s"(pc));
  if (c.w < 4) run_main(c, in, pc); else run_help(c, in);
}

extern "C" void kernel_launch(void* const* d_in, const int* in_sizes, int n_in, void* d_out, int out_size, void* d_ws,
                              size_t ws_size, hipStream_t stream) {
  (void)hipFuncSetAttribute((const void*)mega, hipFuncAttributeMaxDynamicSharedMemorySize, LDS_BYTES);
  mega<<<256, 512, LDS_BYTES, stream>>>((const unsigned short*)d_in[0], (const unsigned short*)d_in[1], (const unsigned short*)d_in[2],
                                        (const unsigned short*)d_in[4], (const unsigned short*)d_in[5], (const unsigned short*)d_in[6],
                                        (char*)d_ws, (unsigned short*)d_out);
}
```

```cpp
#include <hip/hip_runtime.h>
#include <hip/hip_bf16.h>

typedef __attribute__((ext_vector_type(2))) __bf16 bf16x2;
typedef __attribute__((ext_vector_type(8))) __bf16 bf16x8;
typedef __attribute__((ext_vector_type(16))) __bf16 bf16x16;
typedef __attribute__((ext_vector_type(16))) float f32x16;
typedef __attribute__((ext_vector_type(8))) float f32x8;
typedef __attribute__((ext_vector_type(4))) float f32x4;
typedef __attribute__((ext_vector_type(2))) float f32x2;
typedef __attribute__((ext_vector_type(4))) unsigned u32x4;
typedef __attribute__((ext_vector_type(2))) unsigned u32x2;
typedef unsigned long long u64;
#define LAS __attribute__((address_space(3)))
#define GAS __attribute__((address_space(1)))
typedef GAS u64 gu64;
#define DI __device__ __forceinline__
#define RLX_AGENT __ATOMIC_RELAXED, __HIP_MEMORY_SCOPE_AGENT

constexpr int D_ = 512, F_ = 1376;
constexpr int KS_F = F_ / 16;

constexpr unsigned MB = 1u << 20;
constexpr unsigned WS_FLAG = 0;
constexpr unsigned WS_WQKV = 1024 * 1024;
constexpr unsigned WS_WO = WS_WQKV + 48 * 32 * 1024;
constexpr unsigned WS_W1 = WS_WO + 16 * 32 * 1024;
constexpr unsigned WS_W2 = WS_W1 + 43 * 32 * 1024;
constexpr unsigned WS_CSP = WS_W2 + 16 * 86 * 1024;
constexpr unsigned WS_ST = WS_CSP + 2 * 16 * 32 * 384;
constexpr unsigned WS_X = WS_ST + 4 * 64 * 4096;
constexpr unsigned WS_L = WS_X + 8 * MB;
constexpr unsigned L_Q = 0, L_K = 2 * MB, L_V = 4 * MB, L_O = 6 * MB, L_H = 8 * MB, L_SZ = 14 * MB;
constexpr unsigned WS_END = WS_L + 2 * L_SZ;
constexpr int FL_X = 1;
constexpr int FL_H = FL_X + 1024;
constexpr int FL_Q = FL_H + 512;
constexpr int FL_O = FL_Q + 512;
constexpr int FL_WQ = FL_O + 512;
constexpr int FL_W1 = FL_WQ + 256;
constexpr int FL_W2 = FL_W1 + 16;
static_assert((FL_W2 + 8) * 256 <= WS_WQKV, "flag region");

constexpr int LD_W = 0;
constexpr int LD_V = 65536;
constexpr int LD_EX = 98304;
constexpr int LD_ONE = 131072;
constexpr int LD_CMB = 135168;
constexpr int LD_W2E = 122880;
constexpr int LD_SC = 151552;
constexpr int LD_ST = 155648;
constexpr int LD_PAR = 157696;
constexpr int LD_CTR = LD_PAR + 1536;
constexpr int LDS_BYTES = 163840;

#define MFMA(a, b, c) __builtin_amdgcn_mfma_f32_32x32x16_bf16((a), (b), (c), 0, 0, 0)
#define SBAR() __builtin_amdgcn_sched_barrier(0)
#define PIN(x) asm volatile("" : "+v"(x))
#define BARRIER_LDS() asm volatile("s_waitcnt lgkmcnt(0)\n\ts_barrier" ::: "memory")
#define BARRIER_ALL() asm volatile("s_waitcnt vmcnt(0) lgkmcnt(0)\n\ts_barrier" ::: "memory")

DI unsigned pk2(float lo, float hi) {
  bf16x2 v; v[0] = (__bf16)lo; v[1] = (__bf16)hi;
  return __builtin_bit_cast(unsigned, v);
}
DI float bf_lo(unsigned u) { return __uint_as_float(u << 16); }
DI float bf_hi(unsigned u) { return __uint_as_float(u & 0xffff0000u); }
DI float rbf(float x) { return (float)(__bf16)x; }
DI bf16x8 as_bf(u32x4 v) { return __builtin_bit_cast(bf16x8, v); }
DI float swap_half_sum(float x) {
  auto r = __builtin_amdgcn_permlane32_swap(__float_as_uint(x), __float_as_uint(x), false, false);
  return __uint_as_float(r[0]) + __uint_as_float(r[1]);
}
DI float swap_half_max(float x) {
  auto r = __builtin_amdgcn_permlane32_swap(__float_as_uint(x), __float_as_uint(x), false, false);
  return fmaxf(__uint_as_float(r[0]), __uint_as_float(r[1]));
}
DI void glds16x2(const char* sbase, unsigned voff, unsigned lds_base) {
  unsigned sv;
  asm volatile("s_mov_b32 %0, m0\n\ts_mov_b32 m0, %3\n\ts_nop 2\n\tglobal_load_lds_dwordx4 %1, %2 sc1\n\t"
               "global_load_lds_dwordx4 %1, %2 offset:1024 sc1\n\ts_mov_b32 m0, %0"
               : "=&s"(sv) : "v"(voff), "s"(sbase), "s"(lds_base) : "memory");
}
DI void glds16x4(const char* sbase, unsigned voff, unsigned lds_base) {
  unsigned sv;
  asm volatile("s_mov_b32 %0, m0\n\ts_mov_b32 m0, %3\n\ts_nop 2\n\tglobal_load_lds_dwordx4 %1, %2 sc1\n\t"
               "global_load_lds_dwordx4 %1, %2 offset:1024 sc1\n\tglobal_load_lds_dwordx4 %1, %2 offset:2048 sc1\n\t"
               "global_load_lds_dwordx4 %1, %2 offset:3072 sc1\n\ts_mov_b32 m0, %0"
               : "=&s"(sv) : "v"(voff), "s"(sbase), "s"(lds_base) : "memory");
}
template <int I> struct IC { static constexpr int v = I; };
template <int I, int N, class F> DI void static_for(F&& f) {
  if constexpr (I < N) { f(IC<I>{}); static_for<I + 1, N>(f); }
}

struct Ctx {
  char* ws; LAS char* lds; __amdgpu_buffer_rsrc_t rs;
  u64 token; unsigned lbase, voff;
  int bid, lane, w, r, h, tb, nb, tb64, nb8, head, qb;
  bool dead;
};
DI void fresh(Ctx& c) {
  asm volatile("" : "+s"(c.bid));
  asm volatile("" : "+s"(c.w));
  asm volatile("" : "+v"(c.lane));
  const int g = c.bid & 7, idx = c.bid >> 3;
  c.r = c.lane & 31; c.h = c.lane >> 5; c.voff = c.lane * 16;
  c.tb = 2 * g + (idx >> 4); c.nb = idx & 15; c.tb64 = 4 * g + (idx >> 3); c.nb8 = idx & 7;
  c.head = 4 * g + (idx & 3); c.qb = idx >> 2;
}
DI u32x4 ld16(const Ctx& c, unsigned voff, unsigned soff) { return __builtin_amdgcn_raw_buffer_load_b128(c.rs, voff, soff, 16); }
DI float ld4(const Ctx& c, unsigned voff, unsigned soff) { return __uint_as_float(__builtin_amdgcn_raw_buffer_load_b32(c.rs, voff, soff, 16)); }
DI void st16(const Ctx& c, u32x4 v, unsigned voff, unsigned soff) { __builtin_amdgcn_raw_buffer_store_b128(v, c.rs, voff + soff, 0, 16); }
DI void st4(const Ctx& c, float v, unsigned voff, unsigned soff) { __builtin_amdgcn_raw_buffer_store_b32(__float_as_uint(v), c.rs, voff, soff, 16); }
DI gu64* flag_ptr(const Ctx& c, int line, int slot) { return (gu64*)(c.ws + WS_FLAG) + line * 32 + slot; }

DI void publish(const Ctx& c, int ctr, unsigned n, int line0, int slot) {
  asm volatile("s_waitcnt vmcnt(0)" ::: "memory");
  unsigned old = 0;
  if (c.lane == 0) old = __hip_atomic_fetch_add((LAS unsigned*)(c.lds + LD_CTR) + ctr, 1u, __ATOMIC_RELAXED, __HIP_MEMORY_SCOPE_WORKGROUP);
  if (__builtin_amdgcn_readfirstlane(old) == n - 1 && c.lane < 16) __hip_atomic_store(flag_ptr(c, line0 + c.lane, slot), c.token, RLX_AGENT);
}
DI void code_warm(const Ctx& c, u64 pc) {
  unsigned sv;
  asm volatile("s_mov_b32 %0, m0\n\ts_mov_b32 m0, %3\n\ts_nop 2\n\tglobal_load_lds_dword %1, %2\n\ts_mov_b32 m0, %0"
               : "=&s"(sv) : "v"(min((c.bid >> 3) * 1792 + (c.lane & 15) * 128, 56 * 1024)), "s"(pc), "s"(c.lbase + LD_CTR + 256) : "memory");
}
DI void code_rewarm(const Ctx& c) {
  const u64 v = *(const LAS u64*)(c.lds + LD_CTR + 128);
  code_warm(c, ((u64)__builtin_amdgcn_readfirstlane((unsigned)(v >> 32)) << 32) | (unsigned)__builtin_amdgcn_readfirstlane((unsigned)v));
}
DI void poll_any(Ctx& c, int line, int slot) {
  if (!c.dead) {
    gu64* p = flag_ptr(c, line, slot);
    unsigned spins = 0;
    while (!__all(__hip_atomic_load(p, RLX_AGENT) == c.token)) {
      if (++spins > (1u << 18)) { c.dead = true; break; }
    }
    __builtin_amdgcn_fence(__ATOMIC_ACQUIRE, "wavefront");
  }
}
template <bool NAP = false> DI void wait4(const Ctx& c, int ctr) {
  while (__hip_atomic_load((LAS unsigned*)(c.lds + LD_CTR) + ctr, __ATOMIC_RELAXED, __HIP_MEMORY_SCOPE_WORKGROUP) < 4u)
    if (NAP) __builtin_amdgcn_s_sleep(2);
  asm volatile("" ::: "memory");
}
DI void arrive4(const Ctx& c, int ctr) {
  asm volatile("s_waitcnt lgkmcnt(0)" ::: "memory");
  if (c.lane == 0) __hip_atomic_fetch_add((LAS unsigned*)(c.lds + LD_CTR) + ctr, 1u, __ATOMIC_RELAXED, __HIP_MEMORY_SCOPE_WORKGROUP);
}
template <int NAP> DI void poll(Ctx& c, int line, int slot) {
  if (c.w == 0) {
    __builtin_amdgcn_s_sleep(NAP);
    poll_any(c, line, slot);
  }
}
template <int NQ> DI void help_dma(const Ctx& c, unsigned src, int dst) {
#pragma unroll
  for (int u = 0; u < NQ / 4; ++u) {
    const int p = (c.w & 3) + 4 * u;
    glds16x4(c.ws + src + p * 4096, c.voff, c.lbase + dst + p * 4096);
  }
}
DI int w2_run(int run) { return run < 6 ? LD_W + run * 14336 : LD_W2E + (run - 6) * 14336; }
template <int U0, int U1> DI void w2_fetch(const Ctx& c, u32x4 (&h)[28]) {
#pragma unroll
  for (int u = U0; u < U1; ++u) {
    const int f = (c.w & 3) + 4 * u, run = f / 14, kq = run >> 1;
    h[u] = ld16(c, c.voff, WS_W2 + ((2 * c.nb8 + (run & 1)) * KS_F + kq * 21 + (kq < 2 ? kq : 2) + f % 14) * 1024);
  }
  SBAR();
}
template <int U0, int U1> DI void w2_put(const Ctx& c, const u32x4 (&h)[28]) {
#pragma unroll
  for (int u = U0; u < U1; ++u) {
    const int f = (c.w & 3) + 4 * u;
    *(LAS u32x4*)(c.lds + (u < 21 ? LD_W : LD_W2E - 84 * 1024) + f * 1024 + c.voff) = h[u];
  }
}

DI f32x16 unpack16(const u32x4 (&res)[2]) {
  f32x16 v;
#pragma unroll
  for (int i = 0; i < 8; ++i) { const unsigned u = res[i >> 2][i & 3]; v[2 * i] = bf_lo(u); v[2 * i + 1] = bf_hi(u); }
  return v;
}
DI void pack16(const f32x16& v, u32x4 (&res)[2]) {
#pragma unroll
  for (int i = 0; i < 8; ++i) res[i >> 2][i & 3] = pk2(v[2 * i], v[2 * i + 1]);
}
DI void store_x(const Ctx& c, int inst, u32x4 (&res)[2], const f32x16& v) {
  const int tt = 2 * c.tb64 + (c.w >> 1), nt = 2 * c.nb8 + (c.w & 1);
  st16(c, res[0], c.voff, WS_X + inst * (2 * MB) + (tt * 32 + 2 * nt) * 1024);
  st16(c, res[1], c.voff, WS_X + inst * (2 * MB) + (tt * 32 + 2 * nt + 1) * 1024);
  SBAR();
  f32x2 a1 = {v[0], v[1]}, a2 = a1 * a1;
#pragma unroll
  for (int i = 1; i < 8; ++i) { const f32x2 t = {v[2 * i], v[2 * i + 1]}; a1 += t; a2 += t * t; }
  auto r1 = __builtin_amdgcn_permlane32_swap(__float_as_uint(a1[0] + a1[1]), __float_as_uint(a2[0] + a2[1]), false, false);
  st4(c, __uint_as_float(r1[0]) + __uint_as_float(r1[1]), c.lane * 4, WS_ST + inst * (64 * 4096) + tt * 4096 + nt * 256);
  PIN(res[0]); PIN(res[1]);
}

DI void wfrag_load(const unsigned short* W, int N, int nt, int ks, int r, int h, unsigned (&e)[8]) {
#pragma unroll
  for (int j = 0; j < 8; ++j) e[j] = W[(size_t)(16 * ks + 8 * (j >> 2) + 4 * h + (j & 3)) * N + 32 * nt + r];
}
DI void wfrag_store(const Ctx& c, const unsigned (&e)[8], unsigned dst, bool docs, unsigned csdst) {
  u32x4 v;
#pragma unroll
  for (int j = 0; j < 4; ++j) v[j] = e[2 * j] | (e[2 * j + 1] << 16);
  st16(c, v, c.voff, dst);
  if (docs) {
    float s = 0.f;
#pragma unroll
    for (int j = 0; j < 8; ++j) s += __uint_as_float(e[j] << 16);
    s = swap_half_sum(s);
    if (c.h == 0) st4(c, s, c.r * 4, csdst);
  }
}

DI u32x4 mish8(const f32x8& a, float rs) {
  float v[8];
#pragma unroll
  for (int j = 0; j < 8; ++j) {
    const float u = __builtin_amdgcn_exp2f(a[j] * (rs * 1.4426950408889634f));
    v[j] = a[j] * fmaf(-2.f * rs, __builtin_amdgcn_rcpf(fmaf(u, u + 2.f, 2.f)), rs);
  }
  return (u32x4){pk2(v[0], v[1]), pk2(v[2], v[3]), pk2(v[4], v[5]), pk2(v[6], v[7])};
}
#define LO8(s) __builtin_shufflevector(s, s, 0, 1, 2, 3, 4, 5, 6, 7)
#define HI8(s) __builtin_shufflevector(s, s, 8, 9, 10, 11, 12, 13, 14, 15)
DI f32x8 cat8(f32x4 a, f32x4 b) { return __builtin_shufflevector(a, b, 0, 1, 2, 3, 4, 5, 6, 7); }

template <int T, int PARTS = 3>
DI void ln_tile(const Ctx& c, int l, const f32x8& lo, const f32x8& hi, float rstd) {
  const int tt = 4 * c.tb + (c.w & 3);
  const unsigned so = WS_L + l * L_SZ + L_H + (tt * KS_F + 2 * (c.nb < 11 ? 3 * c.nb : 33 + 2 * (c.nb - 11)) + 2 * T) * 1024;
  if (PARTS & 1) st16(c, mish8(lo, rstd), c.voff, so);
  if (PARTS & 2) st16(c, mish8(hi, rstd), c.voff, so + 1024);
}
template <int MODE> DI void ln_publish(const Ctx& c, int l) {
  if (MODE == 0) publish(c, 4 + l, 4, FL_Q + l * 256 + c.nb * 16, c.tb);
  else publish(c, 6 + l, 8, FL_H + l * 256 + c.tb * 16, c.nb);
}

DI void pin16(f32x16& v) { PIN(v); }
DI void pinu(unsigned v) { asm volatile("" ::"v"(v)); }
DI u32x4 wfrag(const LAS char* base, int ks) { return *(const LAS u32x4*)(base + ks * 1024); }
DI void put16(LAS f32x4* ex, const f32x16& a) {
#pragma unroll
  for (int g = 0; g < 4; ++g) ex[g * 64] = (f32x4){a[4 * g], a[4 * g + 1], a[4 * g + 2], a[4 * g + 3]};
}

template <int MODE, bool FIRST>
DI void phase_ln(Ctx& c, int l, int inst, u32x4 (&w2r)[16]) {
  const int tt = 4 * c.tb + c.w;
  const unsigned xs = WS_X + inst * (2 * MB) + tt * 32768;
  constexpr int PD = 14;
  u32x4 xf[32], sp[4];
#pragma unroll
  for (int k = 0; k < PD; ++k) xf[k] = ld16(c, c.voff, xs + k * 1024);
  SBAR();
  const LAS char* wl = c.lds + LD_W + c.lane * 16;
  const LAS char* wl2 = wl + 65536;
  asm volatile("" : "+v"(wl2));
  f32x16 aA, aB;
#pragma unroll
  for (int i = 0; i < 16; ++i) { aA[i] = 0.f; aB[i] = 0.f; }
  u32x4 fa[3], fb[3];
  if (FIRST) BARRIER_LDS();
#pragma unroll
  for (int k = 0; k < 2; ++k) { fa[k] = wfrag(wl, k); fb[k] = wfrag(wl + 32768, k); }
  float rstd = 0.f, sc4[4];
  u32x2 we[3]; u32x4 xe;
  LAS float* sc = (LAS float*)(c.lds + LD_SC) + c.w * 256;
  const int k0q = c.w * 21 + (c.w < 2 ? c.w : 2);
  const unsigned w2s = WS_W2 + ((2 * c.nb8 + (c.w & 1)) * KS_F + k0q + 14) * 1024;
  const unsigned w2o = WS_W2 + ((2 * c.nb8 + 1 - (c.w & 1)) * KS_F + k0q + 14) * 1024;
  auto w2load = [&](int i) __attribute__((always_inline)) {
    w2r[i] = ld16(c, c.voff, ((i & 1) ? w2o : w2s) + (((i >> 1) == 7 && c.w >= 2) ? 6 : (i >> 1)) * 1024);
  };
  static_for<0, 32>([&](auto kc) __attribute__((always_inline)) {
    constexpr int k = decltype(kc)::v;
    if constexpr (FIRST && (((k & 7) == 6 && k < 24) || k == 31)) asm volatile("s_barrier" ::: "memory");
    aA = MFMA(as_bf(fa[k % 3]), as_bf(xf[k]), aA);
    pin16(aA); SBAR();
    if constexpr (k + 2 < 32) fa[(k + 2) % 3] = wfrag(wl, k + 2);
    if constexpr (k + PD < 32) xf[k + PD] = ld16(c, c.voff, xs + (k + PD) * 1024);
    if constexpr (k + PD >= 32 && k + PD < 36) sp[k + PD - 32] = ld16(c, c.voff, WS_ST + inst * (64 * 4096) + tt * 4096 + (k + PD - 32) * 1024);
    if constexpr (MODE == 1 && k >= 24) w2load(k - 24);
    SBAR();
    aB = MFMA(as_bf(fb[k % 3]), as_bf(xf[k]), aB);
    pin16(aB); SBAR();
    if constexpr (k + 2 < 32) fb[(k + 2) % 3] = wfrag(wl + 32768, k + 2);
    if constexpr (k == 29) {
      ((LAS f32x4*)sc)[c.lane] = (__builtin_bit_cast(f32x4, sp[0]) + __builtin_bit_cast(f32x4, sp[1])) +
                                 (__builtin_bit_cast(f32x4, sp[2]) + __builtin_bit_cast(f32x4, sp[3]));
#pragma unroll
      for (int i = 0; i < 4; ++i) sc4[i] = sc[i * 64 + c.lane];
    }
    if constexpr (k == (FIRST ? 31 : 30)) {
#pragma unroll
      for (int i = 0; i < 3; ++i) we[i] = ((const LAS u32x2*)(c.lds + LD_PAR))[MODE * 96 + 32 * i + c.r];
    }
    if constexpr (k == 31) {
      const float s = (sc4[0] + sc4[1]) + (sc4[2] + sc4[3]);
      auto rr = __builtin_amdgcn_permlane32_swap(__float_as_uint(s), __float_as_uint(s), false, false);
      const float mu = __uint_as_float(rr[0]) * (1.f / D_);
      const float var = fmaxf(__uint_as_float(rr[1]) * (1.f / D_) - mu * mu, 0.f);
      const float sd = rbf(__builtin_amdgcn_sqrtf(rbf(rbf(var) + 1e-5f)));
      rstd = __builtin_amdgcn_rcpf(sd);
      ((LAS float*)(c.lds + LD_ST))[c.w * 32 + c.r] = rstd;
      xe = (u32x4){c.h ? 0u : pk2(-mu, -mu), c.h ? 0u : pk2(-mu, sd), 0u, 0u};
    }
    SBAR();
  });
  aB = MFMA(as_bf((u32x4){we[1][0], we[1][1], 0u, 0u}), as_bf(xe), aB);
  aA = MFMA(as_bf((u32x4){we[0][0], we[0][1], 0u, 0u}), as_bf(xe), aA);
  f32x16 aC;
#pragma unroll
  for (int i = 0; i < 16; ++i) aC[i] = 0.f;
  aC = MODE == 0 ? MFMA(as_bf(xe), as_bf((u32x4){we[2][0], we[2][1], 0u, 0u}), aC) : MFMA(as_bf((u32x4){we[2][0], we[2][1], 0u, 0u}), as_bf(xe), aC);
  constexpr int CD = 5;
  u32x4 fc[CD + 1];
#pragma unroll
  for (int k = 0; k < CD; ++k) fc[k] = wfrag(wl2, k);
  SBAR();
  if (MODE == 1) {
    LAS f32x4* ex = (LAS f32x4*)(c.lds + LD_EX) + c.w * 384 + c.lane;
    put16(ex, aB);
    ex[256] = (f32x4){aA[8], aA[9], aA[10], aA[11]}; ex[320] = (f32x4){aA[12], aA[13], aA[14], aA[15]};
    BARRIER_LDS();
  }
  f32x4 ts[4];
  u32x4 pw;
  const float rq = rstd * (0.25f * 1.4426950408889634f);
  const unsigned vok = c.h * 512 + ((c.r & 19) | ((c.r & 4) << 1) | ((c.r & 8) >> 1)) * 16;
  const unsigned soq = WS_L + l * L_SZ + L_Q + (2 * c.nb * 64 + tt) * 1024;
  static_for<0, 32>([&](auto kc) __attribute__((always_inline)) {
    constexpr int k = decltype(kc)::v;
    aC = MODE == 0 ? MFMA(as_bf(xf[k]), as_bf(fc[k % (CD + 1)]), aC) : MFMA(as_bf(fc[k % (CD + 1)]), as_bf(xf[k]), aC);
    pin16(aC); SBAR();
    if constexpr (k + CD < 32) fc[(k + CD) % (CD + 1)] = wfrag(wl2, k + CD);
    if constexpr (MODE == 1 && (k & 1) == 0 && 8 + k / 2 < 16) w2load(8 + k / 2);
    if constexpr (MODE == 0 && k >= 6 && k <= 18 && (k & 3) == 2)
      st16(c, pw, k < 12 ? vok : c.voff, (k < 12 ? soq + (L_K - L_Q) : soq) + ((k & 4) ? 0 : 65536));
    if constexpr (MODE == 0 && k >= 2 && k < 18) {
      constexpr int i = k - 2;
      const f32x16& a = i < 8 ? aB : aA;
      const float rs = i < 8 ? rstd : rq;
      pw[i & 3] = pk2(a[2 * (i & 7)] * rs, a[2 * (i & 7) + 1] * rs);
      pinu(pw[i & 3]);
    }
    if constexpr (MODE == 0 && k == 29) {
#pragma unroll
      for (int g = 0; g < 4; ++g) ts[g] = *(const LAS f32x4*)((const LAS float*)(c.lds + LD_ST) + c.w * 32 + 8 * g + 4 * c.h);
    }
    SBAR();
  });
  if (MODE == 1) arrive4(c, 14 + l);
  if (MODE == 1) { ln_tile<0, 1>(c, l, LO8(aA), LO8(aA), rstd); if (c.nb < 11) ln_tile<2>(c, l, LO8(aC), HI8(aC), rstd); }
  else {
    unsigned q[8];
#pragma unroll
    for (int g = 0; g < 8; ++g) q[g] = pk2(aC[2 * g] * ts[g >> 1][2 * (g & 1)], aC[2 * g + 1] * ts[g >> 1][2 * (g & 1) + 1]);
    const unsigned vo = (c.r >> 4) * 65536 + c.h * 256 + (c.r & 15) * 16, so = WS_L + l * L_SZ + L_V + (2 * c.nb * 128 + 2 * tt) * 512;
    st16(c, (u32x4){q[0], q[1], q[2], q[3]}, vo, so);
    st16(c, (u32x4){q[4], q[5], q[6], q[7]}, vo, so + 512);
  }
  ln_publish<MODE>(c, l);
}
template <class F>
DI void help_ln(Ctx& c, int l, F&& mid) {
  BARRIER_LDS();
  const LAS f32x4* ex = (const LAS f32x4*)(c.lds + LD_EX) + (c.w & 3) * 384 + c.lane;
  const float rstd = ((const LAS float*)(c.lds + LD_ST))[(c.w & 3) * 32 + c.r];
  const f32x8 b0 = cat8(ex[0], ex[64]), b1 = cat8(ex[128], ex[192]), a1 = cat8(ex[256], ex[320]);
  SBAR();
  ln_tile<1>(c, l, b0, b1, rstd);
  ln_tile<0, 2>(c, l, a1, a1, rstd);
  SBAR();
  mid();
  ln_publish<1>(c, l);
}
DI void colsum_load(const Ctx& c, int mode, float (&v)[32]) {
  const int t = min((c.w - 4) * 64 + c.lane, 95);
#pragma unroll
  for (int i = 0; i < 32; ++i) v[i] = ld4(c, t * 4, WS_CSP + ((mode * 16 + c.nb) * 32 + i) * 384);
  SBAR();
}
DI void colsum_put(const Ctx& c, int mode, const float (&v)[32], unsigned bias) {
  const int t = (c.w - 4) * 64 + c.lane;
  float s = 0.f;
#pragma unroll
  for (int i = 0; i < 32; i += 4) s += (v[i] + v[i + 1]) + (v[i + 2] + v[i + 3]);
  const float s0 = rbf(s), s1 = rbf(s - s0);
  if (t < 96) ((LAS u32x2*)(c.lds + LD_PAR))[mode * 96 + t] = (u32x2){pk2(s0, s1), (pk2(s - s0 - s1, 0.f) & 0xffffu) | (bias << 16)};
}

DI f32x16 reduce_res(Ctx& c, const f32x16 (&a)[4], u32x4 (&res)[2], int ctr) {
  LAS f32x4* red = (LAS f32x4*)(c.lds + LD_EX) + c.lane;
#pragma unroll
  for (int j = 1; j < 4; ++j) {
    LAS f32x4* p = red + ((c.w ^ j) * 3 + j - 1) * 256;
#pragma unroll
    for (int g = 0; g < 4; ++g) p[g * 64] = (f32x4){a[j][4 * g], a[j][4 * g + 1], a[j][4 * g + 2], a[j][4 * g + 3]};
  }
  if (ctr >= 0) { arrive4(c, ctr); wait4(c, ctr); } else BARRIER_LDS();
  const LAS f32x4* q = red + c.w * 768;
  f32x16 v;
#pragma unroll
  for (int g = 0; g < 4; ++g) {
    const f32x4 s = ((f32x4){a[0][4 * g], a[0][4 * g + 1], a[0][4 * g + 2], a[0][4 * g + 3]} + q[g * 64]) + (q[256 + g * 64] + q[512 + g * 64]);
#pragma unroll
    for (int j = 0; j < 4; ++j) v[4 * g + j] = s[j];
  }
  pack16(v, res);
  return v;
}
#define MM4(w0, w1, x0, x1) do { a[0] = MFMA(as_bf(w0), as_bf(x0), a[0]); a[1] = MFMA(as_bf(w1), as_bf(x0), a[1]); \
                                 a[2] = MFMA(as_bf(w0), as_bf(x1), a[2]); a[3] = MFMA(as_bf(w1), as_bf(x1), a[3]); } while (0)

DI f32x16 phase_wo(Ctx& c, int l, const u32x4 (&wo)[16], u32x4 (&res)[2]) {
  const unsigned xs = WS_L + l * L_SZ + L_O + ((2 * c.tb64 + (c.w >> 1)) * 32 + 8 * c.w) * 1024;
  const unsigned xo = WS_L + l * L_SZ + L_O + ((2 * c.tb64 + 1 - (c.w >> 1)) * 32 + 8 * c.w) * 1024;
  u32x4 xf[16];
#pragma unroll
  for (int k = 0; k < 8; ++k) { xf[2 * k] = ld16(c, c.voff, xs + k * 1024); xf[2 * k + 1] = ld16(c, c.voff, xo + k * 1024); }
  asm volatile("s_barrier" ::: "memory");
  SBAR();
  f32x16 a[4];
#pragma unroll
  for (int i = 16; i < 64; ++i) a[i >> 4][i & 15] = 0.f;
  a[0] = unpack16(res);
#pragma unroll
  for (int k = 0; k < 8; ++k) { MM4(wo[2 * k], wo[2 * k + 1], xf[2 * k], xf[2 * k + 1]); PIN(a[0]); PIN(a[1]); PIN(a[2]); PIN(a[3]); SBAR(); }
  return reduce_res(c, a, res, -1);
}

DI f32x16 phase_w2(Ctx& c, int l, const u32x4 (&w2r)[16], u32x4 (&res)[2]) {
  const int k0 = c.w * 21 + (c.w < 2 ? c.w : 2);
  const unsigned xs = WS_L + l * L_SZ + L_H + ((2 * c.tb64 + (c.w >> 1)) * KS_F + k0) * 1024;
  const unsigned xo = WS_L + l * L_SZ + L_H + ((2 * c.tb64 + 1 - (c.w >> 1)) * KS_F + k0) * 1024;
  constexpr int WIN = 10;
  u32x4 xf[2 * WIN];
#pragma unroll
  for (int k = 0; k < WIN; ++k) { xf[2 * k] = ld16(c, c.voff, xs + k * 1024); xf[2 * k + 1] = ld16(c, c.voff, xo + k * 1024); }
  SBAR();
  const LAS u32x4* w0 = (const LAS u32x4*)(c.lds + w2_run(2 * c.w + (c.w & 1))) + c.lane;
  const LAS u32x4* w1 = (const LAS u32x4*)(c.lds + w2_run(2 * c.w + 1 - (c.w & 1))) + c.lane;
  f32x16 a[4];
#pragma unroll
  for (int i = 16; i < 64; ++i) a[i >> 4][i & 15] = 0.f;
  a[0] = unpack16(res);
  u32x4 f0[2], f1[2];
  f0[0] = w0[0]; f1[0] = w1[0];
  static_for<0, 22>([&](auto kc) __attribute__((always_inline)) {
    constexpr int k = decltype(kc)::v;
    const u32x4 g0 = k < 14 ? f0[k & 1] : w2r[k < 14 ? 0 : 2 * (k - 14)], g1 = k < 14 ? f1[k & 1] : w2r[k < 14 ? 0 : 2 * (k - 14) + 1];
    const u32x4 x0 = xf[2 * (k % WIN)], x1 = xf[2 * (k % WIN) + 1];
    constexpr int kn = k - 1 + WIN;
    const int kk = (kn == 21 && c.w >= 2) ? 20 : kn;
    if constexpr (kn == 22) asm volatile("s_barrier" ::: "memory");
    if (k < 21 || c.w < 2) {
      a[0] = MFMA(as_bf(g0), as_bf(x0), a[0]);
      pin16(a[0]); SBAR();
      if constexpr (k >= 1 && kn < 22) xf[2 * ((k - 1) % WIN)] = ld16(c, c.voff, xs + kk * 1024);
      SBAR();
      a[1] = MFMA(as_bf(g1), as_bf(x0), a[1]);
      pin16(a[1]); SBAR();
      if constexpr (k + 1 < 14) f0[(k + 1) & 1] = w0[(k + 1) * 64];
      SBAR();
      a[2] = MFMA(as_bf(g0), as_bf(x1), a[2]);
      pin16(a[2]); SBAR();
      if constexpr (k >= 1 && kn < 22) xf[2 * ((k - 1) % WIN) + 1] = ld16(c, c.voff, xo + kk * 1024);
      SBAR();
      a[3] = MFMA(as_bf(g1), as_bf(x1), a[3]);
      pin16(a[3]); SBAR();
      if constexpr (k + 1 < 14) f1[(k + 1) & 1] = w1[(k + 1) * 64];
      SBAR();
    }
  });
  if (l == 0) { arrive4(c, 16); wait4(c, 16); } else BARRIER_LDS();
  return reduce_res(c, a, res, l == 0 ? 17 : -1);
}

#define SMF(a, b8, c) __builtin_amdgcn_smfmac_f32_32x32x32_bf16((a), __builtin_bit_cast(bf16x16, b8), (c), sidx, 0, 0)
#define LDF(p) (*(const LAS bf16x8*)(p))
DI float max16(const f32x16& s) {
  float a = fmaxf(fmaxf(s[0], s[1]), s[2]), b = fmaxf(fmaxf(s[3], s[4]), s[5]);
  a = fmaxf(fmaxf(a, s[6]), s[7]); b = fmaxf(fmaxf(b, s[8]), s[9]);
  a = fmaxf(fmaxf(a, s[10]), s[11]); b = fmaxf(fmaxf(b, s[12]), s[13]);
  a = fmaxf(fmaxf(a, s[14]), s[15]);
  return swap_half_max(fmaxf(a, b));
}
DI void attn_slow(const LAS char* kp, const LAS char* vp, bool isv, bf16x8 q, f32x16& o, float& m) {
  const int sidx = 0xDDDDDDDD;
  f32x16 zero;
#pragma unroll
  for (int i = 0; i < 16; ++i) zero[i] = 0.f;
  o = zero; m = -3.0e38f;
  for (int t = 0; t < 32; ++t) {
    f32x16 s = MFMA(LDF(kp + t * 1024), q, zero);
    const float mn = fmaxf(m, max16(s));
    const float al = __builtin_amdgcn_exp2f(m - mn);
    m = mn;
#pragma unroll
    for (int i = 0; i < 16; ++i) { o[i] *= al; s[i] = __builtin_amdgcn_exp2f(s[i] - mn); }
    const int vo = isv ? t * 1024 : 0;
    o = SMF(LDF(vp + vo), LO8(s), o);
    o = SMF(LDF(vp + vo + 512), HI8(s), o);
  }
}

struct In { const unsigned short *x, *wqkv, *wo, *w1, *b1, *w2; unsigned short* out; };
struct Jobs { unsigned e[4][8], dst[4]; bool ok[4]; };
DI void jobs_load(const Ctx& c, const In& in, Jobs& jb) {
  const int t0 = c.nb < 11 ? 3 * c.nb : 33 + 2 * (c.nb - 11), nt1 = c.nb < 11 ? 3 : 2;
  const unsigned short* W[4]; int N[4];
#pragma unroll
  for (int u = 0; u < 4; ++u) {
    const int j = c.w + 4 * u;
    int nt, ks;
    if (j < 6) { W[u] = in.w1; N[u] = F_; jb.ok[u] = (j >> 1) < nt1; nt = t0 + (jb.ok[u] ? j >> 1 : 0); ks = 2 * c.tb + (j & 1); jb.dst[u] = WS_W1 + (nt * 32 + ks) * 1024; }
    else if (j < 8) { W[u] = in.wo; N[u] = D_; jb.ok[u] = true; nt = 2 * c.nb8 + (j & 1); ks = c.tb64; jb.dst[u] = WS_WO + (nt * 32 + ks) * 1024; }
    else { W[u] = in.w2; N[u] = D_; ks = c.tb64 + 32 * ((j - 8) >> 1); jb.ok[u] = ks < KS_F; if (!jb.ok[u]) ks = 0; nt = 2 * c.nb8 + (j & 1); jb.dst[u] = WS_W2 + (nt * KS_F + ks) * 1024; }
    W[u] += 16 * ks * N[u] + 32 * nt;
    asm volatile("" : "+s"(W[u]));
    asm volatile("" : "+s"(N[u]));
  }
#pragma unroll
  for (int u = 0; u < 4; ++u) {
    const __amdgpu_buffer_rsrc_t rw = __builtin_amdgcn_make_buffer_rsrc((void*)W[u], 0, 32 * N[u], 0x00020000);
    const unsigned o = (4 * c.h * N[u] + c.r) * 2;
#pragma unroll
    for (int j = 0; j < 8; ++j) jb.e[u][j] = __builtin_amdgcn_raw_buffer_load_b16(rw, o, (8 * (j >> 2) + (j & 3)) * 2 * N[u], 0);
  }
}
DI void jobs_put(const Ctx& c, Jobs& jb) {
#pragma unroll
  for (int i = 0; i < 32; ++i) PIN(jb.e[i >> 3][i & 7]);
#pragma unroll
  for (int u = 0; u < 4; ++u) {
    const int j = c.w + 4 * u;
    if (jb.ok[u]) wfrag_store(c, jb.e[u], jb.dst[u], j < 6, WS_CSP + ((16 + c.nb) * 32 + 2 * c.tb + (j & 1)) * 384 + (j >> 1) * 128);
  }
}
DI void jobs_publish(const Ctx& c) {
  asm volatile("s_waitcnt vmcnt(0)" ::: "memory");
  unsigned old = 0;
  if (c.lane == 0) old = __hip_atomic_fetch_add((LAS unsigned*)(c.lds + LD_CTR) + 2, 1u, __ATOMIC_RELAXED, __HIP_MEMORY_SCOPE_WORKGROUP);
  if (__builtin_amdgcn_readfirstlane(old) == 3 && c.lane < 2)
    __hip_atomic_store(c.lane ? flag_ptr(c, FL_W1 + c.nb, c.tb) : flag_ptr(c, FL_W2 + c.nb8, c.tb64), c.token, RLX_AGENT);
}

#define EX1(r) "v_exp_f32 v" #r ", v" #r "\n\t"
#define ASTEP(KOFF, VOFF, kc_, kn_, v0_, v1_, nv0_, pv1_)                                                                  \
  asm volatile("s_waitcnt lgkmcnt(0)\n\t" EX1(64) EX1(65)                                                                  \
               "v_smfmac_f32_32x32x32_bf16 %[o1], %[pv1], v[88:95], %[idx]\n\t"                                            \
               "ds_read_b128 %[nv0], %[vp] offset:%[vo]\n\tds_read_b128 %[pv1], %[vp] offset:%[vo1]\n\t"                   \
               EX1(66) EX1(67) EX1(68)                                                                                     \
               "v_mfma_f32_32x32x16_bf16 v[80:95], %[kc], %[q1], %[c1]\n\t"                                                \
               "ds_read_b128 %[kc], %[kp] offset:%[ko]\n\t"                                                                \
               EX1(69) EX1(70) EX1(71) EX1(72) EX1(73)                                                                     \
               "v_smfmac_f32_32x32x32_bf16 %[o0], %[v0], v[64:71], %[idx]\n\t"                                             \
               EX1(74) EX1(75) EX1(76) EX1(77) EX1(78) EX1(79) EX1(80) EX1(81)                                             \
               "v_smfmac_f32_32x32x32_bf16 %[o0], %[v1], v[72:79], %[idx]\n\t"                                             \
               EX1(82) EX1(83) EX1(84)                                                                                     \
               "v_mfma_f32_32x32x16_bf16 v[64:79], %[kn], %[q0], %[c0]\n\t"                                                \
               EX1(85) EX1(86) EX1(87) EX1(88) EX1(89)                                                                     \
               "v_smfmac_f32_32x32x32_bf16 %[o1], %[v0], v[80:87], %[idx]\n\t"                                             \
               EX1(90) EX1(91) EX1(92) EX1(93) EX1(94) "v_exp_f32 v95, v95"                                                \
               : "+{v[64:79]}"(sA), "+{v[80:95]}"(sB), [o0] "+v"(o0), [o1] "+v"(o1), [kc] "+v"(kc_), [nv0] "=&v"(nv0_),    \
                 [pv1] "+v"(pv1_)                                                                                          \
               : [kn] "v"(kn_), [v0] "v"(v0_), [v1] "v"(v1_), [q0] "v"(q0), [q1] "v"(q1), [c0] "v"(cin0), [c1] "v"(cin1),  \
                 [idx] "v"(sidx), [kp] "v"(kpa), [vp] "v"(vpa), [ko] "n"(KOFF), [vo] "n"(VOFF), [vo1] "n"((VOFF) + 512)     \
               : "memory")

template <int half, int l>
DI void phase_attn(Ctx& c, const In& in, u32x4 (&wo)[16], u32x4 (&hw)[28]) {
  const int qt = c.qb * 8 + (c.w & 3) * 2;
  const unsigned lay = WS_L + l * L_SZ;
  const char* kvg = c.ws + lay + ((c.w & 1) ? L_V : L_K) + c.head * 65536 + ((c.w >> 1) & 1) * 32768 + half * 4096;
  const unsigned kvd = c.lbase + ((c.w & 1) ? LD_V : LD_W) + ((c.w >> 1) & 1) * 32768 + half * 4096;
  u32x4 qv0, qv1;
  asm volatile("s_nop 4\n\tglobal_load_dwordx4 %0, %2, %3 sc1\n\tglobal_load_dwordx4 %1, %2, %3 offset:1024 sc1"
               : "=&v"(qv0), "=&v"(qv1) : "v"(c.voff), "s"(c.ws + lay + L_Q + (c.head * 64 + qt) * 1024) : "memory");
#define KV4(u) glds16x4(kvg + (u) * 8192, c.voff, kvd + (u) * 8192)
  glds16x4(kvg, c.voff, kvd);
  if (half == 0) {
    const unsigned o1 = (c.lane & 11) == 0 ? 0x3f803f80u : 0u;
    ((LAS u32x4*)(c.lds + LD_ONE))[c.w * 64 + c.lane] = (u32x4){o1, o1, o1, o1};
  }
  int sidx = 0xDDDDDDDD;
  const bool isv = c.r < 16;
  const LAS char* kp0 = c.lds + LD_W + half * 32768 + c.lane * 16;
  const LAS char* vp0 = c.lds + (isv ? LD_V + half * 32768 : LD_ONE) + c.h * 256 + (c.r & 15) * 16;
  const int vstep = isv ? 4096 : 0;
  if (half == 0) asm volatile("s_waitcnt vmcnt(0) lgkmcnt(0)\n\ts_barrier" : "+v"(qv0), "+v"(qv1)::"memory");
  else asm volatile("s_waitcnt vmcnt(4)\n\ts_barrier" : "+v"(qv0), "+v"(qv1)::"memory");
  if (half == 0) __builtin_amdgcn_s_setprio(1);
  const bf16x8 q0 = as_bf(qv0), q1 = as_bf(qv1);
  f32x16 zero;
#pragma unroll
  for (int i = 0; i < 16; ++i) zero[i] = 0.f;
  f32x16 cin0, cin1, o0 = zero, o1 = zero, sA, sB = zero;
  float m0, m1;
  bf16x8 ka = LDF(kp0), kb = LDF(kp0 + 1024), va0 = LDF(vp0), va1 = LDF(vp0 + 512), vb0, vb1 = as_bf((u32x4){0u, 0u, 0u, 0u});
  {
    const f32x16 s = MFMA(ka, q0, zero), s1 = MFMA(ka, q1, zero);
    m0 = max16(s); m1 = max16(s1);
#pragma unroll
    for (int i = 0; i < 16; ++i) { cin0[i] = -m0; cin1[i] = -m1; sA[i] = s[i] - m0; }
  }
  unsigned kpa = (unsigned)(size_t)kp0, vpa = (unsigned)(size_t)vp0;
  asm volatile("s_nop 1" : "+v"(cin0), "+v"(cin1), "+v"(sA), "+v"(sB), "+v"(o0), "+v"(o1), "+v"(sidx), "+v"(vb1));
#define QUAD(A, B, MID, C, D)                                                        \
  do {                                                                               \
    ASTEP(2048, 1024, ka, kb, va0, va1, vb0, vb1); A;                                \
    ASTEP(3072, 2048, kb, ka, vb0, vb1, va0, va1); B; MID;                           \
    ASTEP(4096, 3072, ka, kb, va0, va1, vb0, vb1); C;                                \
    vpa += vstep;                                                                    \
    ASTEP(5120, 0, kb, ka, vb0, vb1, va0, va1); D;                                   \
    kpa += 4096;                                                                      \
  } while (0)
#define NOP (void)0
#define WBAR(n) asm volatile("s_waitcnt vmcnt(" #n ")\n\ts_barrier" ::: "memory")
  constexpr bool JOBS = half == 0 && l == 0;
  Jobs jb;
  KV4(1);
  QUAD(KV4(2), NOP, WBAR(8), KV4(3), if (JOBS) { jobs_load(c, in, jb); SBAR(); });
  QUAD(NOP, NOP, if (JOBS) WBAR(32); else WBAR(0); if (half == 1 && l == 1) (w2_fetch<0, 21>(c, hw)), NOP,
       if (JOBS) { jobs_put(c, jb); SBAR(); });
  QUAD(if (JOBS) jobs_publish(c), NOP, NOP,
       if (half == 1 && l == 0) { poll_any(c, FL_W2 + c.nb8, c.lane & 31); (w2_fetch<0, 21>(c, hw)); }, NOP);
  for (int it = 3; it < 8; ++it) QUAD(NOP, NOP, NOP, NOP, NOP);
#undef WBAR
  asm volatile("s_nop 1\n\tv_smfmac_f32_32x32x32_bf16 %[o1], %[pv1], v[88:95], %[idx]\n\ts_waitcnt lgkmcnt(0)\n\ts_nop 7\n\ts_nop 7"
               : [o1] "+v"(o1), "+v"(o0), "+v"(ka), "+v"(kb), "+v"(va0), "+v"(va1) : [pv1] "v"(vb1), "{v[80:95]}"(sB), [idx] "v"(sidx));
#undef QUAD
#undef KV4
  if (__any(!(o0[8] < 1.0995116e12f))) attn_slow(kp0, vp0, isv, q0, o0, m0);
  if (__any(!(o1[8] < 1.0995116e12f))) attn_slow(kp0, vp0, isv, q1, o1, m1);
  LAS f32x4* cmb = (LAS f32x4*)(c.lds + LD_CMB) + (c.w & 3) * 320 + c.lane;
  const unsigned w1src = WS_W1 + (c.nb < 11 ? 3 * c.nb : 33 + 2 * (c.nb - 11)) * 32768;
  if (half == 0) {
    __builtin_amdgcn_s_setprio(0);
    cmb[0] = (f32x4){o0[0], o0[1], o0[2], o0[3]}; cmb[64] = (f32x4){o0[4], o0[5], o0[6], o0[7]};
    cmb[128] = (f32x4){o1[0], o1[1], o1[2], o1[3]}; cmb[192] = (f32x4){o1[4], o1[5], o1[6], o1[7]};
    cmb[256] = (f32x4){o0[8], m0, o1[8], m1};
    arrive4(c, 12 + l);
    if (l == 0 && c.w == 3) code_rewarm(c);
    if (l == 0) poll_any(c, c.lane < 32 ? FL_W1 + c.nb : FL_W2 + c.nb8, c.lane < 32 ? (c.lane & 15) : (c.lane & 31));
    const unsigned wos = WS_WO + ((2 * c.nb8 + (c.w & 1)) * 32 + 8 * c.w) * 1024;
    const unsigned woo = WS_WO + ((2 * c.nb8 + 1 - (c.w & 1)) * 32 + 8 * c.w) * 1024;
#pragma unroll
    for (int k = 0; k < 8; ++k) { wo[2 * k] = ld16(c, c.voff, wos + k * 1024); wo[2 * k + 1] = ld16(c, c.voff, woo + k * 1024); }
    SBAR();
    wait4<true>(c, 12 + l);
    help_dma<8>(c, w1src, LD_W);
    if (c.nb < 11) help_dma<8>(c, w1src + 65536, LD_W + 65536);
  }
  BARRIER_LDS();
  if (half == 1) {
    const f32x4 lm = cmb[256];
#pragma unroll
    for (int u = 0; u < 2; ++u) {
      const f32x16& o = u ? o1 : o0; const float m = u ? m1 : m0;
      const f32x8 p2 = cat8(cmb[128 * u], cmb[128 * u + 64]);
      const float m2 = lm[2 * u + 1];
      const float mm = fmaxf(m, m2);
      const float f1 = __builtin_amdgcn_exp2f(m - mm), f2 = __builtin_amdgcn_exp2f(m2 - mm);
      const float inv = __builtin_amdgcn_rcpf(o[8] * f1 + lm[2 * u] * f2);
      const float g1 = f1 * inv, g2 = f2 * inv;
      unsigned p[4];
#pragma unroll
      for (int i = 0; i < 4; ++i) p[i] = pk2(o[2 * i] * g1 + p2[2 * i] * g2, o[2 * i + 1] * g1 + p2[2 * i + 1] * g2);
      st16(c, (u32x4){p[0], p[1], p[2], p[3]}, c.voff, lay + L_O + ((qt + u) * 32 + c.head) * 1024);
    }
    asm volatile("s_waitcnt vmcnt(0)" ::: "memory");
    if (c.lane < 8) __hip_atomic_store(flag_ptr(c, FL_O + l * 256 + (c.qb * 4 + (c.w & 3)) * 8 + c.lane, c.head), c.token, RLX_AGENT);
  }
}

DI u64 uniform64(u64 v) {
  return ((u64)__builtin_amdgcn_readfirstlane((unsigned)(v >> 32)) << 32) | (unsigned)__builtin_amdgcn_readfirstlane((unsigned)v);
}

DI void run_main(Ctx& c, const In& in, u64 pc) {
  const u64 epoch = __hip_atomic_load((gu64*)c.ws, RLX_AGENT);
  u32x4 res[2];
  {
    if (c.w == 0 && c.lane < 32) ((LAS unsigned*)(c.lds + LD_CTR))[c.lane] = 0u;
    const unsigned short* src = in.x + (size_t)((2 * c.tb64 + (c.w >> 1)) * 32 + c.r) * D_ + 32 * (2 * c.nb8 + (c.w & 1)) + 4 * c.h;
    u32x2 xr[4];
#pragma unroll
    for (int i = 0; i < 4; ++i) xr[i] = *(const u32x2*)(src + 8 * i);
    SBAR();
    if (c.w == 2) {
      if (c.lane == 0) *(LAS u64*)(c.lds + LD_CTR + 128) = pc;
      code_warm(c, pc);
    }
    if (c.w == 3) {
      const int i = c.lane & 15;
      const int line = i < 4 ? FL_X + i * 256 + c.tb * 16 + c.nb : i < 6 ? FL_H + (i & 1) * 256 + c.tb * 16 + c.nb
                     : i < 8 ? FL_Q + (i & 1) * 256 + c.head * 8 + c.qb : i < 12 ? FL_O + (i & 1) * 256 + c.tb64 * 8 + c.nb8
                     : i == 12 ? FL_WQ + c.nb * 16 + c.tb : i == 13 ? FL_W1 + c.nb : FL_W2 + c.nb8;
      unsigned sv;
      asm volatile("s_mov_b32 %0, m0\n\ts_mov_b32 m0, %3\n\ts_nop 2\n\tglobal_load_lds_dword %1, %2 sc1\n\ts_mov_b32 m0, %0"
                   : "=&s"(sv) : "v"(line * 256 + ((i == 10 || i == 11 || i == 15) ? 128 : 0)), "s"(c.ws), "s"(c.lbase + LD_SC) : "memory");
    }
    BARRIER_LDS();
    c.token = uniform64(epoch) + 1;
    res[0] = (u32x4){xr[0][0], xr[0][1], xr[1][0], xr[1][1]}; res[1] = (u32x4){xr[2][0], xr[2][1], xr[3][0], xr[3][1]};
    store_x(c, 0, res, unpack16(res));
    publish(c, 0, 4, FL_X + c.tb * 16, c.nb);
    poll<20>(c, (c.lane & 16) ? FL_WQ + c.nb * 16 + c.tb : FL_X + c.tb * 16 + c.nb, c.lane & 15);
    BARRIER_LDS();
    u32x4 dummy[16];
    fresh(c);
    phase_ln<0, true>(c, 0, 0, dummy);
  }
  for (int l = 0; l < 2; ++l) {
    u32x4 wo[16], w2r[16];
    if (l == 1) {
      fresh(c);
      u32x4 dummy[16];
      phase_ln<0, false>(c, l, 2 * l, dummy);
    }
    fresh(c);
    poll<10>(c, FL_Q + l * 256 + c.head * 8 + c.qb, c.lane & 15);
    BARRIER_LDS();
    { u32x4 unused[28]; if (l == 0) phase_attn<0, 0>(c, in, wo, unused); else phase_attn<0, 1>(c, in, wo, unused); }
    fresh(c);
    poll<28>(c, FL_O + l * 256 + c.tb64 * 8 + c.nb8, c.lane & 31);
    BARRIER_LDS();
    store_x(c, 2 * l + 1, res, phase_wo(c, l, wo, res));
    publish(c, 8 + l, 4, FL_X + (2 * l + 1) * 256 + c.tb * 16, c.nb);
    fresh(c);
    poll<12>(c, FL_X + (2 * l + 1) * 256 + c.tb * 16 + c.nb, c.lane & 15);
    BARRIER_ALL();
    phase_ln<1, false>(c, l, 2 * l + 1, w2r);
    fresh(c);
    poll<22>(c, FL_H + l * 256 + c.tb * 16 + c.nb, c.lane & 15);
    BARRIER_LDS();
    const f32x16 x2 = phase_w2(c, l, w2r, res);
    fresh(c);
    if (l == 0) {
      store_x(c, 2, res, x2);
      publish(c, 10, 4, FL_X + 2 * 256 + c.tb * 16, c.nb);
      poll<12>(c, FL_X + 2 * 256 + c.tb * 16 + c.nb, c.lane & 15);
      BARRIER_LDS();
    }
  }
  {
    auto s0 = __builtin_amdgcn_permlane32_swap(res[0][0], res[0][2], false, false), s1 = __builtin_amdgcn_permlane32_swap(res[0][1], res[0][3], false, false);
    auto s2 = __builtin_amdgcn_permlane32_swap(res[1][0], res[1][2], false, false), s3 = __builtin_amdgcn_permlane32_swap(res[1][1], res[1][3], false, false);
    unsigned short* op = in.out + (size_t)((2 * c.tb64 + (c.w >> 1)) * 32 + c.r) * D_ + 32 * (2 * c.nb8 + (c.w & 1)) + 8 * c.h;
    __builtin_nontemporal_store((u32x4){s0[0], s1[0], s0[1], s1[1]}, (u32x4*)op);
    __builtin_nontemporal_store((u32x4){s2[0], s3[0], s2[1], s3[1]}, (u32x4*)(op + 16));
  }
  if (c.bid == 0 && c.w == 0 && c.lane == 0) __hip_atomic_store((gu64*)c.ws, c.token, RLX_AGENT);
}

DI void run_help(Ctx& c, const In& in) {
  const u64 epoch = __hip_atomic_load((gu64*)c.ws, RLX_AGENT);
  {
    unsigned e0[8], e1[8];
    const int qk = 2 * c.tb + (c.w & 1), qa = (c.w >> 1) & 1;
    wfrag_load(in.wqkv, 3 * D_, qa * 16 + c.nb, qk, c.r, c.h, e0);
    wfrag_load(in.wqkv, 3 * D_, 32 + c.nb, qk, c.r, c.h, e1);
    BARRIER_LDS();
    c.token = uniform64(epoch) + 1;
    wfrag_store(c, e0, WS_WQKV + ((qa * 16 + c.nb) * 32 + qk) * 1024, true, WS_CSP + (c.nb * 32 + qk) * 384 + qa * 128);
    if (qa == 0) wfrag_store(c, e1, WS_WQKV + ((32 + c.nb) * 32 + qk) * 1024, true, WS_CSP + (c.nb * 32 + qk) * 384 + 256);
    publish(c, 1, 4, FL_WQ + c.nb * 16, c.tb);
    BARRIER_LDS();
    fresh(c);
#define WQC(q) _Pragma("unroll") for (int t_ = 0; t_ < 2; ++t_) \
    glds16x2(c.ws + WS_WQKV + ((16 * t_ + c.nb) * 32 + 8 * (q) + 2 * (c.w & 3)) * 1024, c.voff, c.lbase + LD_W + t_ * 32768 + (8 * (q) + 2 * (c.w & 3)) * 1024)
    WQC(0); WQC(1);
    asm volatile("s_waitcnt vmcnt(4)\n\ts_barrier" ::: "memory");
    WQC(2); WQC(3);
    asm volatile("s_waitcnt vmcnt(8)\n\ts_barrier" ::: "memory");
    help_dma<8>(c, WS_WQKV + (32 + c.nb) * 32768, LD_W + 65536);
    asm volatile("s_waitcnt vmcnt(12)\n\ts_barrier" ::: "memory");
    if (c.w < 6) {
      float cv[32];
      colsum_load(c, 0, cv);
      asm volatile("s_waitcnt vmcnt(40)\n\ts_barrier" ::: "memory");
      colsum_put(c, 0, cv, 0u);
    } else {
      asm volatile("s_waitcnt vmcnt(8)\n\ts_barrier" ::: "memory");
    }
    BARRIER_ALL();
#undef WQC
  }
  for (int l = 0; l < 2; ++l) {
    u32x4 wo[16];
    fresh(c);
    BARRIER_LDS();
    fresh(c);
    u32x4 hw[28];
    if (l == 0) phase_attn<1, 0>(c, in, wo, hw); else phase_attn<1, 1>(c, in, wo, hw);
    fresh(c);
    BARRIER_LDS();
    asm volatile("s_barrier" ::: "memory");
    help_dma<8>(c, WS_W1 + (c.nb < 11 ? 3 * c.nb + 1 : 34 + 2 * (c.nb - 11)) * 32768, LD_W + 32768);
    w2_fetch<21, 28>(c, hw);
    asm volatile("s_barrier" ::: "memory");
    if (l == 0 && c.w < 6) {
      const int bcol = 32 * (c.nb < 11 ? 3 * c.nb : 33 + 2 * (c.nb - 11)) + (c.w - 4) * 64 + c.lane;
      const unsigned bv = in.b1[bcol < F_ ? bcol : 0];
      float cv[32];
      colsum_load(c, 1, cv);
      colsum_put(c, 1, cv, bv);
    }
    BARRIER_ALL();
    w2_put<21, 28>(c, hw);
    help_ln(c, l, [&]() __attribute__((always_inline)) { wait4<true>(c, 14 + l); w2_put<0, 12>(c, hw); });
    w2_put<12, 21>(c, hw);
    BARRIER_LDS();
    asm volatile("s_barrier" ::: "memory");
    if (l == 0) {
      fresh(c);
#pragma unroll
      for (int u = 0; u < 24; ++u) hw[u] = ld16(c, c.voff, WS_WQKV + (((u >> 3) * 16 + c.nb) * 32 + (u & 7) * 4 + (c.w & 3)) * 1024);
      SBAR();
      wait4<true>(c, 16);
#pragma unroll
      for (int u = 0; u < 24; ++u) *(LAS u32x4*)(c.lds + LD_W + (u * 4 + (c.w & 3)) * 1024 + c.voff) = hw[u];
      BARRIER_LDS();
    } else {
      asm volatile("s_barrier\n\ts_barrier" ::: "memory");
    }
  }
}

__global__ __launch_bounds__(512, 2) void mega(const unsigned short* __restrict__ x, const unsigned short* __restrict__ wqkv,
                                               const unsigned short* __restrict__ wo_g, const unsigned short* __restrict__ w1,
                                               const unsigned short* __restrict__ b1, const unsigned short* __restrict__ w2,
                                               char* __restrict__ ws, unsigned short* __restrict__ out) {
  extern __shared__ __attribute__((aligned(16))) char lds_[];
  Ctx c;
  c.ws = ws; c.lds = (LAS char*)lds_; c.rs = __builtin_amdgcn_make_buffer_rsrc(ws, 0, WS_END, 0x00020000);
  c.lbase = (unsigned)(size_t)c.lds;
  c.bid = blockIdx.x; c.lane = threadIdx.x & 63; c.w = __builtin_amdgcn_readfirstlane(threadIdx.x >> 6); c.dead = false;
  fresh(c);
  const In in = {x, wqkv, wo_g, w1, b1, w2, out};
  u64 pc;
  asm volatile("s_getpc_b64 %0" : "=s"(pc));
  if (c.w < 4) run_main(c, in, pc); else run_help(c, in);
}

extern "C" void kernel_launch(void* const* d_in, const int* in_sizes, int n_in, void* d_out, int out_size, void* d_ws,
                              size_t ws_size, hipStream_t stream) {
  (void)hipFuncSetAttribute((const void*)mega, hipFuncAttributeMaxDynamicSharedMemorySize, LDS_BYTES);
  mega<<<256, 512, LDS_BYTES, stream>>>((const unsigned short*)d_in[0], (const unsigned short*)d_in[1], (const unsigned short*)d_in[2],
                                        (const unsigned short*)d_in[4], (const unsigned short*)d_in[5], (const unsigned short*)d_in[6],
                                        (char*)d_ws, (unsigned short*)d_out);
}
```
